```python
import torch

def _softmax(x, axis=-1):
    return torch.softmax(x, dim=axis)
def _gelu(x):
    return torch.nn.functional.gelu(x, approximate="tanh")
def _silu(x):
    return torch.nn.functional.silu(x)
def _sigmoid(x):
    return torch.sigmoid(x)
def _layernorm(x, axis=-1, eps=1e-5):
    m = x.mean(dim=axis, keepdim=True)
    v = x.var(dim=axis, keepdim=True, unbiased=False)
    return (x - m) / torch.sqrt(v + eps)
def _scatter_add(tgt, idx, src, axis=0):
    return tgt.clone().index_add_(axis, idx, src)
def _segment_sum(src, seg, n):
    out = torch.zeros((n,) + src.shape[1:], dtype=src.dtype)
    return out.index_add_(0, seg, src)
def _pad(x, pw):
    flat = tuple(v for pair in reversed(pw) for v in pair)
    return torch.nn.functional.pad(x, flat)

kernel_name = 'sequential_preLN__spectral_fnet__glu_gated_glu__prompt'

def setup_inputs():

    _w = (lambda a: (lambda t: t.to(torch.bfloat16) if t.is_floating_point() else t)(torch.as_tensor(a)))
    def _A_setup():
        B, S, D = 1, 2048, 512
        ns = torch.arange(S)[:, None] * torch.arange(S)[None, :]
        nd = torch.arange(D)[:, None] * torch.arange(D)[None, :]
        Ws_re = torch.cos(-2.0 * torch.pi * ns / S).to(float)
        Ws_im = torch.sin(-2.0 * torch.pi * ns / S).to(float)
        Wd_re = torch.cos(-2.0 * torch.pi * nd / D).to(float)
        Wd_im = torch.sin(-2.0 * torch.pi * nd / D).to(float)
        return {
            "x": _w((lambda *s: torch.randn(*s, dtype=torch.bfloat16) * 0.05)(B, S, D)),
            "dft_seq_re": _w(Ws_re),
            "dft_seq_im": _w(Ws_im),
            "dft_hid_re": _w(Wd_re),
            "dft_hid_im": _w(Wd_im),
        }
    _A_in = _A_setup()

    def _B_setup():
        B, S, D, F = 1, 2048, 512, 1792
        return {
            "x": _w((lambda *s: torch.randn(*s, dtype=torch.bfloat16) * 0.05)(B, S, D)),
            "w_gate": _w((lambda *s: torch.randn(*s, dtype=torch.bfloat16) * 1.6)(D, F)),
            "w_up": _w((lambda *s: torch.randn(*s, dtype=torch.bfloat16) * 0.8)(D, F)),
            "w_down": _w((lambda *s: torch.randn(*s, dtype=torch.bfloat16) * 0.8)(F, D)),
        }
    _B_in = _B_setup()

    return {
        "A_x": _A_in["x"],
        "A_dft_seq_re": _A_in["dft_seq_re"],
        "A_dft_seq_im": _A_in["dft_seq_im"],
        "A_dft_hid_re": _A_in["dft_hid_re"],
        "A_dft_hid_im": _A_in["dft_hid_im"],
        "B_x": _B_in["x"],
        "B_w_gate": _B_in["w_gate"],
        "B_w_up": _B_in["w_up"],
        "B_w_down": _B_in["w_down"],
    }

def reference(A_x, A_dft_seq_re, A_dft_seq_im, A_dft_hid_re, A_dft_hid_im, B_x, B_w_gate, B_w_up, B_w_down):
    def _A_ref(x, dft_seq_re, dft_seq_im, dft_hid_re, dft_hid_im):
        a = torch.einsum("bsd,ed->bse", x, dft_hid_re)
        b = torch.einsum("bsd,ed->bse", x, dft_hid_im)
        y = torch.einsum("ts,bse->bte", dft_seq_re, a) - torch.einsum("ts,bse->bte", dft_seq_im, b)
        return y

    def _B_ref(x, w_gate, w_up, w_down):
        g = torch.einsum("bsd,df->bsf", x, w_gate)
        u = torch.einsum("bsd,df->bsf", x, w_up)
        h = _sigmoid(g) * u
        return torch.einsum("bsf,fd->bsd", h, w_down)

    x = A_x
    h = x + _A_ref(_layernorm(x, axis=-1), A_dft_seq_re, A_dft_seq_im, A_dft_hid_re, A_dft_hid_im)
    h2 = h + _B_ref(_layernorm(h, axis=-1), B_w_gate, B_w_up, B_w_down)
    return h2
```

```cpp
#include <hip/hip_runtime.h>
#include <hip/hip_bf16.h>
#include <stdint.h>

typedef unsigned short u16;
using bf16x8 = __attribute__((ext_vector_type(8))) short;
using f32x4 = __attribute__((ext_vector_type(4))) float;

__device__ __forceinline__ float bf2f(u16 b) { return __uint_as_float(((unsigned)b) << 16); }
__device__ __forceinline__ u16 f2bf(float f) {
  __hip_bfloat16 h = __float2bfloat16(f);
  return *reinterpret_cast<u16*>(&h);
}

constexpr int S = 2048, D = 512, F = 1792;

__global__ __launch_bounds__(256) void k_ln(const u16* __restrict__ x, u16* __restrict__ y) {
  int row = blockIdx.x * 4 + (threadIdx.x >> 6), lane = threadIdx.x & 63;
  bf16x8 v = *reinterpret_cast<const bf16x8*>(x + (size_t)row * D + lane * 8);
  float f[8], s = 0.f;
  for (int j = 0; j < 8; ++j) { f[j] = bf2f((u16)v[j]); s += f[j]; }
  for (int o = 32; o > 0; o >>= 1) s += __shfl_xor(s, o);
  float m = s * (1.f / D);
  float q = 0.f;
  for (int j = 0; j < 8; ++j) { float d = f[j] - m; q += d * d; }
  for (int o = 32; o > 0; o >>= 1) q += __shfl_xor(q, o);
  float r = rsqrtf(q * (1.f / D) + 1e-5f);
  bf16x8 o8;
  for (int j = 0; j < 8; ++j) o8[j] = (short)f2bf((f[j] - m) * r);
  *reinterpret_cast<bf16x8*>(y + (size_t)row * D + lane * 8) = o8;
}

__global__ void k_tr(const u16* __restrict__ in, u16* __restrict__ out, int R, int C) {
  __shared__ u16 t[32][33];
  int c0 = blockIdx.x * 32, r0 = blockIdx.y * 32;
  for (int i = threadIdx.y; i < 32; i += 8) t[i][threadIdx.x] = in[(size_t)(r0 + i) * C + c0 + threadIdx.x];
  __syncthreads();
  for (int i = threadIdx.y; i < 32; i += 8) out[(size_t)(c0 + i) * R + r0 + threadIdx.x] = t[threadIdx.x][i];
}

template <int MODE>
__global__ __launch_bounds__(256) void k_gemm(const u16* __restrict__ A, const u16* __restrict__ B,
                                              const u16* __restrict__ A2, const u16* __restrict__ B2,
                                              const u16* __restrict__ res, u16* __restrict__ out,
                                              int K, int lda, int ldb, int ldo) {
  __shared__ __attribute__((aligned(16))) u16 sA[64][40], sB[64][40];
  int tid = threadIdx.x, lane = tid & 63, w = tid >> 6, wr = w >> 1, wc = w & 1;
  int m0 = blockIdx.y * 64, n0 = blockIdx.x * 64;
  f32x4 acc[2][2][2] = {};
  constexpr int NP = (MODE == 1 || MODE == 2) ? 2 : 1;
  for (int p = 0; p < NP; ++p) {
    const u16* a = (p == 1 && MODE == 1) ? A2 : A;
    const u16* b = (p == 1) ? B2 : B;
    for (int k0 = 0; k0 < K; k0 += 32) {
      int r = tid >> 2, c = (tid & 3) * 8;
      *reinterpret_cast<bf16x8*>(&sA[r][c]) = *reinterpret_cast<const bf16x8*>(a + (size_t)(m0 + r) * lda + k0 + c);
      *reinterpret_cast<bf16x8*>(&sB[r][c]) = *reinterpret_cast<const bf16x8*>(b + (size_t)(n0 + r) * ldb + k0 + c);
      __syncthreads();
      bf16x8 fa[2], fb[2];
      for (int i = 0; i < 2; ++i) {
        fa[i] = *reinterpret_cast<const bf16x8*>(&sA[wr * 32 + i * 16 + (lane & 15)][(lane >> 4) * 8]);
        fb[i] = *reinterpret_cast<const bf16x8*>(&sB[wc * 32 + i * 16 + (lane & 15)][(lane >> 4) * 8]);
      }
      if (p == 0) {
        for (int i = 0; i < 2; ++i) for (int j = 0; j < 2; ++j)
          acc[0][i][j] = __builtin_amdgcn_mfma_f32_16x16x32_bf16(fa[i], fb[j], acc[0][i][j], 0, 0, 0);
      } else {
        for (int i = 0; i < 2; ++i) for (int j = 0; j < 2; ++j)
          acc[1][i][j] = __builtin_amdgcn_mfma_f32_16x16x32_bf16(fa[i], fb[j], acc[1][i][j], 0, 0, 0);
      }
      __syncthreads();
    }
  }
  for (int i = 0; i < 2; ++i) for (int j = 0; j < 2; ++j) for (int r = 0; r < 4; ++r) {
    int m = m0 + wr * 32 + i * 16 + (lane >> 4) * 4 + r, n = n0 + wc * 32 + j * 16 + (lane & 15);
    float v = acc[0][i][j][r];
    if (MODE == 0) out[(size_t)n * ldo + m] = f2bf(v);
    if (MODE == 1) {
      float y = bf2f(f2bf(bf2f(f2bf(v)) - bf2f(f2bf(acc[1][i][j][r]))));
      out[(size_t)m * ldo + n] = f2bf(bf2f(res[(size_t)m * ldo + n]) + y);
    }
    if (MODE == 2) {
      float g = bf2f(f2bf(v)), u = bf2f(f2bf(acc[1][i][j][r]));
      float sg = bf2f(f2bf(1.f / (1.f + __expf(-g))));
      out[(size_t)m * ldo + n] = f2bf(sg * u);
    }
    if (MODE == 3) out[(size_t)m * ldo + n] = f2bf(bf2f(res[(size_t)m * ldo + n]) + bf2f(f2bf(v)));
  }
}

extern "C" void kernel_launch(void* const* d_in, const int* in_sizes, int n_in, void* d_out, int out_size,
                              void* d_ws, size_t ws_size, hipStream_t stream) {
  const u16* x = (const u16*)d_in[0];
  const u16* Cr = (const u16*)d_in[1];
  const u16* Ci = (const u16*)d_in[2];
  const u16* Dr = (const u16*)d_in[3];
  const u16* Di = (const u16*)d_in[4];
  const u16* Wg = (const u16*)d_in[6];
  const u16* Wu = (const u16*)d_in[7];
  const u16* Wd = (const u16*)d_in[8];
  u16* out = (u16*)d_out;
  u16* ws = (u16*)d_ws;
  u16* xn = ws;
  u16* aT = xn + S * D;
  u16* bT = aT + S * D;
  u16* h = bT + S * D;
  u16* hn = h + S * D;
  u16* hh = hn + S * D;
  u16* WgT = hh + S * F;
  u16* WuT = WgT + F * D;
  u16* WdT = WuT + F * D;
  k_tr<<<dim3(F / 32, D / 32), dim3(32, 8), 0, stream>>>(Wg, WgT, D, F);
  k_tr<<<dim3(F / 32, D / 32), dim3(32, 8), 0, stream>>>(Wu, WuT, D, F);
  k_tr<<<dim3(D / 32, F / 32), dim3(32, 8), 0, stream>>>(Wd, WdT, F, D);
  k_ln<<<S / 4, 256, 0, stream>>>(x, xn);
  k_gemm<0><<<dim3(D / 64, S / 64), 256, 0, stream>>>(xn, Dr, nullptr, nullptr, nullptr, aT, D, D, D, S);
  k_gemm<0><<<dim3(D / 64, S / 64), 256, 0, stream>>>(xn, Di, nullptr, nullptr, nullptr, bT, D, D, D, S);
  k_gemm<1><<<dim3(D / 64, S / 64), 256, 0, stream>>>(Cr, aT, Ci, bT, x, h, S, S, S, D);
  k_ln<<<S / 4, 256, 0, stream>>>(h, hn);
  k_gemm<2><<<dim3(F / 64, S / 64), 256, 0, stream>>>(hn, WgT, nullptr, WuT, nullptr, hh, D, D, D, F);
  k_gemm<3><<<dim3(D / 64, S / 64), 256, 0, stream>>>(hh, WdT, nullptr, nullptr, h, out, F, F, F, D);
}
```

```cpp
#include <hip/hip_runtime.h>
#include <stdint.h>

typedef unsigned short u16;
typedef __attribute__((ext_vector_type(8))) short bf16x8;
typedef __attribute__((ext_vector_type(4))) unsigned u32x4;
typedef __attribute__((ext_vector_type(2))) unsigned u32x2;
typedef __attribute__((ext_vector_type(4))) float f32x4;
typedef __attribute__((ext_vector_type(2))) float f32x2;
typedef __attribute__((ext_vector_type(16))) float f32x16;
typedef __attribute__((ext_vector_type(2))) __bf16 bf16x2_t;
#define LAS __attribute__((address_space(3)))
#define DI __device__ __forceinline__

constexpr int S = 2048, D = 512, F = 1792;
template <int V> struct IC { static constexpr int value = V; };
constexpr size_t OFF_AB = 0;
constexpr size_t OFF_H = OFF_AB + (4u << 20);
constexpr size_t OFF_ST = OFF_H + (2u << 20);
constexpr size_t OFF_HH = OFF_ST + (256u << 10);
constexpr size_t OFF_WF = OFF_HH + (7u << 20);
constexpr size_t OFF_WD = OFF_WF + 3584u * 1024;
constexpr size_t OFF_CS = OFF_WD + 1792u * 1024;
constexpr size_t OFF_STAMP = 32u << 20;

#define STAMP_DECL do {} while (0)
#define STAMP_AT(ws, k, i) do {} while (0)

DI f32x16 mfma32(u32x4 a, u32x4 b, f32x16 c) {
  return __builtin_amdgcn_mfma_f32_32x32x16_bf16(__builtin_bit_cast(bf16x8, a), __builtin_bit_cast(bf16x8, b), c, 0, 0, 0);
}
DI unsigned pk2(float lo, float hi) {
  bf16x2_t v = {(__bf16)lo, (__bf16)hi};
  return __builtin_bit_cast(unsigned, v);
}
DI float bflo(unsigned u) { return __uint_as_float(u << 16); }
DI float bfhi(unsigned u) { return __uint_as_float(u & 0xffff0000u); }
DI float dot2(unsigned a, unsigned b, float c) {
  return __builtin_amdgcn_fdot2_f32_bf16(__builtin_bit_cast(bf16x2_t, a), __builtin_bit_cast(bf16x2_t, b), c, false);
}
DI float xhalf(float v) { return __shfl_xor(v, 32); }
#define SB() __builtin_amdgcn_sched_barrier(0)
#define WAITV(n) asm volatile("s_waitcnt vmcnt(" #n ")" ::: "memory")
#define BAR() do { asm volatile("s_waitcnt lgkmcnt(0)" ::: "memory"); __builtin_amdgcn_s_barrier(); asm volatile("" ::: "memory"); } while (0)

#define DMA_FIRST "s_waitcnt lgkmcnt(0)\n\ts_mov_b32 %0, m0\n\ts_mov_b32 m0, %1\n\ts_nop 0\n\tglobal_load_lds_dwordx4 %3, %2\n\t"
#define DMA_NEXT(i) "s_add_u32 m0, m0, 0x400\n\ts_nop 0\n\tglobal_load_lds_dwordx4 %" #i ", %2\n\t"
#define DMA_END "s_mov_b32 m0, %0"
DI void dma2(unsigned dst, const void* sb, unsigned v0, unsigned v1) {
  unsigned keep;
  asm volatile(DMA_FIRST DMA_NEXT(4) DMA_END : "=&s"(keep) : "s"(dst), "s"(sb), "v"(v0), "v"(v1) : "memory", "scc");
}
DI void dma4(unsigned dst, const void* sb, unsigned v0, unsigned v1, unsigned v2, unsigned v3) {
  unsigned keep;
  asm volatile(DMA_FIRST DMA_NEXT(4) DMA_NEXT(5) DMA_NEXT(6) DMA_END : "=&s"(keep) : "s"(dst), "s"(sb), "v"(v0), "v"(v1), "v"(v2), "v"(v3) : "memory", "scc");
}
DI void dma8(unsigned dst, const void* sb, const unsigned (&v)[8]) {
  unsigned keep;
  asm volatile(DMA_FIRST DMA_NEXT(4) DMA_NEXT(5) DMA_NEXT(6) DMA_NEXT(7) DMA_NEXT(8) DMA_NEXT(9) DMA_NEXT(10) DMA_END
               : "=&s"(keep) : "s"(dst), "s"(sb), "v"(v[0]), "v"(v[1]), "v"(v[2]), "v"(v[3]), "v"(v[4]), "v"(v[5]), "v"(v[6]), "v"(v[7]) : "memory", "scc");
}
DI void dma_lin4(unsigned dst, const void* sb, unsigned vl) {
  unsigned keep;
  asm volatile("s_mov_b32 %0, m0\n\ts_mov_b32 m0, %1\n\ts_nop 0\n\t"
               "global_load_lds_dwordx4 %3, %2\n\tglobal_load_lds_dwordx4 %3, %2 offset:1024\n\t"
               "global_load_lds_dwordx4 %3, %2 offset:2048\n\tglobal_load_lds_dwordx4 %3, %2 offset:3072\n\t" DMA_END
               : "=&s"(keep) : "s"(dst), "s"(sb), "v"(vl) : "memory");
}
DI void dma_lin3(unsigned dst, const void* sb, unsigned vl) {
  unsigned keep;
  asm volatile("s_mov_b32 %0, m0\n\ts_mov_b32 m0, %1\n\ts_nop 0\n\t"
               "global_load_lds_dwordx4 %3, %2\n\tglobal_load_lds_dwordx4 %3, %2 offset:1024\n\t"
               "global_load_lds_dwordx4 %3, %2 offset:2048\n\t" DMA_END
               : "=&s"(keep) : "s"(dst), "s"(sb), "v"(vl) : "memory");
}
DI void ld4(u32x4& a, u32x4& b, u32x4& c, u32x4& d, const void* sb, unsigned vl) {
  asm volatile("global_load_dwordx4 %0, %4, %5\n\tglobal_load_dwordx4 %1, %4, %5 offset:1024\n\t"
               "global_load_dwordx4 %2, %4, %5 offset:2048\n\tglobal_load_dwordx4 %3, %4, %5 offset:3072"
               : "=&v"(a), "=&v"(b), "=&v"(c), "=&v"(d) : "v"(vl), "s"(sb) : "memory");
}
#define WAIT4(n, a, b, c, d) asm volatile("s_waitcnt vmcnt(" #n ")" : "+v"(a), "+v"(b), "+v"(c), "+v"(d) :: "memory")
#define WAIT8(n, x) asm volatile("s_waitcnt vmcnt(" #n ")" : "+v"(x[0]), "+v"(x[1]), "+v"(x[2]), "+v"(x[3]), "+v"(x[4]), "+v"(x[5]), "+v"(x[6]), "+v"(x[7]) :: "memory")

template <int OFF> DI void dma1(unsigned dst, const void* sb, unsigned v) {
  asm volatile("s_add_u32 m0, %0, %3\n\ts_nop 0\n\tglobal_load_lds_dwordx4 %2, %1" :: "s"(dst), "s"(sb), "v"(v), "i"(OFF) : "memory", "scc");
}
template <int OFF> DI void ld1(u32x4& a, const void* sb, unsigned vl) {
  asm volatile("global_load_dwordx4 %0, %1, %2 offset:%3" : "=&v"(a) : "v"(vl), "s"(sb), "i"(OFF) : "memory");
}
#define WAITL0() asm volatile("s_waitcnt lgkmcnt(0)" ::: "memory")
#define RAWBAR() do { asm volatile("" ::: "memory"); __builtin_amdgcn_s_barrier(); asm volatile("" ::: "memory"); } while (0)
DI u32x4 lds_rd(unsigned addr) { return *(const LAS u32x4*)(size_t)addr; }

__global__ __launch_bounds__(256, 1) void k1(const u16* __restrict__ x, const u16* __restrict__ Dr, const u16* __restrict__ Di, char* __restrict__ ws) {
  extern __shared__ __attribute__((aligned(16))) char smem[];
  const unsigned L0 = (unsigned)(size_t)smem;
  STAMP_DECL; STAMP_AT(ws, 0, 0);
  const int lane = threadIdx.x & 63, w = __builtin_amdgcn_readfirstlane(threadIdx.x >> 6);
  const int g = blockIdx.x & 7, j = blockIdx.x >> 3;
  const int is = (g >> 1) * 4 + (j & 3), ie = (g & 1) * 8 + (j >> 2);
  const int r = lane & 31, hh = lane >> 5;
  unsigned vo[4];
#pragma unroll
  for (int q = 0; q < 4; ++q) vo[q] = (8 * q + (lane >> 3)) * 1024 + (((lane & 7) ^ ((4 * q + (lane >> 4)) & 7)) * 16);
  const char* xb = (const char*)x + (size_t)(128 * is + 32 * w) * 1024;
  const char* db = (const char*)((ie >= 8) ? Di : Dr) + (size_t)(64 * (ie & 7) + 16 * w) * 1024;
  auto issue = [&](int c) {
    const unsigned slot = L0 + (c % 6) * 24576;
    dma4(slot + w * 4096, xb + c * 128, vo[0], vo[1], vo[2], vo[3]);
    dma2(slot + 16384 + w * 2048, db + c * 128, vo[0], vo[1]);
  };
#pragma unroll
  for (int c = 0; c < 6; ++c) issue(c);
  unsigned ra[4];
#pragma unroll
  for (int k = 0; k < 4; ++k) ra[k] = r * 128 + (((2 * k + hh) ^ ((r >> 1) & 7)) * 16);
  f32x16 acc[2] = {};
  float sx = 0.f, sxx = 0.f, cs0 = 0.f, cs1 = 0.f;
  const unsigned ones = 0x3f803f80u;
  auto compute = [&](int c) {
    const unsigned slot = L0 + (c % 6) * 24576;
#pragma unroll
    for (int k = 0; k < 4; ++k) {
      u32x4 a = lds_rd(slot + w * 4096 + ra[k]);
      u32x4 b0 = lds_rd(slot + 16384 + ra[k]);
      u32x4 b1 = lds_rd(slot + 16384 + 4096 + ra[k]);
      acc[0] = mfma32(a, b0, acc[0]);
      acc[1] = mfma32(a, b1, acc[1]);
#pragma unroll
      for (int q = 0; q < 4; ++q) {
        sx = dot2(a[q], ones, sx); sxx = dot2(a[q], a[q], sxx);
        cs0 = dot2(b0[q], ones, cs0); cs1 = dot2(b1[q], ones, cs1);
      }
    }
  };
  WAITV(30); BAR(); STAMP_AT(ws, 0, 3); compute(0);
  WAITV(24); BAR(); issue(6); compute(1);
  WAITV(24); BAR(); issue(7); compute(2);
  WAITV(24); BAR(); compute(3);
  WAITV(18); BAR(); compute(4);
  WAITV(12); BAR(); compute(5);
  WAITV(6); BAR(); compute(6);
  WAITV(0); BAR(); STAMP_AT(ws, 0, 2); compute(7);
  sx += xhalf(sx); sxx += xhalf(sxx); cs0 += xhalf(cs0); cs1 += xhalf(cs1);
  const float mean = sx * (1.f / D);
  const float var = fmaxf(sxx * (1.f / D) - mean * mean, 0.f);
  float rs = rsqrtf(var + 1e-5f);
  if (ie >= 8) rs = -rs;
  const unsigned sb = L0 + 2 * 24576 + w * 4096;
  if (hh == 0) *(LAS f32x2*)(size_t)(sb + r * 8) = f32x2{mean, rs};
  asm volatile("s_waitcnt lgkmcnt(0)" ::: "memory");
  char* outb = ws + OFF_AB + ((size_t)((ie >> 3) * 16 + 2 * (ie & 7)) * 128 + (8 * is + 2 * w)) * 1024 + lane * 16;
#pragma unroll
  for (int kk = 0; kk < 2; ++kk) {
    float m[8], sc[8];
#pragma unroll
    for (int q = 0; q < 2; ++q) {
      const int row = 16 * kk + 8 * q + 4 * hh;
      f32x4 t0 = *(const LAS f32x4*)(size_t)(sb + row * 8), t1 = *(const LAS f32x4*)(size_t)(sb + row * 8 + 16);
      m[4 * q + 0] = t0[0]; sc[4 * q + 0] = t0[1]; m[4 * q + 1] = t0[2]; sc[4 * q + 1] = t0[3];
      m[4 * q + 2] = t1[0]; sc[4 * q + 2] = t1[1]; m[4 * q + 3] = t1[2]; sc[4 * q + 3] = t1[3];
    }
#pragma unroll
    for (int n = 0; n < 2; ++n) {
      const float cs = n ? cs1 : cs0;
      float v[8];
#pragma unroll
      for (int q = 0; q < 8; ++q) v[q] = (acc[n][8 * kk + q] - m[q] * cs) * sc[q];
      unsigned p0 = pk2(v[0], v[1]), p1 = pk2(v[2], v[3]), p2 = pk2(v[4], v[5]), p3 = pk2(v[6], v[7]);
      auto s0 = __builtin_amdgcn_permlane32_swap(p0, p2, false, false);
      auto s1 = __builtin_amdgcn_permlane32_swap(p1, p3, false, false);
      u32x4 o = {s0[0], s1[0], s0[1], s1[1]};
      *(u32x4*)(outb + (size_t)n * 128 * 1024 + kk * 1024) = o;
    }
  }
  STAMP_AT(ws, 0, 1);
}

DI u32x2 tr_rd(unsigned addr) {
  u32x2 v;
  asm volatile("ds_read_b64_tr_b16 %0, %1\n\ts_waitcnt lgkmcnt(0)" : "=&v"(v) : "v"(addr) : "memory");
  return v;
}
DI void retile_issue(int u, unsigned scr, int lane, const u16* Wg, const u16* Wu, const u16* Wd) {
  if (u < 448) {
    const int tq = u % 28, k32 = u / 28;
    const unsigned v0 = (lane >> 3) * (F * 2) + (lane & 7) * 16;
    const size_t o = (size_t)(32 * k32) * (F * 2) + tq * 128;
    dma4(scr, (const char*)Wg + o, v0, v0 + 8 * F * 2, v0 + 16 * F * 2, v0 + 24 * F * 2);
    dma4(scr + 4096, (const char*)Wu + o, v0, v0 + 8 * F * 2, v0 + 16 * F * 2, v0 + 24 * F * 2);
  } else if (u < 672) {
    const int dq = (u - 448) & 7, k64 = (u - 448) >> 3;
    const unsigned v0 = (lane >> 3) * 1024 + (lane & 7) * 16;
    const size_t o = (size_t)(64 * k64) * 1024 + dq * 128;
    dma4(scr, (const char*)Wd + o, v0, v0 + 8192, v0 + 16384, v0 + 24576);
    dma4(scr + 4096, (const char*)Wd + o + 32768, v0, v0 + 8192, v0 + 16384, v0 + 24576);
  }
}
DI void retile_finish(int u, unsigned scr, int lane, char* ws) {
  const int hh = lane >> 5, g16 = (lane >> 4) & 1, q = (lane & 15) >> 2, p = lane & 3;
  if (u < 448) {
    const int tq = u % 28, k32 = u / 28;
    const unsigned ones = 0x3f803f80u;
#pragma unroll
    for (int t = 0; t < 4; ++t) {
      const int tile = 4 * tq + t;
      float cs = 0.f;
#pragma unroll
      for (int kk = 0; kk < 2; ++kk) {
        const unsigned a = scr + g16 * 4096 + (16 * kk + 4 * hh + q) * 128 + t * 32 + p * 8;
        u32x2 lo = tr_rd(a), hi = tr_rd(a + 8 * 128);
        u32x4 f = {lo[0], lo[1], hi[0], hi[1]};
        cs = dot2(f[0], ones, cs); cs = dot2(f[1], ones, cs); cs = dot2(f[2], ones, cs); cs = dot2(f[3], ones, cs);
        const int kap = 2 * k32 + kk;
        *(u32x4*)(ws + OFF_WF + ((size_t)((tile / 7) * 32 + kap) * 7 + (tile % 7)) * 1024 + lane * 16) = f;
      }
      cs += xhalf(cs);
      if (hh == 0) *(float*)(ws + OFF_CS + ((size_t)(tile * 32 + lane) * 16 + k32) * 4) = cs;
    }
  } else if (u < 672) {
    const int dq = (u - 448) & 7, k64 = (u - 448) >> 3;
#pragma unroll
    for (int t = 0; t < 2; ++t)
#pragma unroll
      for (int kk = 0; kk < 4; ++kk) {
        const unsigned a = scr + (16 * kk + 4 * hh + q) * 128 + t * 64 + g16 * 32 + p * 8;
        u32x2 lo = tr_rd(a), hi = tr_rd(a + 8 * 128);
        u32x4 f = {lo[0], lo[1], hi[0], hi[1]};
        *(u32x4*)(ws + OFF_WD + ((size_t)(2 * dq + t) * 112 + 4 * k64 + kk) * 1024 + lane * 16) = f;
      }
  }
}

DI f32x16 reduce4(f32x16 (&acc)[2][2], unsigned L0, unsigned wstride, int w, int lane) {
  const unsigned mine = L0 + w * wstride + lane * 16;
#pragma unroll
  for (int sub = 0; sub < 4; ++sub) {
    if (sub != w) {
#pragma unroll
      for (int q = 0; q < 4; ++q) {
        const f32x16& a = acc[sub & 1][sub >> 1];
        *(LAS f32x4*)(size_t)(mine + sub * 4096 + q * 1024) = f32x4{a[4 * q], a[4 * q + 1], a[4 * q + 2], a[4 * q + 3]};
      }
    }
  }
  f32x16 o = (w == 0) ? acc[0][0] : (w == 1) ? acc[1][0] : (w == 2) ? acc[0][1] : acc[1][1];
  BAR();
#pragma unroll
  for (int ow = 1; ow < 4; ++ow) {
    const unsigned src = L0 + ((w + ow) & 3) * wstride + w * 4096 + lane * 16;
#pragma unroll
    for (int q = 0; q < 4; ++q) {
      f32x4 t = *(const LAS f32x4*)(size_t)(src + q * 1024);
      o[4 * q] += t[0]; o[4 * q + 1] += t[1]; o[4 * q + 2] += t[2]; o[4 * q + 3] += t[3];
    }
  }
  return o;
}

__global__ __launch_bounds__(256, 1) void k2(const u16* __restrict__ x, const u16* __restrict__ Cr, const u16* __restrict__ Ci,
                                             const u16* __restrict__ Wg, const u16* __restrict__ Wu, const u16* __restrict__ Wd, char* __restrict__ ws) {
  extern __shared__ __attribute__((aligned(16))) char smem[];
  const unsigned L0 = (unsigned)(size_t)smem;
  STAMP_DECL; STAMP_AT(ws, 1, 0);
  const int lane = threadIdx.x & 63, w = __builtin_amdgcn_readfirstlane(threadIdx.x >> 6);
  const int g = blockIdx.x & 7, j = blockIdx.x >> 3;
  const int it = 4 * g + (j & 3), ie = j >> 2;
  const int r = lane & 31, hh = lane >> 5;
  u32x2 xr[4];
  {
    const char* xp = (const char*)x + (size_t)(64 * it + 32 * (w >> 1) + r) * 1024 + (64 * ie + 32 * (w & 1) + 4 * hh) * 2;
#pragma unroll
    for (int q = 0; q < 4; ++q) xr[q] = *(const u32x2*)(xp + q * 16);
  }
  const int u = w * 256 + blockIdx.x;
  const unsigned scr = L0 + 131072 + w * 8192;
  retile_issue(u, scr, lane, Wg, Wu, Wd);

  unsigned vo[8];
#pragma unroll
  for (int q = 0; q < 8; ++q) vo[q] = (8 * q + (lane >> 3)) * 4096 + (((lane & 7) ^ ((4 * q + (lane >> 4)) & 7)) * 16);
  const char* cb = (const char*)((w >= 2) ? Ci : Cr) + (size_t)(64 * it) * 4096 + (w & 1) * 2048;
  const char* ab0 = ws + OFF_AB + ((size_t)((w >> 1) * 16 + 2 * ie) * 128 + (w & 1) * 64) * 1024;
  const unsigned vl = lane * 16;
  const unsigned ring = L0 + w * 32768;
  u32x4 bf[4][8];
  const int s0 = 2 * ie + (w >> 1);
  auto issue = [&](int s, u32x4 (&b)[8]) {
    const int sx = (s + s0) & 15;
    ld4(b[0], b[1], b[2], b[3], ab0 + sx * 4096, vl);
    ld4(b[4], b[5], b[6], b[7], ab0 + 131072 + sx * 4096, vl);
    dma8(ring + (s & 3) * 8192, cb + sx * 128, vo);
  };
  issue(0, bf[0]); issue(1, bf[1]); issue(2, bf[2]);
  WAITV(48);
  retile_finish(u, scr, lane, ws);
  issue(3, bf[3]);
  unsigned ra[4];
#pragma unroll
  for (int k = 0; k < 4; ++k) ra[k] = ring + r * 128 + (((2 * k + hh) ^ ((r >> 1) & 7)) * 16);
  f32x16 acc[2][2] = {};
#define MM(i, jx, x, y) acc[i][jx] = mfma32(x, y, acc[i][jx]); SB()
  auto stage = [&](auto slotc, auto refill, int snext, u32x4 (&b)[8]) {
    constexpr int SL = decltype(slotc)::value * 8192;
    constexpr bool RF = decltype(refill)::value;
    u32x4 c[8];
#pragma unroll
    for (int k = 0; k < 4; ++k) { c[2 * k] = lds_rd(ra[k] + SL); c[2 * k + 1] = lds_rd(ra[k] + SL + 4096); }
    const int sx = (snext + s0) & 15;
    const char* pa = ab0 + sx * 4096; const char* pb = pa + 131072; const char* pc = cb + sx * 128;
    SB();
    MM(0, 0, b[0], c[0]); MM(1, 0, b[4], c[0]); MM(0, 1, b[0], c[1]); MM(1, 1, b[4], c[1]);
    if (RF) { WAITL0(); ld1<0>(b[0], pa, vl); SB(); }
    MM(0, 0, b[1], c[2]); if (RF) { ld1<0>(b[4], pb, vl); SB(); }
    MM(1, 0, b[5], c[2]); if (RF) { dma1<SL>(ring, pc, vo[0]); SB(); }
    MM(0, 1, b[1], c[3]); if (RF) { dma1<SL + 1024>(ring, pc, vo[1]); SB(); }
    MM(1, 1, b[5], c[3]); if (RF) { ld1<1024>(b[1], pa, vl); dma1<SL + 2048>(ring, pc, vo[2]); SB(); }
    MM(0, 0, b[2], c[4]); if (RF) { ld1<1024>(b[5], pb, vl); SB(); }
    MM(1, 0, b[6], c[4]); if (RF) { dma1<SL + 3072>(ring, pc, vo[3]); SB(); }
    MM(0, 1, b[2], c[5]); if (RF) { dma1<SL + 4096>(ring, pc, vo[4]); SB(); }
    MM(1, 1, b[6], c[5]); if (RF) { ld1<2048>(b[2], pa, vl); dma1<SL + 5120>(ring, pc, vo[5]); SB(); }
    MM(0, 0, b[3], c[6]); if (RF) { ld1<2048>(b[6], pb, vl); SB(); }
    MM(1, 0, b[7], c[6]); if (RF) { dma1<SL + 6144>(ring, pc, vo[6]); SB(); }
    MM(0, 1, b[3], c[7]); if (RF) { dma1<SL + 7168>(ring, pc, vo[7]); SB(); }
    MM(1, 1, b[7], c[7]); if (RF) { ld1<3072>(b[3], pa, vl); ld1<3072>(b[7], pb, vl); SB(); }
  };
  using I0 = IC<0>; using I1 = IC<1>; using I2 = IC<2>; using I3 = IC<3>;
#pragma unroll 1
  for (int sb = 0; sb < 12; sb += 4) {
    WAIT8(48, bf[0]); if (sb == 0) STAMP_AT(ws, 1, 3); stage(I0{}, I1{}, sb + 4, bf[0]);
    WAIT8(48, bf[1]); stage(I1{}, I1{}, sb + 5, bf[1]);
    WAIT8(48, bf[2]); stage(I2{}, I1{}, sb + 6, bf[2]);
    WAIT8(48, bf[3]); stage(I3{}, I1{}, sb + 7, bf[3]);
  }
  WAIT8(48, bf[0]); stage(I0{}, I0{}, 0, bf[0]);
  WAIT8(32, bf[1]); stage(I1{}, I0{}, 0, bf[1]);
  WAIT8(16, bf[2]); stage(I2{}, I0{}, 0, bf[2]);
  WAIT8(0, bf[3]); stage(I3{}, I0{}, 0, bf[3]);
  STAMP_AT(ws, 1, 2);
  f32x16 o = reduce4(acc, L0, 32768, w, lane);
  float s1 = 0.f, s2 = 0.f;
  u32x4 fr[2];
  const unsigned ones = 0x3f803f80u;
#pragma unroll
  for (int q = 0; q < 4; ++q) {
    unsigned p0 = pk2(o[4 * q] + bflo(xr[q][0]), o[4 * q + 1] + bfhi(xr[q][0]));
    unsigned p1 = pk2(o[4 * q + 2] + bflo(xr[q][1]), o[4 * q + 3] + bfhi(xr[q][1]));
    s1 = dot2(p0, ones, s1); s1 = dot2(p1, ones, s1);
    s2 = dot2(p0, p0, s2); s2 = dot2(p1, p1, s2);
    fr[q >> 1][2 * (q & 1)] = p0; fr[q >> 1][2 * (q & 1) + 1] = p1;
  }
  const int st = 2 * it + (w >> 1), et = 2 * ie + (w & 1);
  char* hp = ws + OFF_H + ((size_t)st * 32 + 2 * et) * 1024 + lane * 16;
  *(u32x4*)hp = fr[0];
  *(u32x4*)(hp + 1024) = fr[1];
  s1 += xhalf(s1); s2 += xhalf(s2);
  if (hh == 0) *(f32x2*)(ws + OFF_ST + ((size_t)(st * 16 + et) * 32 + r) * 8) = f32x2{s1, s2};
  STAMP_AT(ws, 1, 1);
}

__global__ __launch_bounds__(256, 1) void k3(char* __restrict__ ws) {
  extern __shared__ __attribute__((aligned(16))) char smem[];
  const unsigned L0 = (unsigned)(size_t)smem;
  STAMP_DECL; STAMP_AT(ws, 2, 0);
  const int lane = threadIdx.x & 63, w = __builtin_amdgcn_readfirstlane(threadIdx.x >> 6);
  const int g = blockIdx.x & 7, j = blockIdx.x >> 3;
  const int is = (g >> 2) * 8 + (j & 7), jf = (g & 3) * 4 + (j >> 3);
  const int r = lane & 31, hh = lane >> 5;
  const int st = 4 * is + w;
  f32x2 sp[8];
  f32x4 cp[4];
  {
    const char* sb_ = ws + OFF_ST + (size_t)st * 4096;
    const unsigned v_ = hh * 2048 + r * 8;
    asm volatile("global_load_dwordx2 %0, %8, %9\n\tglobal_load_dwordx2 %1, %8, %9 offset:256\n\tglobal_load_dwordx2 %2, %8, %9 offset:512\n\tglobal_load_dwordx2 %3, %8, %9 offset:768\n\t"
                 "global_load_dwordx2 %4, %8, %9 offset:1024\n\tglobal_load_dwordx2 %5, %8, %9 offset:1280\n\tglobal_load_dwordx2 %6, %8, %9 offset:1536\n\tglobal_load_dwordx2 %7, %8, %9 offset:1792"
                 : "=&v"(sp[0]), "=&v"(sp[1]), "=&v"(sp[2]), "=&v"(sp[3]), "=&v"(sp[4]), "=&v"(sp[5]), "=&v"(sp[6]), "=&v"(sp[7]) : "v"(v_), "s"(sb_) : "memory");
    const char* cb_ = ws + OFF_CS + (size_t)jf * 224 * 64;
    const unsigned t_ = (threadIdx.x < 224 ? threadIdx.x : 223) * 64;
    asm volatile("global_load_dwordx4 %0, %4, %5\n\tglobal_load_dwordx4 %1, %4, %5 offset:16\n\tglobal_load_dwordx4 %2, %4, %5 offset:32\n\tglobal_load_dwordx4 %3, %4, %5 offset:48"
                 : "=&v"(cp[0]), "=&v"(cp[1]), "=&v"(cp[2]), "=&v"(cp[3]) : "v"(t_), "s"(cb_) : "memory");
  }
  const unsigned vl = lane * 16;
  const char* hb = ws + OFF_H + (size_t)st * 32768;
  const char* wb = ws + OFF_WF + (size_t)jf * 229376 + w * 7168;
  u32x4 hf[32];
  auto issueW = [&](int c) {
    const unsigned dst = L0 + (c % 5) * 28672 + w * 7168;
    dma_lin4(dst, wb + c * 28672, vl);
    dma_lin3(dst + 4096, wb + c * 28672 + 4096, vl);
  };
#pragma unroll
  for (int c = 0; c < 5; ++c) { ld4(hf[4 * c], hf[4 * c + 1], hf[4 * c + 2], hf[4 * c + 3], hb + c * 4096, vl); issueW(c); }
#pragma unroll
  for (int c = 5; c < 8; ++c) ld4(hf[4 * c], hf[4 * c + 1], hf[4 * c + 2], hf[4 * c + 3], hb + c * 4096, vl);
  f32x16 acc[7] = {};
  u32x4 wr[2][7];
  const unsigned lb = L0 + vl;
  const unsigned csb = L0 + 3 * 28672;
  auto kstep = [&](auto kc) {
    constexpr int kap = decltype(kc)::value;
    constexpr int nx = kap + 1;
    constexpr int off = ((nx >> 2) % 5) * 28672 + (nx & 3) * 7168;
#pragma unroll
    for (int n = 0; n < 7; ++n) {
      if (nx < 32) wr[nx & 1][n] = lds_rd(lb + off + n * 1024);
      acc[n] = mfma32(wr[kap & 1][n], hf[kap], acc[n]);
      SB();
    }
  };
#define HW(n, c) WAIT4(n, hf[4 * c], hf[4 * c + 1], hf[4 * c + 2], hf[4 * c + 3])
#define KS3(c) kstep(IC<4 * c>{}); kstep(IC<4 * c + 1>{}); kstep(IC<4 * c + 2>{})
  HW(56, 0); WAIT8(56, sp); WAIT4(56, cp[0], cp[1], cp[2], cp[3]); RAWBAR();
  STAMP_AT(ws, 2, 3);
#pragma unroll
  for (int n = 0; n < 7; ++n) wr[0][n] = lds_rd(lb + n * 1024);
  SB();
  KS3(0); HW(45, 1); RAWBAR(); kstep(IC<3>{});
  KS3(1); HW(34, 2); RAWBAR(); issueW(5); kstep(IC<7>{});
  KS3(2); HW(30, 3); RAWBAR(); issueW(6); kstep(IC<11>{});
  KS3(3); HW(26, 4); RAWBAR(); issueW(7); kstep(IC<15>{});
  KS3(4); HW(14, 5); RAWBAR(); kstep(IC<19>{});
  if (threadIdx.x < 224) {
    f32x4 t = cp[0] + cp[1] + cp[2] + cp[3];
    *(LAS float*)(size_t)(csb + threadIdx.x * 4) = (t[0] + t[1]) + (t[2] + t[3]);
  }
  KS3(5); HW(7, 6); RAWBAR(); kstep(IC<23>{});
  KS3(6); HW(0, 7); BAR(); kstep(IC<27>{});
  KS3(7); kstep(IC<31>{});
  STAMP_AT(ws, 2, 2);
  float s1 = 0.f, s2 = 0.f;
#pragma unroll
  for (int q = 0; q < 8; ++q) { s1 += sp[q][0]; s2 += sp[q][1]; }
  s1 += xhalf(s1); s2 += xhalf(s2);
  const float mean = s1 * (1.f / D);
  const float rs = rsqrtf(fmaxf(s2 * (1.f / D) - mean * mean, 0.f) + 1e-5f);
  const float nm = -mean * rs;
  char* op = ws + OFF_HH + ((size_t)st * 112 + 7 * jf) * 1024 + vl;
#pragma unroll
  for (int n = 0; n < 7; ++n) {
    const unsigned cb = csb + n * 128 + hh * 16;
    f32x4 cg0 = *(const LAS f32x4*)(size_t)(cb), cg1 = *(const LAS f32x4*)(size_t)(cb + 32);
    f32x4 cu0 = *(const LAS f32x4*)(size_t)(cb + 64), cu1 = *(const LAS f32x4*)(size_t)(cb + 96);
    float v[8];
#pragma unroll
    for (int q = 0; q < 8; ++q) {
      const float cg = q < 4 ? cg0[q & 3] : cg1[q & 3], cu = q < 4 ? cu0[q & 3] : cu1[q & 3];
      const float gg = acc[n][q] * rs + nm * cg, uu = acc[n][8 + q] * rs + nm * cu;
      v[q] = uu * __builtin_amdgcn_rcpf(1.f + __expf(-gg));
    }
    u32x4 o = {pk2(v[0], v[1]), pk2(v[2], v[3]), pk2(v[4], v[5]), pk2(v[6], v[7])};
    *(u32x4*)(op + n * 1024) = o;
  }
  STAMP_AT(ws, 2, 1);
}

__global__ __launch_bounds__(256, 1) void k4(char* __restrict__ ws, u16* __restrict__ out) {
  extern __shared__ __attribute__((aligned(16))) char smem[];
  const unsigned L0 = (unsigned)(size_t)smem;
  STAMP_DECL; STAMP_AT(ws, 3, 0);
  const int lane = threadIdx.x & 63, w = __builtin_amdgcn_readfirstlane(threadIdx.x >> 6);
  const int g = blockIdx.x & 7, j = blockIdx.x >> 3;
  const int it = 4 * g + (j & 3), id = j >> 2;
  const int r = lane & 31, hh = lane >> 5;
  const unsigned vl = lane * 16;
  u32x4 hr[2];
  {
    const char* hp = ws + OFF_H + ((size_t)(2 * it + (w >> 1)) * 32 + 2 * (2 * id + (w & 1))) * 1024 + vl;
    hr[0] = *(const u32x4*)hp; hr[1] = *(const u32x4*)(hp + 1024);
  }
  const char* wb = ws + OFF_WD + ((size_t)(2 * id) * 112 + 28 * w) * 1024;
  const char* ab = ws + OFF_HH + ((size_t)(2 * it) * 112 + 28 * w) * 1024;
  u32x4 f[3][16];
  auto issue = [&](int s, u32x4 (&b)[16]) {
    ld4(b[0], b[1], b[2], b[3], wb + s * 4096, vl);
    ld4(b[8], b[9], b[10], b[11], ab + s * 4096, vl);
    ld4(b[4], b[5], b[6], b[7], wb + 114688 + s * 4096, vl);
    ld4(b[12], b[13], b[14], b[15], ab + 114688 + s * 4096, vl);
  };
  f32x16 acc[2][2] = {};
  auto compute = [&](u32x4 (&b)[16]) {
#pragma unroll
    for (int k = 0; k < 4; ++k) {
      acc[0][0] = mfma32(b[k], b[8 + k], acc[0][0]);
      acc[1][0] = mfma32(b[4 + k], b[8 + k], acc[1][0]);
      acc[0][1] = mfma32(b[k], b[12 + k], acc[0][1]);
      acc[1][1] = mfma32(b[4 + k], b[12 + k], acc[1][1]);
    }
    SB();
  };
#define W16(n, x) do { WAIT8(n, x); asm volatile("" : "+v"(x[8]), "+v"(x[9]), "+v"(x[10]), "+v"(x[11]), "+v"(x[12]), "+v"(x[13]), "+v"(x[14]), "+v"(x[15])); } while (0)
  issue(0, f[0]); issue(1, f[1]); issue(2, f[2]);
  W16(32, f[0]); STAMP_AT(ws, 3, 3); compute(f[0]); issue(3, f[0]);
  W16(32, f[1]); compute(f[1]); issue(4, f[1]);
  W16(32, f[2]); compute(f[2]); issue(5, f[2]);
  W16(32, f[0]); compute(f[0]); issue(6, f[0]);
  W16(32, f[1]); compute(f[1]);
  W16(16, f[2]); compute(f[2]);
  W16(0, f[0]); compute(f[0]);
  STAMP_AT(ws, 3, 2);
  f32x16 o = reduce4(acc, L0, 16384, w, lane);
  unsigned p[8];
#pragma unroll
  for (int q = 0; q < 8; ++q) {
    const unsigned hv = hr[q >> 2][q & 3];
    p[q] = pk2(o[2 * q] + bflo(hv), o[2 * q + 1] + bfhi(hv));
  }
  char* op = (char*)out + (size_t)(64 * it + 32 * (w >> 1) + r) * 1024 + (64 * id + 32 * (w & 1) + 8 * hh) * 2;
#pragma unroll
  for (int kk = 0; kk < 2; ++kk) {
    auto s0 = __builtin_amdgcn_permlane32_swap(p[4 * kk], p[4 * kk + 2], false, false);
    auto s1 = __builtin_amdgcn_permlane32_swap(p[4 * kk + 1], p[4 * kk + 3], false, false);
    u32x4 v = {s0[0], s1[0], s0[1], s1[1]};
    *(u32x4*)(op + kk * 32) = v;
  }
  STAMP_AT(ws, 3, 1);
}

extern "C" void kernel_launch(void* const* d_in, const int* in_sizes, int n_in, void* d_out, int out_size,
                              void* d_ws, size_t ws_size, hipStream_t stream) {
  const u16* x = (const u16*)d_in[0];
  const u16* Cr = (const u16*)d_in[1];
  const u16* Ci = (const u16*)d_in[2];
  const u16* Dr = (const u16*)d_in[3];
  const u16* Di = (const u16*)d_in[4];
  const u16* Wg = (const u16*)d_in[6];
  const u16* Wu = (const u16*)d_in[7];
  const u16* Wd = (const u16*)d_in[8];
  char* ws = (char*)d_ws;
  hipFuncSetAttribute((const void*)k1, hipFuncAttributeMaxDynamicSharedMemorySize, 147456);
  hipFuncSetAttribute((const void*)k2, hipFuncAttributeMaxDynamicSharedMemorySize, 163840);
  hipFuncSetAttribute((const void*)k3, hipFuncAttributeMaxDynamicSharedMemorySize, 143360);
  hipFuncSetAttribute((const void*)k4, hipFuncAttributeMaxDynamicSharedMemorySize, 65536);
  k1<<<256, 256, 147456, stream>>>(x, Dr, Di, ws);
  k2<<<256, 256, 163840, stream>>>(x, Cr, Ci, Wg, Wu, Wd, ws);
  k3<<<256, 256, 143360, stream>>>(ws);
  k4<<<256, 256, 65536, stream>>>(ws, (u16*)d_out);
}
```

```cpp
#include <hip/hip_runtime.h>
#include <stdint.h>
#define NSTAMPS 0

__global__ __launch_bounds__(256, 1) void mega(const void* x, const void* cr, const void* ci, const void* dr, const void* di,
                                               const void* wg, const void* wu, const void* wd, void* ws, void* out) {
  unsigned tid = threadIdx.x;
  asm volatile(
    "s_load_dwordx16 s[8:23], %1, 0\n"
    "s_load_dwordx4 s[24:27], %1, 64\n"
    "s_mov_b32 s60, %2\n"
    "v_and_b32 v1, 63, %0\n"
    "v_lshrrev_b32 v7, 6, %0\n"
    "v_lshlrev_b32 v2, 4, v1\n"
    "v_readfirstlane_b32 s61, v7\n"
    "v_and_b32 v5, 31, v1\n"
    "v_lshrrev_b32 v6, 5, v1\n"
    "s_bfe_u32 s63, s60, 0x20001\n"
    "s_lshr_b32 s64, s60, 6\n"
    "s_bfe_u32 s75, s60, 0x20003\n"
    "s_and_b32 s74, s60, 4\n"
    "s_andn2_b32 s76, 2, s60\n"
    "s_lshl_b32 s65, s76, 2\n"
    "s_add_u32 s65, s65, s74\n"
    "s_add_u32 s65, s65, s75\n"
    "s_and_b32 s74, s60, 1\n"
    "s_bfe_u32 s75, s60, 0x10005\n"
    "s_bfe_u32 s76, s60, 0x10007\n"
    "s_lshl_b32 s68, s74, 2\n"
    "s_lshl_b32 s73, s76, 1\n"
    "s_add_u32 s68, s68, s73\n"
    "s_add_u32 s68, s68, s75\n"
    "s_bfe_u32 s75, s60, 0x30003\n"
    "s_lshl_b32 s62, s74, 3\n"
    "s_add_u32 s62, s62, s75\n"
    "s_mov_b32 s30, 0x200000\n"
    "s_mov_b32 s31, 0x20000\n"
    "s_mov_b32 s34, 0x80000\n"
    "s_mov_b32 s35, 0x20000\n"
    "s_cmp_lt_u32 s68, 4\n"
    "s_waitcnt lgkmcnt(0)\n"
    "s_cselect_b64 s[32:33], s[14:15], s[16:17]\n"
    "s_mov_b64 s[28:29], s[8:9]\n"
    "s_bitcmp1_b32 s64, 0\n"
    "s_cbranch_scc1 Ly1_%=\n"
    "s_lshl_b32 s74, s61, 15\n"
    "v_lshrrev_b32 v7, 4, v1\n"
    "v_and_b32 v31, 7, v1\n"
    "v_xor_b32 v7, v7, v31\n"
    "v_lshlrev_b32 v7, 4, v7\n"
    "v_lshrrev_b32 v31, 3, v1\n"
    "v_mul_u32_u24 v31, 0x400, v31\n"
    "v_add3_u32 v28, v7, v31, s74\n"
    "v_xor_b32 v29, 64, v28\n"
    "v_add_u32 v29, 0x1c00, v29\n"
    "v_add_u32 v30, 0x3800, v28\n"
    "v_add_u32 v31, 0x3800, v29\n"
    "s_lshl_b32 s84, s65, 17\n"
    "s_and_b32 s85, s68, 3\n"
    "s_lshl_b32 s85, s85, 17\n"
    "s_lshl_b32 s86, s61, 12\n"
    "s_add_u32 m0, s86, 0\n"
    "s_nop 0\n"
    "buffer_load_dwordx4 v28, s[28:31], s84 offen lds\n"
    "buffer_load_dwordx4 v29, s[28:31], s84 offen offset:1024 lds\n"
    "buffer_load_dwordx4 v30, s[28:31], s84 offen offset:2048 lds\n"
    "buffer_load_dwordx4 v31, s[28:31], s84 offen offset:3072 lds\n"
    "s_add_u32 m0, s86, 0x4000\n"
    "s_nop 0\n"
    "buffer_load_dwordx4 v28, s[32:35], s85 offen lds\n"
    "buffer_load_dwordx4 v29, s[32:35], s85 offen offset:1024 lds\n"
    "buffer_load_dwordx4 v30, s[32:35], s85 offen offset:2048 lds\n"
    "buffer_load_dwordx4 v31, s[32:35], s85 offen offset:3072 lds\n"
    "s_mov_b64 s[52:53], s[24:25]\n"
    "s_mov_b32 s54, 0x1600000\n"
    "s_mov_b32 s55, 0x20000\n"
    "s_mov_b32 s76, 0x480000\n"
    "buffer_load_dword v4, off, s[52:55], s76 sc1\n"
    "s_mov_b32 s67, 0x3f803f80\n"
    "s_lshl_b32 s69, s63, 2\n"
    "s_add_u32 s69, s69, s64\n"
    "s_lshl_b32 s72, s61, 13\n"
    "s_cmp_lt_u32 s63, 2\n"
    "s_cselect_b64 s[36:37], s[10:11], s[12:13]\n"
    "s_lshl_b32 s74, s62, 19\n"
    "s_add_u32 s36, s36, s74\n"
    "s_addc_u32 s37, s37, 0\n"
    "s_mov_b32 s38, 0x80000\n"
    "s_mov_b32 s39, 0x20000\n"
    "s_mov_b64 s[40:41], s[18:19]\n"
    "s_mov_b64 s[44:45], s[20:21]\n"
    "s_mov_b64 s[48:49], s[22:23]\n"
    "s_mov_b64 s[56:57], s[26:27]\n"
    "s_mov_b32 s58, 0x200000\n"
    "s_mov_b32 s59, 0x20000\n"
    "v_bfe_u32 v7, v5, 1, 3\n"
    "v_xor_b32 v7, v7, v6\n"
    "v_lshlrev_b32 v7, 4, v7\n"
    "v_lshl_or_b32 v3, v5, 7, v7\n"
    "s_xor_b32 s84, s84, 0x80\n"
    "s_add_u32 m0, s86, 0x8000\n"
    "s_nop 0\n"
    "buffer_load_dwordx4 v28, s[28:31], s84 offen lds\n"
    "buffer_load_dwordx4 v29, s[28:31], s84 offen offset:1024 lds\n"
    "buffer_load_dwordx4 v30, s[28:31], s84 offen offset:2048 lds\n"
    "buffer_load_dwordx4 v31, s[28:31], s84 offen offset:3072 lds\n"
    "s_xor_b32 s85, s85, 0x80\n"
    "s_add_u32 m0, s86, 0xc000\n"
    "s_nop 0\n"
    "buffer_load_dwordx4 v28, s[32:35], s85 offen lds\n"
    "buffer_load_dwordx4 v29, s[32:35], s85 offen offset:1024 lds\n"
    "buffer_load_dwordx4 v30, s[32:35], s85 offen offset:2048 lds\n"
    "buffer_load_dwordx4 v31, s[32:35], s85 offen offset:3072 lds\n"
    "s_branch Lx2_%=\n"
    "Ly1_%=:\n"
    "s_nop 0\n"
    "s_lshl_b32 s88, s65, 5\n"
    "s_lshl_b32 s73, s68, 2\n"
    "s_add_u32 s88, s88, s73\n"
    "s_add_u32 s88, s88, s61\n"
    "v_lshrrev_b32 v51, 4, v1\n"
    "v_and_b32 v52, 15, v1\n"
    "v_lshlrev_b32 v52, 4, v52\n"
    "v_lshl_or_b32 v51, v51, 10, v52\n"
    "s_lshl_b32 s74, s88, 12\n"
    "buffer_load_dwordx4 v[24:27], v51, s[28:31], s74 offen\n"
    "buffer_load_dwordx4 v[28:31], v51, s[28:31], s74 offen offset:256\n"
    "buffer_load_dwordx4 v[32:35], v51, s[28:31], s74 offen offset:512\n"
    "buffer_load_dwordx4 v[36:39], v51, s[28:31], s74 offen offset:768\n"
    "v_lshlrev_b32 v53, 7, v1\n"
    "s_and_b32 s75, s65, 3\n"
    "s_and_b32 s76, s68, 3\n"
    "s_lshl_b32 s75, s75, 17\n"
    "s_lshl_b32 s73, s76, 15\n"
    "s_add_u32 s75, s75, s73\n"
    "s_lshl_b32 s73, s61, 13\n"
    "s_add_u32 s75, s75, s73\n"
    "s_xor_b32 s76, s74, 0x10000\n"
    "s_mov_b32 exec_lo, 0xfcfcfcfc\n"
    "s_mov_b32 exec_hi, 0xfcfcfcfc\n"
    "buffer_load_dword v48, v53, s[32:35], s75 offen\n"
    "s_mov_b32 exec_hi, 0\n"
    "buffer_load_dword v49, v53, s[28:31], s76 offen\n"
    "s_getpc_b64 s[92:93]\n"
    "Lpc_%=:\n"
    "s_mov_b32 s95, 0x20000\n"
    "s_mov_b32 s94, Lfin_%=-Lpc_%=\n"
    "s_bfe_u32 s75, s60, 0x30003\n"
    "s_lshr_b32 s76, s60, 7\n"
    "s_lshl_b32 s75, s75, 13\n"
    "s_lshl_b32 s73, s76, 12\n"
    "s_add_u32 s75, s75, s73\n"
    "s_lshl_b32 s73, s61, 10\n"
    "s_add_u32 s75, s75, s73\n"
    "s_mov_b32 exec_lo, 0xff\n"
    "buffer_load_dword v50, v53, s[92:95], s75 offen\n"
    "s_mov_b64 exec, -1\n"
    "v_lshlrev_b32 v52, 4, v5\n"
    "v_lshl_or_b32 v52, v6, 10, v52\n"
    "s_and_b32 s87, s68, 3\n"
    "s_lshl_b32 s87, s87, 2\n"
    "s_lshl_b32 s73, s65, 4\n"
    "s_add_u32 s87, s87, s73\n"
    "s_add_u32 s87, s87, s61\n"
    "s_lshl_b32 s79, s87, 11\n"
    "buffer_load_dwordx4 v[40:43], v52, s[32:35], s79 offen\n"
    "buffer_load_dwordx4 v[44:47], v52, s[32:35], s79 offen offset:512\n"
    "s_mov_b64 s[52:53], s[24:25]\n"
    "s_mov_b32 s54, 0x1600000\n"
    "s_mov_b32 s55, 0x20000\n"
    "s_mov_b32 s76, 0x480000\n"
    "buffer_load_dword v4, off, s[52:55], s76 sc1\n"
    "s_mov_b32 s67, 0x3f803f80\n"
    "s_lshl_b32 s69, s63, 2\n"
    "s_add_u32 s69, s69, s64\n"
    "s_lshl_b32 s72, s61, 13\n"
    "s_cmp_lt_u32 s63, 2\n"
    "s_cselect_b64 s[36:37], s[10:11], s[12:13]\n"
    "s_lshl_b32 s74, s62, 19\n"
    "s_add_u32 s36, s36, s74\n"
    "s_addc_u32 s37, s37, 0\n"
    "s_mov_b32 s38, 0x80000\n"
    "s_mov_b32 s39, 0x20000\n"
    "s_mov_b64 s[40:41], s[18:19]\n"
    "s_mov_b64 s[44:45], s[20:21]\n"
    "s_mov_b64 s[48:49], s[22:23]\n"
    "s_mov_b64 s[56:57], s[26:27]\n"
    "s_mov_b32 s58, 0x200000\n"
    "s_mov_b32 s59, 0x20000\n"
    "v_bfe_u32 v7, v5, 1, 3\n"
    "v_xor_b32 v7, v7, v6\n"
    "v_lshlrev_b32 v7, 4, v7\n"
    "v_lshl_or_b32 v3, v5, 7, v7\n"
    "v_lshrrev_b32 v7, 4, v1\n"
    "v_and_b32 v11, 7, v1\n"
    "v_xor_b32 v7, v7, v11\n"
    "v_lshlrev_b32 v7, 4, v7\n"
    "v_lshrrev_b32 v11, 3, v1\n"
    "v_mul_u32_u24 v11, 0x1000, v11\n"
    "v_add_u32 v8, v7, v11\n"
    "v_xor_b32 v9, 64, v8\n"
    "v_add_u32 v9, 0x7c00, v9\n"
    "v_add_u32 v10, 0xf800, v8\n"
    "v_add_u32 v11, 0xf800, v9\n"
    "s_and_b32 s75, s64, 2\n"
    "s_and_b32 s74, s63, 1\n"
    "s_lshl_b32 s27, s61, 17\n"
    "s_lshl_b32 s73, s74, 11\n"
    "s_add_u32 s27, s27, s73\n"
    "s_lshl_b32 s73, s75, 9\n"
    "s_add_u32 s27, s27, s73\n"
    "s_cmp_eq_u32 s61, 0\n"
    "s_cselect_b64 s[24:25], -1, 0\n"
    "v_or_b32 v7, s72, v3\n"
    "v_xor_b32 v12, 0, v7\n"
    "v_xor_b32 v13, 32, v7\n"
    "v_xor_b32 v14, 64, v7\n"
    "v_xor_b32 v15, 0x60, v7\n"
    "v_xor_b32 v16, 0x10000, v7\n"
    "v_xor_b32 v17, 0x10020, v7\n"
    "v_xor_b32 v18, 0x10040, v7\n"
    "v_xor_b32 v19, 0x10060, v7\n"
    "v_xor_b32 v20, 0x20000, v7\n"
    "v_xor_b32 v21, 0x20020, v7\n"
    "v_xor_b32 v22, 0x20040, v7\n"
    "v_xor_b32 v23, 0x20060, v7\n"
    "s_xor_b32 s96, s27, 0\n"
    "s_add_u32 s97, s96, 0x80\n"
    "s_add_u32 m0, s72, 0\n"
    "s_nop 0\n"
    "buffer_load_dwordx4 v8, s[36:39], s96 offen lds\n"
    "buffer_load_dwordx4 v9, s[36:39], s96 offen offset:1024 lds\n"
    "buffer_load_dwordx4 v10, s[36:39], s96 offen offset:2048 lds\n"
    "buffer_load_dwordx4 v11, s[36:39], s96 offen offset:3072 lds\n"
    "v_mov_b32 v56, 0\n"
    "v_mov_b32 v58, 0\n"
    "v_mov_b32 v60, 0\n"
    "s_waitcnt vmcnt(13)\n"
    "v_dot2c_f32_bf16 v56, s67, v24\n"
    "v_dot2c_f32_bf16 v58, v24, v24\n"
    "v_dot2c_f32_bf16 v56, s67, v25\n"
    "v_dot2c_f32_bf16 v58, v25, v25\n"
    "v_dot2c_f32_bf16 v56, s67, v26\n"
    "v_dot2c_f32_bf16 v58, v26, v26\n"
    "v_dot2c_f32_bf16 v56, s67, v27\n"
    "v_dot2c_f32_bf16 v58, v27, v27\n"
    "s_waitcnt vmcnt(12)\n"
    "v_dot2c_f32_bf16 v56, s67, v28\n"
    "v_dot2c_f32_bf16 v58, v28, v28\n"
    "v_dot2c_f32_bf16 v56, s67, v29\n"
    "v_dot2c_f32_bf16 v58, v29, v29\n"
    "v_dot2c_f32_bf16 v56, s67, v30\n"
    "v_dot2c_f32_bf16 v58, v30, v30\n"
    "v_dot2c_f32_bf16 v56, s67, v31\n"
    "v_dot2c_f32_bf16 v58, v31, v31\n"
    "s_waitcnt vmcnt(11)\n"
    "v_dot2c_f32_bf16 v56, s67, v32\n"
    "v_dot2c_f32_bf16 v58, v32, v32\n"
    "v_dot2c_f32_bf16 v56, s67, v33\n"
    "v_dot2c_f32_bf16 v58, v33, v33\n"
    "v_dot2c_f32_bf16 v56, s67, v34\n"
    "v_dot2c_f32_bf16 v58, v34, v34\n"
    "v_dot2c_f32_bf16 v56, s67, v35\n"
    "v_dot2c_f32_bf16 v58, v35, v35\n"
    "s_waitcnt vmcnt(10)\n"
    "v_dot2c_f32_bf16 v56, s67, v36\n"
    "v_dot2c_f32_bf16 v58, v36, v36\n"
    "v_dot2c_f32_bf16 v56, s67, v37\n"
    "v_dot2c_f32_bf16 v58, v37, v37\n"
    "v_dot2c_f32_bf16 v56, s67, v38\n"
    "v_dot2c_f32_bf16 v58, v38, v38\n"
    "v_dot2c_f32_bf16 v56, s67, v39\n"
    "v_dot2c_f32_bf16 v58, v39, v39\n"
    "s_add_u32 m0, s72, 0x1000\n"
    "s_nop 0\n"
    "buffer_load_dwordx4 v8, s[36:39], s97 offen lds\n"
    "buffer_load_dwordx4 v9, s[36:39], s97 offen offset:1024 lds\n"
    "buffer_load_dwordx4 v10, s[36:39], s97 offen offset:2048 lds\n"
    "buffer_load_dwordx4 v11, s[36:39], s97 offen offset:3072 lds\n"
    "s_waitcnt vmcnt(10)\n"
    "v_dot2c_f32_bf16 v60, s67, v40\n"
    "v_dot2c_f32_bf16 v60, s67, v41\n"
    "v_dot2c_f32_bf16 v60, s67, v42\n"
    "v_dot2c_f32_bf16 v60, s67, v43\n"
    "s_waitcnt vmcnt(9)\n"
    "v_dot2c_f32_bf16 v60, s67, v44\n"
    "v_dot2c_f32_bf16 v60, s67, v45\n"
    "v_dot2c_f32_bf16 v60, s67, v46\n"
    "v_dot2c_f32_bf16 v60, s67, v47\n"
    "s_waitcnt vmcnt(8)\n"
    "v_readfirstlane_b32 s66, v4\n"
    "s_nop 1\n"
    "s_or_b32 s66, s66, 0x7fc00000\n"
    "s_add_u32 s66, s66, 0x801779b1\n"
    "s_or_b32 s66, s66, 0x7fc00000\n"
    "v_mov_b32 v4, s66\n"
    "v_add_f32_dpp v56, v56, v56 row_ror:8 row_mask:0xf bank_mask:0xf\n"
    "v_add_f32_dpp v58, v58, v58 row_ror:8 row_mask:0xf bank_mask:0xf\n"
    "v_add_f32_dpp v60, v60, v60 row_ror:8 row_mask:0xf bank_mask:0xf\n"
    "v_add_f32_dpp v56, v56, v56 row_ror:4 row_mask:0xf bank_mask:0xf\n"
    "v_add_f32_dpp v58, v58, v58 row_ror:4 row_mask:0xf bank_mask:0xf\n"
    "v_add_f32_dpp v60, v60, v60 row_ror:4 row_mask:0xf bank_mask:0xf\n"
    "v_add_f32_dpp v56, v56, v56 row_ror:2 row_mask:0xf bank_mask:0xf\n"
    "v_add_f32_dpp v58, v58, v58 row_ror:2 row_mask:0xf bank_mask:0xf\n"
    "v_add_f32_dpp v60, v60, v60 row_ror:2 row_mask:0xf bank_mask:0xf\n"
    "v_add_f32_dpp v56, v56, v56 row_ror:1 row_mask:0xf bank_mask:0xf\n"
    "v_add_f32_dpp v58, v58, v58 row_ror:1 row_mask:0xf bank_mask:0xf\n"
    "v_add_f32_dpp v60, v60, v60 row_ror:1 row_mask:0xf bank_mask:0xf\n"
    "v_mov_b32 v51, v60\n"
    "s_nop 1\n"
    "v_permlane16_swap_b32 v60, v51\n"
    "v_add_f32 v60, v60, v51\n"
    "v_mul_f32 v56, 0x3b000000, v56\n"
    "v_mul_f32 v58, 0x3b000000, v58\n"
    "v_fma_f32 v58, -v56, v56, v58\n"
    "v_max_f32 v58, 0, v58\n"
    "v_add_f32 v58, 0x3727c5ac, v58\n"
    "v_rsq_f32 v58, v58\n"
    "v_mov_b32 v57, s66\n"
    "v_mov_b32 v59, s66\n"
    "v_mov_b32 v61, s66\n"
    "v_cvt_pk_bf16_f32 v51, v60, v60\n"
    "v_and_b32 v51, 0xffff0000, v51\n"
    "v_sub_f32 v51, v60, v51\n"
    "v_cvt_pk_bf16_f32 v60, v60, v51\n"
    "s_lshr_b32 s78, s68, 2\n"
    "s_lshl_b32 s79, s88, 6\n"
    "s_add_u32 s79, s79, 0x4a0000\n"
    "s_lshl_b32 s80, s78, 12\n"
    "s_lshl_b32 s73, s87, 4\n"
    "s_add_u32 s80, s80, s73\n"
    "s_add_u32 s80, s80, 0x4a8000\n"
    "v_lshlrev_b32 v51, 3, v6\n"
    "s_mov_b32 exec_lo, 0x10001\n"
    "s_mov_b32 exec_hi, 0x10001\n"
    "buffer_store_dwordx4 v[56:59], v1, s[52:55], s79 offen sc1\n"
    "s_mov_b32 exec_lo, 1\n"
    "s_mov_b32 exec_hi, 1\n"
    "buffer_store_dwordx2 v[60:61], v51, s[52:55], s80 offen sc1\n"
    "s_mov_b64 exec, -1\n"
    "s_branch Ly2_%=\n"
    "Lx2_%=:\n"
    "v_and_b32 v24, 19, v5\n"
    "v_bfe_u32 v25, v5, 2, 1\n"
    "v_lshl_or_b32 v24, v25, 3, v24\n"
    "v_bfe_u32 v25, v5, 3, 1\n"
    "v_lshl_or_b32 v24, v25, 2, v24\n"
    "v_lshlrev_b32 v220, 4, v24\n"
    "v_lshlrev_b32 v221, 3, v5\n"
    "v_bfe_u32 v25, v24, 1, 3\n"
    "v_xor_b32 v25, v25, v6\n"
    "v_lshlrev_b32 v25, 4, v25\n"
    "v_lshl_or_b32 v24, v24, 7, v25\n"
    "v_or_b32 v24, s86, v24\n"
    "v_xor_b32 v32, 0, v24\n"
    "v_xor_b32 v40, 0, v3\n"
    "v_xor_b32 v33, 32, v24\n"
    "v_xor_b32 v41, 32, v3\n"
    "v_xor_b32 v34, 64, v24\n"
    "v_xor_b32 v42, 64, v3\n"
    "v_xor_b32 v35, 0x60, v24\n"
    "v_xor_b32 v43, 0x60, v3\n"
    "v_xor_b32 v36, 0x10000, v24\n"
    "v_xor_b32 v44, 0x10000, v3\n"
    "v_xor_b32 v37, 0x10020, v24\n"
    "v_xor_b32 v45, 0x10020, v3\n"
    "v_xor_b32 v38, 0x10040, v24\n"
    "v_xor_b32 v46, 0x10040, v3\n"
    "v_xor_b32 v39, 0x10060, v24\n"
    "v_xor_b32 v47, 0x10060, v3\n"
    "s_waitcnt vmcnt(9)\n"
    "s_barrier\n"
    "Lback_%=:\n"
    "ds_read_b128 v[112:115], v32\n"
    "ds_read_b128 v[132:135], v40 offset:16384\n"
    "ds_read_b128 v[136:139], v40 offset:20480\n"
    "ds_read_b128 v[140:143], v40 offset:24576\n"
    "ds_read_b128 v[144:147], v40 offset:28672\n"
    "ds_read_b128 v[116:119], v33\n"
    "ds_read_b128 v[148:151], v41 offset:16384\n"
    "ds_read_b128 v[152:155], v41 offset:20480\n"
    "ds_read_b128 v[156:159], v41 offset:24576\n"
    "ds_read_b128 v[160:163], v41 offset:28672\n"
    "s_waitcnt lgkmcnt(5)\n"
    "v_mfma_f32_32x32x16_bf16 v[48:63], v[112:115], v[132:135], 0\n"
    "ds_read_b128 v[120:123], v34\n"
    "s_xor_b32 s84, s84, 0x180\n"
    "v_mfma_f32_32x32x16_bf16 v[64:79], v[112:115], v[136:139], 0\n"
    "ds_read_b128 v[164:167], v42 offset:16384\n"
    "s_add_u32 m0, s86, 0x10000\n"
    "s_nop 0\n"
    "buffer_load_dwordx4 v28, s[28:31], s84 offen lds\n"
    "v_mfma_f32_32x32x16_bf16 v[80:95], v[112:115], v[140:143], 0\n"
    "ds_read_b128 v[168:171], v42 offset:20480\n"
    "buffer_load_dwordx4 v29, s[28:31], s84 offen offset:1024 lds\n"
    "v_mfma_f32_32x32x16_bf16 v[96:111], v[112:115], v[144:147], 0\n"
    "ds_read_b128 v[172:175], v42 offset:24576\n"
    "ds_read_b128 v[176:179], v42 offset:28672\n"
    "buffer_load_dwordx4 v30, s[28:31], s84 offen offset:2048 lds\n"
    "buffer_load_dwordx4 v31, s[28:31], s84 offen offset:3072 lds\n"
    "s_sleep 1\n"
    "s_waitcnt lgkmcnt(5)\n"
    "v_mfma_f32_32x32x16_bf16 v[48:63], v[116:119], v[148:151], v[48:63]\n"
    "ds_read_b128 v[124:127], v35\n"
    "s_xor_b32 s85, s85, 0x180\n"
    "v_mfma_f32_32x32x16_bf16 v[64:79], v[116:119], v[152:155], v[64:79]\n"
    "ds_read_b128 v[132:135], v43 offset:16384\n"
    "s_add_u32 m0, s86, 0x14000\n"
    "s_nop 0\n"
    "buffer_load_dwordx4 v28, s[32:35], s85 offen lds\n"
    "v_mfma_f32_32x32x16_bf16 v[80:95], v[116:119], v[156:159], v[80:95]\n"
    "ds_read_b128 v[136:139], v43 offset:20480\n"
    "buffer_load_dwordx4 v29, s[32:35], s85 offen offset:1024 lds\n"
    "v_mfma_f32_32x32x16_bf16 v[96:111], v[116:119], v[160:163], v[96:111]\n"
    "ds_read_b128 v[140:143], v43 offset:24576\n"
    "ds_read_b128 v[144:147], v43 offset:28672\n"
    "buffer_load_dwordx4 v30, s[32:35], s85 offen offset:2048 lds\n"
    "buffer_load_dwordx4 v31, s[32:35], s85 offen offset:3072 lds\n"
    "s_sleep 1\n"
    "s_waitcnt vmcnt(8)\n"
    "s_barrier\n"
    "s_waitcnt lgkmcnt(5)\n"
    "v_mfma_f32_32x32x16_bf16 v[48:63], v[120:123], v[164:167], v[48:63]\n"
    "ds_read_b128 v[112:115], v32 offset:32768\n"
    "v_mfma_f32_32x32x16_bf16 v[64:79], v[120:123], v[168:171], v[64:79]\n"
    "ds_read_b128 v[148:151], v40 offset:49152\n"
    "s_xor_b32 s84, s84, 0x80\n"
    "v_mfma_f32_32x32x16_bf16 v[80:95], v[120:123], v[172:175], v[80:95]\n"
    "ds_read_b128 v[152:155], v40 offset:53248\n"
    "v_readfirstlane_b32 s66, v4\n"
    "s_nop 1\n"
    "s_or_b32 s66, s66, 0x7fc00000\n"
    "s_add_u32 s66, s66, 0x801779b1\n"
    "s_or_b32 s66, s66, 0x7fc00000\n"
    "v_mov_b32 v4, s66\n"
    "v_mfma_f32_32x32x16_bf16 v[96:111], v[120:123], v[176:179], v[96:111]\n"
    "ds_read_b128 v[156:159], v40 offset:57344\n"
    "ds_read_b128 v[160:163], v40 offset:61440\n"
    "s_add_u32 m0, s86, 0x18000\n"
    "s_nop 0\n"
    "buffer_load_dwordx4 v28, s[28:31], s84 offen lds\n"
    "s_sleep 1\n"
    "s_waitcnt lgkmcnt(5)\n"
    "v_mfma_f32_32x32x16_bf16 v[48:63], v[124:127], v[132:135], v[48:63]\n"
    "ds_read_b128 v[116:119], v33 offset:32768\n"
    "buffer_load_dwordx4 v29, s[28:31], s84 offen offset:1024 lds\n"
    "v_lshrrev_b32 v7, 4, v1\n"
    "v_mfma_f32_32x32x16_bf16 v[64:79], v[124:127], v[136:139], v[64:79]\n"
    "ds_read_b128 v[164:167], v41 offset:49152\n"
    "v_and_b32 v11, 7, v1\n"
    "v_mfma_f32_32x32x16_bf16 v[80:95], v[124:127], v[140:143], v[80:95]\n"
    "ds_read_b128 v[168:171], v41 offset:53248\n"
    "buffer_load_dwordx4 v30, s[28:31], s84 offen offset:2048 lds\n"
    "v_xor_b32 v7, v7, v11\n"
    "v_mfma_f32_32x32x16_bf16 v[96:111], v[124:127], v[144:147], v[96:111]\n"
    "ds_read_b128 v[172:175], v41 offset:57344\n"
    "ds_read_b128 v[176:179], v41 offset:61440\n"
    "buffer_load_dwordx4 v31, s[28:31], s84 offen offset:3072 lds\n"
    "v_lshlrev_b32 v7, 4, v7\n"
    "s_sleep 1\n"
    "s_waitcnt lgkmcnt(5)\n"
    "v_mfma_f32_32x32x16_bf16 v[48:63], v[112:115], v[148:151], v[48:63]\n"
    "ds_read_b128 v[120:123], v34 offset:32768\n"
    "v_lshrrev_b32 v11, 3, v1\n"
    "v_mfma_f32_32x32x16_bf16 v[64:79], v[112:115], v[152:155], v[64:79]\n"
    "ds_read_b128 v[132:135], v42 offset:49152\n"
    "s_xor_b32 s85, s85, 0x80\n"
    "v_mul_u32_u24 v11, 0x1000, v11\n"
    "v_mfma_f32_32x32x16_bf16 v[80:95], v[112:115], v[156:159], v[80:95]\n"
    "ds_read_b128 v[136:139], v42 offset:53248\n"
    "v_add_u32 v8, v7, v11\n"
    "v_mfma_f32_32x32x16_bf16 v[96:111], v[112:115], v[160:163], v[96:111]\n"
    "ds_read_b128 v[140:143], v42 offset:57344\n"
    "ds_read_b128 v[144:147], v42 offset:61440\n"
    "s_add_u32 m0, s86, 0x1c000\n"
    "s_nop 0\n"
    "buffer_load_dwordx4 v28, s[32:35], s85 offen lds\n"
    "v_xor_b32 v9, 64, v8\n"
    "s_sleep 1\n"
    "s_waitcnt lgkmcnt(5)\n"
    "v_mfma_f32_32x32x16_bf16 v[48:63], v[116:119], v[164:167], v[48:63]\n"
    "ds_read_b128 v[124:127], v35 offset:32768\n"
    "buffer_load_dwordx4 v29, s[32:35], s85 offen offset:1024 lds\n"
    "v_add_u32 v9, 0x7c00, v9\n"
    "v_mfma_f32_32x32x16_bf16 v[64:79], v[116:119], v[168:171], v[64:79]\n"
    "ds_read_b128 v[148:151], v43 offset:49152\n"
    "v_add_u32 v10, 0xf800, v8\n"
    "v_mfma_f32_32x32x16_bf16 v[80:95], v[116:119], v[172:175], v[80:95]\n"
    "ds_read_b128 v[152:155], v43 offset:53248\n"
    "buffer_load_dwordx4 v30, s[32:35], s85 offen offset:2048 lds\n"
    "v_add_u32 v11, 0xf800, v9\n"
    "v_mfma_f32_32x32x16_bf16 v[96:111], v[116:119], v[176:179], v[96:111]\n"
    "ds_read_b128 v[156:159], v43 offset:57344\n"
    "ds_read_b128 v[160:163], v43 offset:61440\n"
    "buffer_load_dwordx4 v31, s[32:35], s85 offen offset:3072 lds\n"
    "s_and_b32 s75, s64, 2\n"
    "s_sleep 1\n"
    "s_waitcnt vmcnt(8)\n"
    "s_barrier\n"
    "s_waitcnt lgkmcnt(5)\n"
    "v_mfma_f32_32x32x16_bf16 v[48:63], v[120:123], v[132:135], v[48:63]\n"
    "ds_read_b128 v[112:115], v36\n"
    "s_and_b32 s74, s63, 1\n"
    "v_mfma_f32_32x32x16_bf16 v[64:79], v[120:123], v[136:139], v[64:79]\n"
    "ds_read_b128 v[164:167], v44 offset:16384\n"
    "s_xor_b32 s84, s84, 0x380\n"
    "s_lshl_b32 s27, s61, 17\n"
    "v_mfma_f32_32x32x16_bf16 v[80:95], v[120:123], v[140:143], v[80:95]\n"
    "ds_read_b128 v[168:171], v44 offset:20480\n"
    "s_lshl_b32 s73, s74, 11\n"
    "v_mfma_f32_32x32x16_bf16 v[96:111], v[120:123], v[144:147], v[96:111]\n"
    "ds_read_b128 v[172:175], v44 offset:24576\n"
    "ds_read_b128 v[176:179], v44 offset:28672\n"
    "s_add_u32 m0, s86, 0\n"
    "s_nop 0\n"
    "buffer_load_dwordx4 v28, s[28:31], s84 offen lds\n"
    "s_add_u32 s27, s27, s73\n"
    "s_sleep 1\n"
    "s_waitcnt lgkmcnt(5)\n"
    "v_mfma_f32_32x32x16_bf16 v[48:63], v[124:127], v[148:151], v[48:63]\n"
    "ds_read_b128 v[116:119], v37\n"
    "buffer_load_dwordx4 v29, s[28:31], s84 offen offset:1024 lds\n"
    "s_lshl_b32 s73, s75, 9\n"
    "v_mfma_f32_32x32x16_bf16 v[64:79], v[124:127], v[152:155], v[64:79]\n"
    "ds_read_b128 v[132:135], v45 offset:16384\n"
    "s_add_u32 s27, s27, s73\n"
    "v_mfma_f32_32x32x16_bf16 v[80:95], v[124:127], v[156:159], v[80:95]\n"
    "ds_read_b128 v[136:139], v45 offset:20480\n"
    "buffer_load_dwordx4 v30, s[28:31], s84 offen offset:2048 lds\n"
    "s_cmp_eq_u32 s61, 0\n"
    "s_cselect_b64 s[24:25], -1, 0\n"
    "v_mfma_f32_32x32x16_bf16 v[96:111], v[124:127], v[160:163], v[96:111]\n"
    "ds_read_b128 v[140:143], v45 offset:24576\n"
    "ds_read_b128 v[144:147], v45 offset:28672\n"
    "buffer_load_dwordx4 v31, s[28:31], s84 offen offset:3072 lds\n"
    "v_or_b32 v7, s72, v3\n"
    "s_sleep 1\n"
    "s_waitcnt lgkmcnt(5)\n"
    "v_mfma_f32_32x32x16_bf16 v[48:63], v[112:115], v[164:167], v[48:63]\n"
    "ds_read_b128 v[120:123], v38\n"
    "v_xor_b32 v12, 0, v7\n"
    "v_mfma_f32_32x32x16_bf16 v[64:79], v[112:115], v[168:171], v[64:79]\n"
    "ds_read_b128 v[148:151], v46 offset:16384\n"
    "s_xor_b32 s85, s85, 0x380\n"
    "v_xor_b32 v13, 32, v7\n"
    "v_mfma_f32_32x32x16_bf16 v[80:95], v[112:115], v[172:175], v[80:95]\n"
    "ds_read_b128 v[152:155], v46 offset:20480\n"
    "v_xor_b32 v14, 64, v7\n"
    "v_mfma_f32_32x32x16_bf16 v[96:111], v[112:115], v[176:179], v[96:111]\n"
    "ds_read_b128 v[156:159], v46 offset:24576\n"
    "ds_read_b128 v[160:163], v46 offset:28672\n"
    "s_add_u32 m0, s86, 0x4000\n"
    "s_nop 0\n"
    "buffer_load_dwordx4 v28, s[32:35], s85 offen lds\n"
    "v_xor_b32 v15, 0x60, v7\n"
    "s_sleep 1\n"
    "s_waitcnt lgkmcnt(5)\n"
    "v_mfma_f32_32x32x16_bf16 v[48:63], v[116:119], v[132:135], v[48:63]\n"
    "ds_read_b128 v[124:127], v39\n"
    "buffer_load_dwordx4 v29, s[32:35], s85 offen offset:1024 lds\n"
    "v_xor_b32 v16, 0x10000, v7\n"
    "v_mfma_f32_32x32x16_bf16 v[64:79], v[116:119], v[136:139], v[64:79]\n"
    "ds_read_b128 v[164:167], v47 offset:16384\n"
    "v_xor_b32 v17, 0x10020, v7\n"
    "v_mfma_f32_32x32x16_bf16 v[80:95], v[116:119], v[140:143], v[80:95]\n"
    "ds_read_b128 v[168:171], v47 offset:20480\n"
    "buffer_load_dwordx4 v30, s[32:35], s85 offen offset:2048 lds\n"
    "v_xor_b32 v18, 0x10040, v7\n"
    "v_mfma_f32_32x32x16_bf16 v[96:111], v[116:119], v[144:147], v[96:111]\n"
    "ds_read_b128 v[172:175], v47 offset:24576\n"
    "ds_read_b128 v[176:179], v47 offset:28672\n"
    "buffer_load_dwordx4 v31, s[32:35], s85 offen offset:3072 lds\n"
    "v_xor_b32 v19, 0x10060, v7\n"
    "s_sleep 1\n"
    "s_waitcnt vmcnt(8)\n"
    "s_barrier\n"
    "s_waitcnt lgkmcnt(5)\n"
    "v_mfma_f32_32x32x16_bf16 v[48:63], v[120:123], v[148:151], v[48:63]\n"
    "ds_read_b128 v[112:115], v36 offset:32768\n"
    "v_xor_b32 v20, 0x20000, v7\n"
    "v_mfma_f32_32x32x16_bf16 v[64:79], v[120:123], v[152:155], v[64:79]\n"
    "ds_read_b128 v[132:135], v44 offset:49152\n"
    "s_xor_b32 s84, s84, 0x80\n"
    "v_xor_b32 v21, 0x20020, v7\n"
    "v_xor_b32 v22, 0x20040, v7\n"
    "v_mfma_f32_32x32x16_bf16 v[80:95], v[120:123], v[156:159], v[80:95]\n"
    "ds_read_b128 v[136:139], v44 offset:53248\n"
    "v_xor_b32 v23, 0x20060, v7\n"
    "v_mfma_f32_32x32x16_bf16 v[96:111], v[120:123], v[160:163], v[96:111]\n"
    "ds_read_b128 v[140:143], v44 offset:57344\n"
    "ds_read_b128 v[144:147], v44 offset:61440\n"
    "s_add_u32 m0, s86, 0x8000\n"
    "s_nop 0\n"
    "buffer_load_dwordx4 v28, s[28:31], s84 offen lds\n"
    "s_lshl_b32 s87, s65, 11\n"
    "s_sleep 1\n"
    "s_waitcnt lgkmcnt(5)\n"
    "v_mfma_f32_32x32x16_bf16 v[48:63], v[124:127], v[164:167], v[48:63]\n"
    "ds_read_b128 v[116:119], v37 offset:32768\n"
    "buffer_load_dwordx4 v29, s[28:31], s84 offen offset:1024 lds\n"
    "s_lshl_b32 s73, s61, 9\n"
    "v_mfma_f32_32x32x16_bf16 v[64:79], v[124:127], v[168:171], v[64:79]\n"
    "ds_read_b128 v[148:151], v45 offset:49152\n"
    "s_add_u32 s87, s87, s73\n"
    "v_mfma_f32_32x32x16_bf16 v[80:95], v[124:127], v[172:175], v[80:95]\n"
    "ds_read_b128 v[152:155], v45 offset:53248\n"
    "buffer_load_dwordx4 v30, s[28:31], s84 offen offset:2048 lds\n"
    "s_add_u32 s87, s87, 0x4a0000\n"
    "v_mfma_f32_32x32x16_bf16 v[96:111], v[124:127], v[176:179], v[96:111]\n"
    "ds_read_b128 v[156:159], v45 offset:57344\n"
    "ds_read_b128 v[160:163], v45 offset:61440\n"
    "buffer_load_dwordx4 v31, s[28:31], s84 offen offset:3072 lds\n"
    "s_lshl_b32 s88, s68, 10\n"
    "s_sleep 1\n"
    "s_waitcnt lgkmcnt(5)\n"
    "v_mfma_f32_32x32x16_bf16 v[48:63], v[112:115], v[132:135], v[48:63]\n"
    "ds_read_b128 v[120:123], v38 offset:32768\n"
    "s_add_u32 s88, s88, 0x4a8000\n"
    "v_mfma_f32_32x32x16_bf16 v[64:79], v[112:115], v[136:139], v[64:79]\n"
    "ds_read_b128 v[164:167], v46 offset:49152\n"
    "s_xor_b32 s85, s85, 0x80\n"
    "v_mov_b32 v186, 0\n"
    "v_mfma_f32_32x32x16_bf16 v[80:95], v[112:115], v[140:143], v[80:95]\n"
    "ds_read_b128 v[168:171], v46 offset:53248\n"
    "v_mov_b32 v187, 0\n"
    "v_mfma_f32_32x32x16_bf16 v[96:111], v[112:115], v[144:147], v[96:111]\n"
    "ds_read_b128 v[172:175], v46 offset:57344\n"
    "ds_read_b128 v[176:179], v46 offset:61440\n"
    "s_add_u32 m0, s86, 0xc000\n"
    "s_nop 0\n"
    "buffer_load_dwordx4 v28, s[32:35], s85 offen lds\n"
    "v_mov_b32 v190, 0\n"
    "s_sleep 1\n"
    "s_waitcnt lgkmcnt(5)\n"
    "v_mfma_f32_32x32x16_bf16 v[48:63], v[116:119], v[148:151], v[48:63]\n"
    "ds_read_b128 v[124:127], v39 offset:32768\n"
    "buffer_load_dwordx4 v29, s[32:35], s85 offen offset:1024 lds\n"
    "v_mov_b32 v191, 0\n"
    "v_mfma_f32_32x32x16_bf16 v[64:79], v[116:119], v[152:155], v[64:79]\n"
    "ds_read_b128 v[132:135], v47 offset:49152\n"
    "v_mov_b32 v194, 0\n"
    "v_mfma_f32_32x32x16_bf16 v[80:95], v[116:119], v[156:159], v[80:95]\n"
    "ds_read_b128 v[136:139], v47 offset:53248\n"
    "buffer_load_dwordx4 v30, s[32:35], s85 offen offset:2048 lds\n"
    "v_mov_b32 v195, 0\n"
    "v_mfma_f32_32x32x16_bf16 v[96:111], v[116:119], v[160:163], v[96:111]\n"
    "ds_read_b128 v[140:143], v47 offset:57344\n"
    "ds_read_b128 v[144:147], v47 offset:61440\n"
    "buffer_load_dwordx4 v31, s[32:35], s85 offen offset:3072 lds\n"
    "v_mov_b32 v198, 0\n"
    "s_sleep 1\n"
    "s_waitcnt vmcnt(8)\n"
    "s_barrier\n"
    "s_waitcnt lgkmcnt(5)\n"
    "v_mfma_f32_32x32x16_bf16 v[48:63], v[120:123], v[164:167], v[48:63]\n"
    "ds_read_b128 v[112:115], v32\n"
    "v_mov_b32 v199, 0\n"
    "v_mfma_f32_32x32x16_bf16 v[64:79], v[120:123], v[168:171], v[64:79]\n"
    "ds_read_b128 v[148:151], v40 offset:16384\n"
    "s_xor_b32 s84, s84, 0x180\n"
    "v_mov_b32 v202, 0\n"
    "v_mfma_f32_32x32x16_bf16 v[80:95], v[120:123], v[172:175], v[80:95]\n"
    "ds_read_b128 v[152:155], v40 offset:20480\n"
    "v_mov_b32 v203, 0\n"
    "v_mfma_f32_32x32x16_bf16 v[96:111], v[120:123], v[176:179], v[96:111]\n"
    "ds_read_b128 v[156:159], v40 offset:24576\n"
    "ds_read_b128 v[160:163], v40 offset:28672\n"
    "s_add_u32 m0, s86, 0x10000\n"
    "s_nop 0\n"
    "buffer_load_dwordx4 v28, s[28:31], s84 offen lds\n"
    "s_lshl_b32 s89, s68, 19\n"
    "s_sleep 1\n"
    "s_waitcnt lgkmcnt(5)\n"
    "v_mfma_f32_32x32x16_bf16 v[48:63], v[124:127], v[132:135], v[48:63]\n"
    "ds_read_b128 v[116:119], v33\n"
    "buffer_load_dwordx4 v29, s[28:31], s84 offen offset:1024 lds\n"
    "s_lshl_b32 s73, s65, 13\n"
    "v_mfma_f32_32x32x16_bf16 v[64:79], v[124:127], v[136:139], v[64:79]\n"
    "ds_read_b128 v[164:167], v41 offset:16384\n"
    "s_add_u32 s89, s89, s73\n"
    "v_mfma_f32_32x32x16_bf16 v[80:95], v[124:127], v[140:143], v[80:95]\n"
    "ds_read_b128 v[168:171], v41 offset:20480\n"
    "buffer_load_dwordx4 v30, s[28:31], s84 offen offset:2048 lds\n"
    "s_lshl_b32 s73, s61, 11\n"
    "v_mfma_f32_32x32x16_bf16 v[96:111], v[124:127], v[144:147], v[96:111]\n"
    "ds_read_b128 v[172:175], v41 offset:24576\n"
    "ds_read_b128 v[176:179], v41 offset:28672\n"
    "buffer_load_dwordx4 v31, s[28:31], s84 offen offset:3072 lds\n"
    "s_add_u32 s89, s89, s73\n"
    "s_sleep 1\n"
    "s_waitcnt lgkmcnt(5)\n"
    "v_mfma_f32_32x32x16_bf16 v[48:63], v[112:115], v[148:151], v[48:63]\n"
    "ds_read_b128 v[120:123], v34\n"
    "s_lshl_b32 s83, s68, 10\n"
    "v_mfma_f32_32x32x16_bf16 v[64:79], v[112:115], v[152:155], v[64:79]\n"
    "ds_read_b128 v[132:135], v42 offset:16384\n"
    "s_xor_b32 s85, s85, 0x180\n"
    "s_lshl_b32 s73, s65, 6\n"
    "v_mfma_f32_32x32x16_bf16 v[80:95], v[112:115], v[156:159], v[80:95]\n"
    "ds_read_b128 v[136:139], v42 offset:20480\n"
    "s_add_u32 s83, s83, s73\n"
    "v_mfma_f32_32x32x16_bf16 v[96:111], v[112:115], v[160:163], v[96:111]\n"
    "ds_read_b128 v[140:143], v42 offset:24576\n"
    "ds_read_b128 v[144:147], v42 offset:28672\n"
    "s_add_u32 m0, s86, 0x14000\n"
    "s_nop 0\n"
    "buffer_load_dwordx4 v28, s[32:35], s85 offen lds\n"
    "s_add_u32 s83, s83, 0x488000\n"
    "s_waitcnt lgkmcnt(5)\n"
    "v_mfma_f32_32x32x16_bf16 v[48:63], v[116:119], v[164:167], v[48:63]\n"
    "ds_read_b128 v[124:127], v35\n"
    "buffer_load_dwordx4 v29, s[32:35], s85 offen offset:1024 lds\n"
    "s_lshr_b32 s75, s68, 2\n"
    "v_mfma_f32_32x32x16_bf16 v[64:79], v[116:119], v[168:171], v[64:79]\n"
    "ds_read_b128 v[148:151], v43 offset:16384\n"
    "s_lshl_b32 s75, s75, 31\n"
    "v_mfma_f32_32x32x16_bf16 v[80:95], v[116:119], v[172:175], v[80:95]\n"
    "ds_read_b128 v[152:155], v43 offset:20480\n"
    "buffer_load_dwordx4 v30, s[32:35], s85 offen offset:2048 lds\n"
    "s_add_u32 s74, s72, 0x20000\n"
    "v_mfma_f32_32x32x16_bf16 v[96:111], v[116:119], v[176:179], v[96:111]\n"
    "ds_read_b128 v[156:159], v43 offset:24576\n"
    "ds_read_b128 v[160:163], v43 offset:28672\n"
    "buffer_load_dwordx4 v31, s[32:35], s85 offen offset:3072 lds\n"
    "v_lshl_add_u32 v25, v5, 2, s74\n"
    "v_lshl_add_u32 v26, v6, 4, s74\n"
    "s_waitcnt vmcnt(8)\n"
    "s_barrier\n"
    "s_waitcnt lgkmcnt(5)\n"
    "v_mfma_f32_32x32x16_bf16 v[48:63], v[120:123], v[132:135], v[48:63]\n"
    "ds_read_b128 v[112:115], v32 offset:32768\n"
    "v_mfma_f32_32x32x16_bf16 v[64:79], v[120:123], v[136:139], v[64:79]\n"
    "ds_read_b128 v[164:167], v40 offset:49152\n"
    "s_xor_b32 s84, s84, 0x80\n"
    "v_mfma_f32_32x32x16_bf16 v[80:95], v[120:123], v[140:143], v[80:95]\n"
    "ds_read_b128 v[168:171], v40 offset:53248\n"
    "v_mfma_f32_32x32x16_bf16 v[96:111], v[120:123], v[144:147], v[96:111]\n"
    "ds_read_b128 v[172:175], v40 offset:57344\n"
    "ds_read_b128 v[176:179], v40 offset:61440\n"
    "s_add_u32 m0, s86, 0x18000\n"
    "s_nop 0\n"
    "buffer_load_dwordx4 v28, s[28:31], s84 offen lds\n"
    "s_waitcnt lgkmcnt(5)\n"
    "v_mfma_f32_32x32x16_bf16 v[48:63], v[124:127], v[148:151], v[48:63]\n"
    "ds_read_b128 v[116:119], v33 offset:32768\n"
    "buffer_load_dwordx4 v29, s[28:31], s84 offen offset:1024 lds\n"
    "v_mfma_f32_32x32x16_bf16 v[64:79], v[124:127], v[152:155], v[64:79]\n"
    "ds_read_b128 v[132:135], v41 offset:49152\n"
    "v_mfma_f32_32x32x16_bf16 v[80:95], v[124:127], v[156:159], v[80:95]\n"
    "ds_read_b128 v[136:139], v41 offset:53248\n"
    "buffer_load_dwordx4 v30, s[28:31], s84 offen offset:2048 lds\n"
    "v_mfma_f32_32x32x16_bf16 v[96:111], v[124:127], v[160:163], v[96:111]\n"
    "ds_read_b128 v[140:143], v41 offset:57344\n"
    "ds_read_b128 v[144:147], v41 offset:61440\n"
    "buffer_load_dwordx4 v31, s[28:31], s84 offen offset:3072 lds\n"
    "s_waitcnt lgkmcnt(5)\n"
    "v_mfma_f32_32x32x16_bf16 v[48:63], v[112:115], v[164:167], v[48:63]\n"
    "ds_read_b128 v[120:123], v34 offset:32768\n"
    "v_mfma_f32_32x32x16_bf16 v[64:79], v[112:115], v[168:171], v[64:79]\n"
    "ds_read_b128 v[148:151], v42 offset:49152\n"
    "s_xor_b32 s85, s85, 0x80\n"
    "v_mfma_f32_32x32x16_bf16 v[80:95], v[112:115], v[172:175], v[80:95]\n"
    "ds_read_b128 v[152:155], v42 offset:53248\n"
    "v_mfma_f32_32x32x16_bf16 v[96:111], v[112:115], v[176:179], v[96:111]\n"
    "ds_read_b128 v[156:159], v42 offset:57344\n"
    "ds_read_b128 v[160:163], v42 offset:61440\n"
    "s_add_u32 m0, s86, 0x1c000\n"
    "s_nop 0\n"
    "buffer_load_dwordx4 v28, s[32:35], s85 offen lds\n"
    "s_waitcnt lgkmcnt(5)\n"
    "v_mfma_f32_32x32x16_bf16 v[48:63], v[116:119], v[132:135], v[48:63]\n"
    "ds_read_b128 v[124:127], v35 offset:32768\n"
    "buffer_load_dwordx4 v29, s[32:35], s85 offen offset:1024 lds\n"
    "v_mfma_f32_32x32x16_bf16 v[64:79], v[116:119], v[136:139], v[64:79]\n"
    "ds_read_b128 v[164:167], v43 offset:49152\n"
    "v_mfma_f32_32x32x16_bf16 v[80:95], v[116:119], v[140:143], v[80:95]\n"
    "ds_read_b128 v[168:171], v43 offset:53248\n"
    "buffer_load_dwordx4 v30, s[32:35], s85 offen offset:2048 lds\n"
    "v_mfma_f32_32x32x16_bf16 v[96:111], v[116:119], v[144:147], v[96:111]\n"
    "ds_read_b128 v[172:175], v43 offset:57344\n"
    "ds_read_b128 v[176:179], v43 offset:61440\n"
    "buffer_load_dwordx4 v31, s[32:35], s85 offen offset:3072 lds\n"
    "s_waitcnt vmcnt(8)\n"
    "s_barrier\n"
    "s_waitcnt lgkmcnt(5)\n"
    "v_mfma_f32_32x32x16_bf16 v[48:63], v[120:123], v[148:151], v[48:63]\n"
    "ds_read_b128 v[112:115], v36\n"
    "v_mfma_f32_32x32x16_bf16 v[64:79], v[120:123], v[152:155], v[64:79]\n"
    "ds_read_b128 v[132:135], v44 offset:16384\n"
    "buffer_load_dwordx4 v[180:183], v220, s[52:55], s87 offen sc1\n"
    "v_mfma_f32_32x32x16_bf16 v[80:95], v[120:123], v[156:159], v[80:95]\n"
    "ds_read_b128 v[136:139], v44 offset:20480\n"
    "v_mfma_f32_32x32x16_bf16 v[96:111], v[120:123], v[160:163], v[96:111]\n"
    "ds_read_b128 v[140:143], v44 offset:24576\n"
    "ds_read_b128 v[144:147], v44 offset:28672\n"
    "buffer_load_dwordx2 v[184:185], v221, s[52:55], s88 offen sc1\n"
    "s_waitcnt lgkmcnt(5)\n"
    "v_mfma_f32_32x32x16_bf16 v[48:63], v[124:127], v[164:167], v[48:63]\n"
    "ds_read_b128 v[116:119], v37\n"
    "buffer_load_dwordx2 v[188:189], v221, s[52:55], s88 offen offset:256 sc1\n"
    "v_mfma_f32_32x32x16_bf16 v[64:79], v[124:127], v[168:171], v[64:79]\n"
    "ds_read_b128 v[148:151], v45 offset:16384\n"
    "v_mfma_f32_32x32x16_bf16 v[80:95], v[124:127], v[172:175], v[80:95]\n"
    "ds_read_b128 v[152:155], v45 offset:20480\n"
    "buffer_load_dwordx2 v[192:193], v221, s[52:55], s88 offen offset:512 sc1\n"
    "v_mfma_f32_32x32x16_bf16 v[96:111], v[124:127], v[176:179], v[96:111]\n"
    "ds_read_b128 v[156:159], v45 offset:24576\n"
    "ds_read_b128 v[160:163], v45 offset:28672\n"
    "buffer_load_dwordx2 v[196:197], v221, s[52:55], s88 offen offset:768 sc1\n"
    "s_waitcnt lgkmcnt(5)\n"
    "v_mfma_f32_32x32x16_bf16 v[48:63], v[112:115], v[132:135], v[48:63]\n"
    "ds_read_b128 v[120:123], v38\n"
    "v_mfma_f32_32x32x16_bf16 v[64:79], v[112:115], v[136:139], v[64:79]\n"
    "ds_read_b128 v[164:167], v46 offset:16384\n"
    "s_xor_b32 s96, s27, 0x300\n"
    "s_add_u32 s97, s96, 0x80\n"
    "v_mfma_f32_32x32x16_bf16 v[80:95], v[112:115], v[140:143], v[80:95]\n"
    "ds_read_b128 v[168:171], v46 offset:20480\n"
    "v_mfma_f32_32x32x16_bf16 v[96:111], v[112:115], v[144:147], v[96:111]\n"
    "ds_read_b128 v[172:175], v46 offset:24576\n"
    "ds_read_b128 v[176:179], v46 offset:28672\n"
    "s_add_u32 m0, s72, 0\n"
    "s_nop 0\n"
    "buffer_load_dwordx4 v8, s[36:39], s96 offen lds\n"
    "s_waitcnt lgkmcnt(5)\n"
    "v_mfma_f32_32x32x16_bf16 v[48:63], v[116:119], v[148:151], v[48:63]\n"
    "ds_read_b128 v[124:127], v39\n"
    "v_mfma_f32_32x32x16_bf16 v[64:79], v[116:119], v[152:155], v[64:79]\n"
    "ds_read_b128 v[132:135], v47 offset:16384\n"
    "buffer_load_dwordx4 v9, s[36:39], s96 offen offset:1024 lds\n"
    "v_mfma_f32_32x32x16_bf16 v[80:95], v[116:119], v[156:159], v[80:95]\n"
    "ds_read_b128 v[136:139], v47 offset:20480\n"
    "v_mfma_f32_32x32x16_bf16 v[96:111], v[116:119], v[160:163], v[96:111]\n"
    "ds_read_b128 v[140:143], v47 offset:24576\n"
    "ds_read_b128 v[144:147], v47 offset:28672\n"
    "buffer_load_dwordx4 v10, s[36:39], s96 offen offset:2048 lds\n"
    "s_waitcnt vmcnt(8)\n"
    "s_barrier\n"
    "s_waitcnt lgkmcnt(5)\n"
    "v_mfma_f32_32x32x16_bf16 v[48:63], v[120:123], v[164:167], v[48:63]\n"
    "ds_read_b128 v[112:115], v36 offset:32768\n"
    "buffer_load_dwordx4 v11, s[36:39], s96 offen offset:3072 lds\n"
    "v_mfma_f32_32x32x16_bf16 v[64:79], v[120:123], v[168:171], v[64:79]\n"
    "ds_read_b128 v[116:119], v37 offset:32768\n"
    "ds_read_b128 v[148:151], v44 offset:49152\n"
    "v_mfma_f32_32x32x16_bf16 v[80:95], v[120:123], v[172:175], v[80:95]\n"
    "ds_read_b128 v[152:155], v45 offset:49152\n"
    "s_waitcnt vmcnt(8)\n"
    "v_xor_b32 v24, s66, v181\n"
    "v_bitop3_b32 v24, v183, s66, v24 bitop3:0xbe\n"
    "s_waitcnt vmcnt(7)\n"
    "v_bitop3_b32 v24, v185, s66, v24 bitop3:0xbe\n"
    "s_waitcnt vmcnt(6)\n"
    "v_bitop3_b32 v24, v189, s66, v24 bitop3:0xbe\n"
    "s_waitcnt vmcnt(5)\n"
    "v_bitop3_b32 v24, v193, s66, v24 bitop3:0xbe\n"
    "s_waitcnt vmcnt(4)\n"
    "v_bitop3_b32 v24, v197, s66, v24 bitop3:0xbe\n"
    "v_cmp_ne_u32 vcc, 0, v24\n"
    "s_cbranch_vccnz Lslow3_%=\n"
    "Lback4_%=:\n"
    "v_mfma_f32_32x32x16_bf16 v[96:111], v[120:123], v[176:179], v[96:111]\n"
    "ds_read_b128 v[156:159], v46 offset:49152\n"
    "ds_read_b128 v[160:163], v47 offset:49152\n"
    "v_xor_b32 v182, s75, v182\n"
    "ds_write_b32 v25, v182\n"
    "s_waitcnt lgkmcnt(7)\n"
    "v_mfma_f32_32x32x16_bf16 v[48:63], v[124:127], v[132:135], v[48:63]\n"
    "ds_read_b128 v[120:123], v38 offset:32768\n"
    "v_cvt_pk_bf16_f32 v200, -v180, -v180\n"
    "v_and_b32 v24, 0xffff0000, v200\n"
    "v_sub_f32 v24, -v180, v24\n"
    "v_cvt_pk_bf16_f32 v201, v24, v24\n"
    "v_mfma_f32_32x32x16_bf16 v[64:79], v[124:127], v[136:139], v[64:79]\n"
    "ds_read_b128 v[128:131], v39 offset:32768\n"
    "ds_read_b128 v[164:167], v44 offset:53248\n"
    "v_cmp_gt_u32 vcc, 32, v1\n"
    "s_nop 1\n"
    "v_cndmask_b32 v200, 0, v200, vcc\n"
    "v_cndmask_b32 v201, 0, v201, vcc\n"
    "v_mfma_f32_32x32x16_bf16 v[80:95], v[124:127], v[140:143], v[80:95]\n"
    "ds_read_b128 v[168:171], v45 offset:53248\n"
    "ds_read_b128 v[204:207], v26\n"
    "v_mfma_f32_32x32x16_bf16 v[96:111], v[124:127], v[144:147], v[96:111]\n"
    "ds_read_b128 v[172:175], v46 offset:53248\n"
    "ds_read_b128 v[176:179], v47 offset:53248\n"
    "ds_read_b128 v[208:211], v26 offset:32\n"
    "ds_read_b128 v[212:215], v26 offset:64\n"
    "s_waitcnt lgkmcnt(10)\n"
    "v_mfma_f32_32x32x16_bf16 v[48:63], v[112:115], v[148:151], v[48:63]\n"
    "ds_read_b128 v[132:135], v44 offset:57344\n"
    "ds_read_b128 v[216:219], v26 offset:96\n"
    "v_mfma_f32_32x32x16_bf16 v[48:63], v[116:119], v[152:155], v[48:63]\n"
    "ds_read_b128 v[136:139], v45 offset:57344\n"
    "v_mov_b32 v185, v184\n"
    "v_mov_b32 v189, v188\n"
    "s_waitcnt lgkmcnt(11)\n"
    "v_mfma_f32_32x32x16_bf16 v[48:63], v[120:123], v[156:159], v[48:63]\n"
    "ds_read_b128 v[140:143], v46 offset:57344\n"
    "v_mov_b32 v193, v192\n"
    "v_mov_b32 v197, v196\n"
    "s_waitcnt lgkmcnt(11)\n"
    "v_mfma_f32_32x32x16_bf16 v[48:63], v[128:131], v[160:163], v[48:63]\n"
    "ds_read_b128 v[144:147], v47 offset:57344\n"
    "v_mfma_f32_32x32x16_bf16 v[48:63], v[200:203], v[184:187], v[48:63]\n"
    "s_waitcnt lgkmcnt(7)\n"
    "v_mfma_f32_32x32x16_bf16 v[64:79], v[112:115], v[164:167], v[64:79]\n"
    "ds_read_b128 v[148:151], v44 offset:61440\n"
    "v_mfma_f32_32x32x16_bf16 v[64:79], v[116:119], v[168:171], v[64:79]\n"
    "ds_read_b128 v[152:155], v45 offset:61440\n"
    "s_nop 6\n"
    "v_mul_f32 v48, v48, v204\n"
    "v_mul_f32 v49, v49, v205\n"
    "v_mul_f32 v50, v50, v206\n"
    "v_mul_f32 v51, v51, v207\n"
    "s_waitcnt lgkmcnt(8)\n"
    "v_mul_f32 v52, v52, v208\n"
    "v_mfma_f32_32x32x16_bf16 v[64:79], v[120:123], v[172:175], v[64:79]\n"
    "ds_read_b128 v[156:159], v46 offset:61440\n"
    "v_mul_f32 v53, v53, v209\n"
    "v_mul_f32 v54, v54, v210\n"
    "v_mul_f32 v55, v55, v211\n"
    "v_cvt_pk_bf16_f32 v222, v48, v49\n"
    "v_cvt_pk_bf16_f32 v223, v50, v51\n"
    "v_mfma_f32_32x32x16_bf16 v[64:79], v[128:131], v[176:179], v[64:79]\n"
    "ds_read_b128 v[160:163], v47 offset:61440\n"
    "v_cvt_pk_bf16_f32 v224, v52, v53\n"
    "v_cvt_pk_bf16_f32 v225, v54, v55\n"
    "buffer_store_dwordx4 v[222:225], v2, s[52:55], s89 offen sc1\n"
    "s_waitcnt lgkmcnt(9)\n"
    "v_mul_f32 v56, v56, v212\n"
    "v_mul_f32 v57, v57, v213\n"
    "v_mfma_f32_32x32x16_bf16 v[64:79], v[200:203], v[188:191], v[64:79]\n"
    "v_mul_f32 v58, v58, v214\n"
    "v_mul_f32 v59, v59, v215\n"
    "s_waitcnt lgkmcnt(7)\n"
    "v_mul_f32 v60, v60, v216\n"
    "v_mul_f32 v61, v61, v217\n"
    "v_mul_f32 v62, v62, v218\n"
    "s_waitcnt lgkmcnt(4)\n"
    "v_mfma_f32_32x32x16_bf16 v[80:95], v[112:115], v[132:135], v[80:95]\n"
    "v_mul_f32 v63, v63, v219\n"
    "v_cvt_pk_bf16_f32 v226, v56, v57\n"
    "v_cvt_pk_bf16_f32 v227, v58, v59\n"
    "v_cvt_pk_bf16_f32 v228, v60, v61\n"
    "v_cvt_pk_bf16_f32 v229, v62, v63\n"
    "buffer_store_dwordx4 v[226:229], v2, s[52:55], s89 offen offset:1024 sc1\n"
    "s_add_u32 s89, s89, 0x20000\n"
    "v_mfma_f32_32x32x16_bf16 v[80:95], v[116:119], v[136:139], v[80:95]\n"
    "v_mul_f32 v64, v64, v204\n"
    "v_mul_f32 v65, v65, v205\n"
    "v_mul_f32 v66, v66, v206\n"
    "v_mul_f32 v67, v67, v207\n"
    "v_mul_f32 v68, v68, v208\n"
    "v_mfma_f32_32x32x16_bf16 v[80:95], v[120:123], v[140:143], v[80:95]\n"
    "v_mul_f32 v69, v69, v209\n"
    "v_mul_f32 v70, v70, v210\n"
    "v_mul_f32 v71, v71, v211\n"
    "v_cvt_pk_bf16_f32 v230, v64, v65\n"
    "v_cvt_pk_bf16_f32 v231, v66, v67\n"
    "v_mfma_f32_32x32x16_bf16 v[80:95], v[128:131], v[144:147], v[80:95]\n"
    "v_cvt_pk_bf16_f32 v232, v68, v69\n"
    "v_cvt_pk_bf16_f32 v233, v70, v71\n"
    "buffer_store_dwordx4 v[230:233], v2, s[52:55], s89 offen sc1\n"
    "v_mul_f32 v72, v72, v212\n"
    "v_mul_f32 v73, v73, v213\n"
    "v_mfma_f32_32x32x16_bf16 v[80:95], v[200:203], v[192:195], v[80:95]\n"
    "v_mul_f32 v74, v74, v214\n"
    "v_mul_f32 v75, v75, v215\n"
    "v_mul_f32 v76, v76, v216\n"
    "v_mul_f32 v77, v77, v217\n"
    "v_mul_f32 v78, v78, v218\n"
    "s_waitcnt lgkmcnt(0)\n"
    "v_mfma_f32_32x32x16_bf16 v[96:111], v[112:115], v[148:151], v[96:111]\n"
    "v_mul_f32 v79, v79, v219\n"
    "v_cvt_pk_bf16_f32 v234, v72, v73\n"
    "v_cvt_pk_bf16_f32 v235, v74, v75\n"
    "v_cvt_pk_bf16_f32 v236, v76, v77\n"
    "v_cvt_pk_bf16_f32 v237, v78, v79\n"
    "buffer_store_dwordx4 v[234:237], v2, s[52:55], s89 offen offset:1024 sc1\n"
    "s_add_u32 s89, s89, 0x20000\n"
    "v_mfma_f32_32x32x16_bf16 v[96:111], v[116:119], v[152:155], v[96:111]\n"
    "v_mul_f32 v80, v80, v204\n"
    "v_mul_f32 v81, v81, v205\n"
    "v_mul_f32 v82, v82, v206\n"
    "v_mul_f32 v83, v83, v207\n"
    "v_mfma_f32_32x32x16_bf16 v[96:111], v[120:123], v[156:159], v[96:111]\n"
    "v_mul_f32 v84, v84, v208\n"
    "v_mul_f32 v85, v85, v209\n"
    "v_mul_f32 v86, v86, v210\n"
    "v_mul_f32 v87, v87, v211\n"
    "v_cvt_pk_bf16_f32 v222, v80, v81\n"
    "v_mfma_f32_32x32x16_bf16 v[96:111], v[128:131], v[160:163], v[96:111]\n"
    "v_cvt_pk_bf16_f32 v223, v82, v83\n"
    "v_cvt_pk_bf16_f32 v224, v84, v85\n"
    "v_cvt_pk_bf16_f32 v225, v86, v87\n"
    "buffer_store_dwordx4 v[222:225], v2, s[52:55], s89 offen sc1\n"
    "v_mfma_f32_32x32x16_bf16 v[96:111], v[200:203], v[196:199], v[96:111]\n"
    "v_mul_f32 v88, v88, v212\n"
    "v_mul_f32 v89, v89, v213\n"
    "v_mul_f32 v90, v90, v214\n"
    "v_mul_f32 v91, v91, v215\n"
    "v_mul_f32 v92, v92, v216\n"
    "v_mul_f32 v93, v93, v217\n"
    "v_mul_f32 v94, v94, v218\n"
    "v_mul_f32 v95, v95, v219\n"
    "v_cvt_pk_bf16_f32 v226, v88, v89\n"
    "v_cvt_pk_bf16_f32 v227, v90, v91\n"
    "v_cvt_pk_bf16_f32 v228, v92, v93\n"
    "v_cvt_pk_bf16_f32 v229, v94, v95\n"
    "buffer_store_dwordx4 v[226:229], v2, s[52:55], s89 offen offset:1024 sc1\n"
    "s_add_u32 s89, s89, 0x20000\n"
    "v_pk_mul_f32 v[96:97], v[96:97], v[204:205]\n"
    "v_pk_mul_f32 v[98:99], v[98:99], v[206:207]\n"
    "v_pk_mul_f32 v[100:101], v[100:101], v[208:209]\n"
    "v_pk_mul_f32 v[102:103], v[102:103], v[210:211]\n"
    "v_cvt_pk_bf16_f32 v230, v96, v97\n"
    "v_cvt_pk_bf16_f32 v231, v98, v99\n"
    "v_cvt_pk_bf16_f32 v232, v100, v101\n"
    "v_cvt_pk_bf16_f32 v233, v102, v103\n"
    "buffer_store_dwordx4 v[230:233], v2, s[52:55], s89 offen sc1\n"
    "v_pk_mul_f32 v[104:105], v[104:105], v[212:213]\n"
    "v_pk_mul_f32 v[106:107], v[106:107], v[214:215]\n"
    "v_pk_mul_f32 v[108:109], v[108:109], v[216:217]\n"
    "v_pk_mul_f32 v[110:111], v[110:111], v[218:219]\n"
    "v_cvt_pk_bf16_f32 v234, v104, v105\n"
    "v_cvt_pk_bf16_f32 v235, v106, v107\n"
    "v_cvt_pk_bf16_f32 v236, v108, v109\n"
    "v_cvt_pk_bf16_f32 v237, v110, v111\n"
    "buffer_store_dwordx4 v[234:237], v2, s[52:55], s89 offen offset:1024 sc1\n"
    "s_add_u32 s89, s89, 0x20000\n"
    "s_barrier\n"
    "s_add_u32 m0, s72, 0x1000\n"
    "s_nop 0\n"
    "buffer_load_dwordx4 v8, s[36:39], s97 offen lds\n"
    "buffer_load_dwordx4 v9, s[36:39], s97 offen offset:1024 lds\n"
    "buffer_load_dwordx4 v10, s[36:39], s97 offen offset:2048 lds\n"
    "buffer_load_dwordx4 v11, s[36:39], s97 offen offset:3072 lds\n"
    "s_xor_b32 s96, s27, 0\n"
    "s_add_u32 s97, s96, 0x80\n"
    "s_add_u32 m0, s72, 0x18000\n"
    "s_nop 0\n"
    "buffer_load_dwordx4 v8, s[36:39], s96 offen lds\n"
    "buffer_load_dwordx4 v9, s[36:39], s96 offen offset:1024 lds\n"
    "buffer_load_dwordx4 v10, s[36:39], s96 offen offset:2048 lds\n"
    "buffer_load_dwordx4 v11, s[36:39], s96 offen offset:3072 lds\n"
    "s_waitcnt vmcnt(8)\n"
    "s_barrier\n"
    "v_lshlrev_b32 v24, 2, v5\n"
    "s_and_b32 exec_lo, s24, 0xffff\n"
    "s_mov_b32 exec_hi, 0\n"
    "buffer_store_dword v4, v24, s[52:55], s83 offen sc1\n"
    "s_mov_b64 exec, -1\n"
    "s_add_u32 m0, s72, 0x19000\n"
    "s_nop 0\n"
    "buffer_load_dwordx4 v8, s[36:39], s97 offen lds\n"
    "buffer_load_dwordx4 v9, s[36:39], s97 offen offset:1024 lds\n"
    "buffer_load_dwordx4 v10, s[36:39], s97 offen offset:2048 lds\n"
    "buffer_load_dwordx4 v11, s[36:39], s97 offen offset:3072 lds\n"
    "s_xor_b32 s96, s27, 0x100\n"
    "s_add_u32 s97, s96, 0x80\n"
    "s_add_u32 m0, s72, 0x20000\n"
    "s_nop 0\n"
    "buffer_load_dwordx4 v8, s[36:39], s96 offen lds\n"
    "buffer_load_dwordx4 v9, s[36:39], s96 offen offset:1024 lds\n"
    "buffer_load_dwordx4 v10, s[36:39], s96 offen offset:2048 lds\n"
    "buffer_load_dwordx4 v11, s[36:39], s96 offen offset:3072 lds\n"
    "s_add_u32 m0, s72, 0x21000\n"
    "s_nop 0\n"
    "buffer_load_dwordx4 v8, s[36:39], s97 offen lds\n"
    "buffer_load_dwordx4 v9, s[36:39], s97 offen offset:1024 lds\n"
    "buffer_load_dwordx4 v10, s[36:39], s97 offen offset:2048 lds\n"
    "buffer_load_dwordx4 v11, s[36:39], s97 offen offset:3072 lds\n"
    "s_waitcnt vmcnt(8)\n"
    "ds_read_b128 a[0:3], v16 offset:32768\n"
    "ds_read_b128 a[4:7], v17 offset:32768\n"
    "ds_read_b128 a[8:11], v18 offset:32768\n"
    "ds_read_b128 a[12:15], v19 offset:32768\n"
    "ds_read_b128 a[16:19], v16 offset:36864\n"
    "ds_read_b128 a[20:23], v17 offset:36864\n"
    "ds_read_b128 a[24:27], v18 offset:36864\n"
    "ds_read_b128 a[28:31], v19 offset:36864\n"
    "s_xor_b32 s96, s27, 0x400\n"
    "s_add_u32 s97, s96, 0x80\n"
    "s_add_u32 m0, s72, 0x10000\n"
    "s_nop 0\n"
    "buffer_load_dwordx4 v8, s[36:39], s96 offen lds\n"
    "buffer_load_dwordx4 v9, s[36:39], s96 offen offset:1024 lds\n"
    "buffer_load_dwordx4 v10, s[36:39], s96 offen offset:2048 lds\n"
    "buffer_load_dwordx4 v11, s[36:39], s96 offen offset:3072 lds\n"
    "s_add_u32 m0, s72, 0x11000\n"
    "s_nop 0\n"
    "buffer_load_dwordx4 v8, s[36:39], s97 offen lds\n"
    "buffer_load_dwordx4 v9, s[36:39], s97 offen offset:1024 lds\n"
    "buffer_load_dwordx4 v10, s[36:39], s97 offen offset:2048 lds\n"
    "buffer_load_dwordx4 v11, s[36:39], s97 offen offset:3072 lds\n"
    "s_waitcnt lgkmcnt(0)\n"
    "v_add_u32 v172, 0x10000, v2\n"
    "v_add_u32 v173, 0x20000, v2\n"
    "s_and_b32 s76, s64, 2\n"
    "s_lshr_b32 s74, s63, 1\n"
    "s_and_b32 s75, s63, 1\n"
    "s_lshl_b32 s8, s74, 21\n"
    "s_lshl_b32 s73, s64, 19\n"
    "s_add_u32 s8, s8, s73\n"
    "s_lshl_b32 s73, s63, 17\n"
    "s_add_u32 s8, s8, s73\n"
    "s_lshl_b32 s73, s75, 16\n"
    "s_add_u32 s8, s8, s73\n"
    "s_lshl_b32 s73, s76, 14\n"
    "s_add_u32 s8, s8, s73\n"
    "s_lshl_b32 s73, s61, 12\n"
    "s_add_u32 s8, s8, s73\n"
    "s_lshl_b32 s9, s61, 12\n"
    "s_lshl_b32 s77, s74, 12\n"
    "s_lshl_b32 s73, s64, 10\n"
    "s_add_u32 s77, s77, s73\n"
    "s_lshl_b32 s73, s75, 9\n"
    "s_add_u32 s77, s77, s73\n"
    "s_add_u32 s77, s77, 0x488000\n"
    "v_and_b32 v168, 7, v1\n"
    "v_lshlrev_b32 v168, 6, v168\n"
    "s_waitcnt vmcnt(8)\n"
    "ds_read_b128 a[32:35], v20\n"
    "ds_read_b128 a[36:39], v21\n"
    "ds_read_b128 a[40:43], v22\n"
    "ds_read_b128 a[44:47], v23\n"
    "ds_read_b128 a[48:51], v20 offset:4096\n"
    "ds_read_b128 a[52:55], v21 offset:4096\n"
    "ds_read_b128 a[56:59], v22 offset:4096\n"
    "ds_read_b128 a[60:63], v23 offset:4096\n"
    "s_mov_b64 exec, s[24:25]\n"
    "buffer_load_dword v169, v168, s[52:55], s77 offen sc1\n"
    "s_mov_b64 exec, -1\n"
    "s_xor_b32 s96, s27, 0x500\n"
    "s_add_u32 s97, s96, 0x80\n"
    "s_add_u32 m0, s72, 0x8000\n"
    "s_nop 0\n"
    "buffer_load_dwordx4 v8, s[36:39], s96 offen lds\n"
    "buffer_load_dwordx4 v9, s[36:39], s96 offen offset:1024 lds\n"
    "buffer_load_dwordx4 v10, s[36:39], s96 offen offset:2048 lds\n"
    "buffer_load_dwordx4 v11, s[36:39], s96 offen offset:3072 lds\n"
    "s_add_u32 m0, s72, 0x9000\n"
    "s_nop 0\n"
    "buffer_load_dwordx4 v8, s[36:39], s97 offen lds\n"
    "buffer_load_dwordx4 v9, s[36:39], s97 offen offset:1024 lds\n"
    "buffer_load_dwordx4 v10, s[36:39], s97 offen offset:2048 lds\n"
    "buffer_load_dwordx4 v11, s[36:39], s97 offen offset:3072 lds\n"
    "s_waitcnt vmcnt(8)\n"
    "s_mov_b64 exec, s[24:25]\n"
    "v_cmp_ne_u32 vcc, s66, v169\n"
    "s_cbranch_vccnz Lslow7_%=\n"
    "Lback8_%=:\n"
    "s_mov_b64 exec, -1\n"
    "s_waitcnt lgkmcnt(0)\n"
    "s_barrier\n"
    "s_xor_b32 s10, s8, 0x20000\n"
    "s_add_u32 m0, s9, 0x18000\n"
    "s_nop 0\n"
    "buffer_load_dwordx4 v2, s[52:55], s10 offen sc1 lds\n"
    "buffer_load_dwordx4 v2, s[52:55], s10 offen offset:1024 sc1 lds\n"
    "buffer_load_dwordx4 v2, s[52:55], s10 offen offset:2048 sc1 lds\n"
    "buffer_load_dwordx4 v2, s[52:55], s10 offen offset:3072 sc1 lds\n"
    "s_xor_b32 s10, s8, 0x40000\n"
    "s_add_u32 m0, s9, 0x1c000\n"
    "s_nop 0\n"
    "buffer_load_dwordx4 v2, s[52:55], s10 offen sc1 lds\n"
    "buffer_load_dwordx4 v2, s[52:55], s10 offen offset:1024 sc1 lds\n"
    "buffer_load_dwordx4 v2, s[52:55], s10 offen offset:2048 sc1 lds\n"
    "buffer_load_dwordx4 v2, s[52:55], s10 offen offset:3072 sc1 lds\n"
    "s_xor_b32 s10, s8, 0x28000\n"
    "s_add_u32 m0, s9, 0x20000\n"
    "s_nop 0\n"
    "buffer_load_dwordx4 v2, s[52:55], s10 offen sc1 lds\n"
    "buffer_load_dwordx4 v2, s[52:55], s10 offen offset:1024 sc1 lds\n"
    "buffer_load_dwordx4 v2, s[52:55], s10 offen offset:2048 sc1 lds\n"
    "buffer_load_dwordx4 v2, s[52:55], s10 offen offset:3072 sc1 lds\n"
    "ds_read_b128 a[160:163], v12\n"
    "ds_read_b128 a[164:167], v13\n"
    "ds_read_b128 a[168:171], v14\n"
    "ds_read_b128 a[172:175], v15\n"
    "ds_read_b128 a[176:179], v12 offset:4096\n"
    "ds_read_b128 a[180:183], v13 offset:4096\n"
    "ds_read_b128 a[184:187], v14 offset:4096\n"
    "ds_read_b128 a[188:191], v15 offset:4096\n"
    "ds_read_b128 a[64:67], v16\n"
    "ds_read_b128 a[68:71], v17\n"
    "ds_read_b128 a[72:75], v18\n"
    "ds_read_b128 a[76:79], v19\n"
    "ds_read_b128 a[80:83], v16 offset:4096\n"
    "ds_read_b128 a[84:87], v17 offset:4096\n"
    "ds_read_b128 a[88:91], v18 offset:4096\n"
    "ds_read_b128 a[92:95], v19 offset:4096\n"
    "s_or_b32 s19, s64, 1\n"
    "s_xor_b32 s19, s61, s19\n"
    "s_lshl_b32 s19, s19, 13\n"
    "s_mul_i32 s73, s69, 0x38000\n"
    "s_add_u32 s19, s19, s73\n"
    "s_add_u32 s19, s19, 0x4c0000\n"
    "v_lshlrev_b32 v170, 10, v5\n"
    "v_lshl_or_b32 v170, v6, 4, v170\n"
    "s_lshl_b32 s74, s62, 17\n"
    "s_lshl_b32 s73, s61, 15\n"
    "s_add_u32 s74, s74, s73\n"
    "s_lshl_b32 s73, s64, 8\n"
    "s_add_u32 s74, s74, s73\n"
    "s_lshl_b32 s73, s63, 6\n"
    "s_add_u32 s74, s74, s73\n"
    "s_waitcnt lgkmcnt(0)\n"
    "s_waitcnt vmcnt(8)\n"
    "s_barrier\n"
    "ds_read_b128 v[112:115], v172 offset:32768\n"
    "ds_read_b128 v[116:119], v172 offset:33792\n"
    "ds_read_b128 v[120:123], v172 offset:34816\n"
    "ds_read_b128 v[124:127], v172 offset:35840\n"
    "ds_read_b128 v[128:131], v172 offset:36864\n"
    "ds_read_b128 v[132:135], v172 offset:37888\n"
    "s_waitcnt lgkmcnt(5)\n"
    "v_mfma_f32_32x32x16_bf16 v[64:79], v[112:115], a[0:3], 0\n"
    "ds_read_b128 v[136:139], v172 offset:38912\n"
    "ds_read_b128 a[96:99], v12 offset:32768\n"
    "s_xor_b32 s96, s27, 0x200\n"
    "s_add_u32 s97, s96, 0x80\n"
    "s_add_u32 m0, s72, 0\n"
    "s_nop 0\n"
    "buffer_load_dwordx4 v8, s[36:39], s96 offen lds\n"
    "s_waitcnt lgkmcnt(6)\n"
    "v_mfma_f32_32x32x16_bf16 v[64:79], v[116:119], a[4:7], v[64:79]\n"
    "ds_read_b128 v[140:143], v172 offset:39936\n"
    "ds_read_b128 a[100:103], v13 offset:32768\n"
    "buffer_load_dwordx4 v9, s[36:39], s96 offen offset:1024 lds\n"
    "s_waitcnt lgkmcnt(6)\n"
    "v_mfma_f32_32x32x16_bf16 v[64:79], v[120:123], a[8:11], v[64:79]\n"
    "ds_read_b128 v[112:115], v172 offset:40960\n"
    "ds_read_b128 a[104:107], v14 offset:32768\n"
    "buffer_load_dwordx4 v10, s[36:39], s96 offen offset:2048 lds\n"
    "v_mfma_f32_32x32x16_bf16 v[64:79], v[124:127], a[12:15], v[64:79]\n"
    "ds_read_b128 v[116:119], v172 offset:41984\n"
    "ds_read_b128 a[108:111], v15 offset:32768\n"
    "buffer_load_dwordx4 v11, s[36:39], s96 offen offset:3072 lds\n"
    "s_waitcnt lgkmcnt(8)\n"
    "v_mfma_f32_32x32x16_bf16 v[64:79], v[128:131], a[16:19], v[64:79]\n"
    "ds_read_b128 v[120:123], v172 offset:43008\n"
    "ds_read_b128 a[112:115], v12 offset:36864\n"
    "v_mov_b32 v43, s66\n"
    "v_mov_b32 v47, s66\n"
    "v_mov_b32 v50, 0\n"
    "v_mov_b32 v51, s66\n"
    "v_mov_b32 v55, 0\n"
    "v_mfma_f32_32x32x16_bf16 v[64:79], v[132:135], a[20:23], v[64:79]\n"
    "ds_read_b128 v[124:127], v172 offset:44032\n"
    "ds_read_b128 a[116:119], v13 offset:36864\n"
    "s_add_u32 m0, s72, 0x1000\n"
    "s_nop 0\n"
    "buffer_load_dwordx4 v8, s[36:39], s97 offen lds\n"
    "v_mov_b32 v59, 0\n"
    "s_waitcnt lgkmcnt(9)\n"
    "v_mfma_f32_32x32x16_bf16 v[64:79], v[136:139], a[24:27], v[64:79]\n"
    "ds_read_b128 v[128:131], v172 offset:45056\n"
    "ds_read_b128 a[120:123], v14 offset:36864\n"
    "buffer_load_dwordx4 v9, s[36:39], s97 offen offset:1024 lds\n"
    "v_mov_b32 v62, 0\n"
    "v_mov_b32 v63, 0\n"
    "v_lshrrev_b32 v169, 3, v5\n"
    "v_mfma_f32_32x32x16_bf16 v[64:79], v[140:143], a[28:31], v[64:79]\n"
    "ds_read_b128 v[132:135], v172 offset:46080\n"
    "ds_read_b128 a[124:127], v15 offset:36864\n"
    "buffer_load_dwordx4 v10, s[36:39], s97 offen offset:2048 lds\n"
    "v_bfe_u32 v171, v5, 1, 1\n"
    "v_lshl_or_b32 v169, v169, 1, v171\n"
    "v_bfe_u32 v171, v5, 2, 1\n"
    "s_waitcnt lgkmcnt(9)\n"
    "v_mfma_f32_32x32x16_bf16 v[64:79], v[112:115], a[32:35], v[64:79]\n"
    "ds_read_b128 v[136:139], v172 offset:47104\n"
    "v_xor_b32 v171, v171, v6\n"
    "v_lshl_or_b32 v169, v171, 3, v169\n"
    "v_and_b32 v171, 1, v5\n"
    "v_lshlrev_b32 v171, 4, v171\n"
    "v_mov_b32 v52, 0x3f80\n"
    "v_lshlrev_b32 v171, v171, v52\n"
    "v_mfma_f32_32x32x16_bf16 v[64:79], v[116:119], a[36:39], v[64:79]\n"
    "ds_read_b128 v[140:143], v172 offset:48128\n"
    "buffer_load_dwordx4 v11, s[36:39], s97 offen offset:3072 lds\n"
    "s_waitcnt lgkmcnt(2)\n"
    "s_xor_b32 s96, s27, 0x600\n"
    "s_add_u32 s97, s96, 0x80\n"
    "v_mfma_f32_32x32x16_bf16 v[64:79], v[120:123], a[40:43], v[64:79]\n"
    "s_waitcnt vmcnt(12)\n"
    "s_barrier\n"
    "ds_read_b128 v[112:115], v172 offset:49152\n"
    "s_xor_b32 s10, s8, 0x48000\n"
    "s_add_u32 m0, s9, 0x24000\n"
    "s_nop 0\n"
    "buffer_load_dwordx4 v2, s[52:55], s10 offen sc1 lds\n"
    "v_mfma_f32_32x32x16_bf16 v[64:79], v[124:127], a[44:47], v[64:79]\n"
    "ds_read_b128 v[116:119], v172 offset:50176\n"
    "buffer_load_dwordx4 v2, s[52:55], s10 offen offset:1024 sc1 lds\n"
    "v_cmp_eq_u32 vcc, 0, v169\n"
    "s_nop 1\n"
    "v_cndmask_b32 v52, 0, v171, vcc\n"
    "v_mfma_f32_32x32x16_bf16 v[64:79], v[128:131], a[48:51], v[64:79]\n"
    "ds_read_b128 v[120:123], v172 offset:51200\n"
    "v_cmp_eq_u32 vcc, 1, v169\n"
    "s_nop 1\n"
    "v_cndmask_b32 v53, 0, v171, vcc\n"
    "v_cmp_eq_u32 vcc, 2, v169\n"
    "s_nop 1\n"
    "v_cndmask_b32 v54, 0, v171, vcc\n"
    "v_mfma_f32_32x32x16_bf16 v[64:79], v[132:135], a[52:55], v[64:79]\n"
    "ds_read_b128 v[124:127], v172 offset:52224\n"
    "buffer_load_dwordx4 v2, s[52:55], s10 offen offset:2048 sc1 lds\n"
    "v_cmp_eq_u32 vcc, 3, v169\n"
    "s_nop 1\n"
    "v_cndmask_b32 v56, 0, v171, vcc\n"
    "s_waitcnt lgkmcnt(4)\n"
    "v_mfma_f32_32x32x16_bf16 v[64:79], v[136:139], a[56:59], v[64:79]\n"
    "ds_read_b128 v[128:131], v172 offset:53248\n"
    "buffer_load_dwordx4 v2, s[52:55], s10 offen offset:3072 sc1 lds\n"
    "v_cmp_eq_u32 vcc, 4, v169\n"
    "s_nop 1\n"
    "v_cndmask_b32 v57, 0, v171, vcc\n"
    "v_mfma_f32_32x32x16_bf16 v[64:79], v[140:143], a[60:63], v[64:79]\n"
    "ds_read_b128 v[132:135], v172 offset:54272\n"
    "s_add_u32 m0, s72, 0x8000\n"
    "s_nop 0\n"
    "buffer_load_dwordx4 v8, s[36:39], s96 offen lds\n"
    "s_waitcnt lgkmcnt(4)\n"
    "v_mfma_f32_32x32x16_bf16 v[80:95], v[112:115], a[0:3], 0\n"
    "ds_read_b128 v[136:139], v172 offset:55296\n"
    "buffer_load_dwordx4 v9, s[36:39], s96 offen offset:1024 lds\n"
    "v_cmp_eq_u32 vcc, 5, v169\n"
    "s_nop 1\n"
    "v_cndmask_b32 v58, 0, v171, vcc\n"
    "v_mfma_f32_32x32x16_bf16 v[80:95], v[116:119], a[4:7], v[80:95]\n"
    "ds_read_b128 v[140:143], v172 offset:56320\n"
    "v_cmp_eq_u32 vcc, 6, v169\n"
    "s_nop 1\n"
    "v_cndmask_b32 v60, 0, v171, vcc\n"
    "v_cmp_eq_u32 vcc, 7, v169\n"
    "s_nop 1\n"
    "v_cndmask_b32 v61, 0, v171, vcc\n"
    "s_waitcnt lgkmcnt(4)\n"
    "v_mfma_f32_32x32x16_bf16 v[80:95], v[120:123], a[8:11], v[80:95]\n"
    "ds_read_b128 v[112:115], v172 offset:57344\n"
    "buffer_load_dwordx4 v10, s[36:39], s96 offen offset:2048 lds\n"
    "s_lshl_b32 s70, s62, 2\n"
    "s_add_u32 s70, s70, s61\n"
    "s_lshl_b32 s71, s64, 2\n"
    "s_add_u32 s71, s71, s63\n"
    "v_mfma_f32_32x32x16_bf16 v[80:95], v[124:127], a[12:15], v[80:95]\n"
    "ds_read_b128 v[116:119], v172 offset:58368\n"
    "buffer_load_dwordx4 v11, s[36:39], s96 offen offset:3072 lds\n"
    "s_mul_i32 s85, s70, 0x30000\n"
    "s_add_u32 s85, s85, 0xa00000\n"
    "s_lshl_b32 s16, s71, 10\n"
    "s_add_u32 s16, s16, s85\n"
    "s_waitcnt lgkmcnt(4)\n"
    "v_mfma_f32_32x32x16_bf16 v[80:95], v[128:131], a[16:19], v[80:95]\n"
    "ds_read_b128 v[120:123], v172 offset:59392\n"
    "s_add_u32 m0, s72, 0x9000\n"
    "s_nop 0\n"
    "buffer_load_dwordx4 v8, s[36:39], s97 offen lds\n"
    "s_add_u32 s17, s16, 0xc000\n"
    "s_add_u32 s18, s16, 0x18000\n"
    "v_mfma_f32_32x32x16_bf16 v[80:95], v[132:135], a[20:23], v[80:95]\n"
    "ds_read_b128 v[124:127], v172 offset:60416\n"
    "s_xor_b32 s11, s16, 0x400\n"
    "s_xor_b32 s12, s17, 0x800\n"
    "s_xor_b32 s15, s18, 0xc00\n"
    "v_add_u32 v174, 0x4000, v2\n"
    "v_add_u32 v175, 0x8000, v2\n"
    "s_lshl_b32 s82, s70, 10\n"
    "s_waitcnt lgkmcnt(4)\n"
    "v_mfma_f32_32x32x16_bf16 v[80:95], v[136:139], a[24:27], v[80:95]\n"
    "ds_read_b128 v[128:131], v172 offset:61440\n"
    "buffer_load_dwordx4 v9, s[36:39], s97 offen offset:1024 lds\n"
    "s_add_u32 s82, s82, 0x490000\n"
    "s_lshl_b32 s81, s71, 6\n"
    "s_add_u32 s81, s81, s82\n"
    "s_lshl_b32 s83, s70, 13\n"
    "v_mfma_f32_32x32x16_bf16 v[80:95], v[140:143], a[28:31], v[80:95]\n"
    "ds_read_b128 v[132:135], v172 offset:62464\n"
    "buffer_load_dwordx4 v10, s[36:39], s97 offen offset:2048 lds\n"
    "s_lshl_b32 s73, s71, 9\n"
    "s_add_u32 s83, s83, s73\n"
    "s_add_u32 s83, s83, 0x400000\n"
    "s_waitcnt lgkmcnt(4)\n"
    "v_mfma_f32_32x32x16_bf16 v[80:95], v[112:115], a[32:35], v[80:95]\n"
    "ds_read_b128 v[136:139], v172 offset:63488\n"
    "buffer_load_dwordx4 v11, s[36:39], s97 offen offset:3072 lds\n"
    "v_mfma_f32_32x32x16_bf16 v[80:95], v[116:119], a[36:39], v[80:95]\n"
    "ds_read_b128 v[140:143], v172 offset:64512\n"
    "s_waitcnt lgkmcnt(4)\n"
    "v_mfma_f32_32x32x16_bf16 v[80:95], v[120:123], a[40:43], v[80:95]\n"
    "s_waitcnt vmcnt(20)\n"
    "s_barrier\n"
    "ds_read_b128 v[112:115], v173\n"
    "s_xor_b32 s10, s8, 0x24000\n"
    "s_add_u32 m0, s9, 0x10000\n"
    "s_nop 0\n"
    "buffer_load_dwordx4 v2, s[52:55], s10 offen sc1 lds\n"
    "v_mfma_f32_32x32x16_bf16 v[80:95], v[124:127], a[44:47], v[80:95]\n"
    "ds_read_b128 v[116:119], v173 offset:1024\n"
    "buffer_load_dwordx4 v2, s[52:55], s10 offen offset:1024 sc1 lds\n"
    "s_waitcnt vmcnt(14)\n"
    "ds_read_b128 a[128:131], v12\n"
    "s_waitcnt lgkmcnt(5)\n"
    "v_mfma_f32_32x32x16_bf16 v[80:95], v[128:131], a[48:51], v[80:95]\n"
    "ds_read_b128 v[120:123], v173 offset:2048\n"
    "buffer_load_dwordx4 v2, s[52:55], s10 offen offset:2048 sc1 lds\n"
    "ds_read_b128 a[132:135], v13\n"
    "v_mfma_f32_32x32x16_bf16 v[80:95], v[132:135], a[52:55], v[80:95]\n"
    "ds_read_b128 v[124:127], v173 offset:3072\n"
    "buffer_load_dwordx4 v2, s[52:55], s10 offen offset:3072 sc1 lds\n"
    "ds_read_b128 a[136:139], v14\n"
    "s_waitcnt lgkmcnt(7)\n"
    "v_mfma_f32_32x32x16_bf16 v[80:95], v[136:139], a[56:59], v[80:95]\n"
    "ds_read_b128 v[128:131], v173 offset:4096\n"
    "ds_read_b128 a[140:143], v15\n"
    "v_mfma_f32_32x32x16_bf16 v[80:95], v[140:143], a[60:63], v[80:95]\n"
    "ds_read_b128 v[132:135], v173 offset:5120\n"
    "ds_read_b128 a[144:147], v12 offset:4096\n"
    "s_waitcnt lgkmcnt(9)\n"
    "v_mfma_f32_32x32x16_bf16 v[64:79], v[112:115], a[64:67], v[64:79]\n"
    "ds_read_b128 v[136:139], v173 offset:6144\n"
    "ds_read_b128 a[148:151], v13 offset:4096\n"
    "v_mfma_f32_32x32x16_bf16 v[64:79], v[116:119], a[68:71], v[64:79]\n"
    "ds_read_b128 v[140:143], v173 offset:7168\n"
    "ds_read_b128 a[152:155], v14 offset:4096\n"
    "s_waitcnt lgkmcnt(9)\n"
    "v_mfma_f32_32x32x16_bf16 v[64:79], v[120:123], a[72:75], v[64:79]\n"
    "ds_read_b128 v[112:115], v173 offset:8192\n"
    "ds_read_b128 a[156:159], v15 offset:4096\n"
    "s_waitcnt lgkmcnt(0)\n"
    "s_xor_b32 s96, s27, 0x700\n"
    "s_add_u32 s97, s96, 0x80\n"
    "s_add_u32 m0, s72, 0\n"
    "s_nop 0\n"
    "buffer_load_dwordx4 v8, s[36:39], s96 offen lds\n"
    "v_mfma_f32_32x32x16_bf16 v[64:79], v[124:127], a[76:79], v[64:79]\n"
    "ds_read_b128 v[116:119], v173 offset:9216\n"
    "buffer_load_dwordx4 v9, s[36:39], s96 offen offset:1024 lds\n"
    "v_mfma_f32_32x32x16_bf16 v[64:79], v[128:131], a[80:83], v[64:79]\n"
    "ds_read_b128 v[120:123], v173 offset:10240\n"
    "buffer_load_dwordx4 v10, s[36:39], s96 offen offset:2048 lds\n"
    "v_mfma_f32_32x32x16_bf16 v[64:79], v[132:135], a[84:87], v[64:79]\n"
    "ds_read_b128 v[124:127], v173 offset:11264\n"
    "buffer_load_dwordx4 v11, s[36:39], s96 offen offset:3072 lds\n"
    "v_mfma_f32_32x32x16_bf16 v[64:79], v[136:139], a[88:91], v[64:79]\n"
    "ds_read_b128 v[128:131], v173 offset:12288\n"
    "v_mfma_f32_32x32x16_bf16 v[64:79], v[140:143], a[92:95], v[64:79]\n"
    "ds_read_b128 v[132:135], v173 offset:13312\n"
    "s_add_u32 m0, s72, 0x1000\n"
    "s_nop 0\n"
    "buffer_load_dwordx4 v8, s[36:39], s97 offen lds\n"
    "s_waitcnt lgkmcnt(4)\n"
    "v_mfma_f32_32x32x16_bf16 v[64:79], v[112:115], a[96:99], v[64:79]\n"
    "ds_read_b128 v[136:139], v173 offset:14336\n"
    "buffer_load_dwordx4 v9, s[36:39], s97 offen offset:1024 lds\n"
    "v_mfma_f32_32x32x16_bf16 v[64:79], v[116:119], a[100:103], v[64:79]\n"
    "ds_read_b128 v[140:143], v173 offset:15360\n"
    "buffer_load_dwordx4 v10, s[36:39], s97 offen offset:2048 lds\n"
    "s_waitcnt lgkmcnt(4)\n"
    "v_mfma_f32_32x32x16_bf16 v[64:79], v[120:123], a[104:107], v[64:79]\n"
    "s_waitcnt vmcnt(19)\n"
    "s_barrier\n"
    "ds_read_b128 v[112:115], v173 offset:16384\n"
    "s_xor_b32 s10, s8, 0x44000\n"
    "s_waitcnt vmcnt(11)\n"
    "ds_read_b128 a[192:195], v12 offset:32768\n"
    "v_mfma_f32_32x32x16_bf16 v[64:79], v[124:127], a[108:111], v[64:79]\n"
    "ds_read_b128 v[116:119], v173 offset:17408\n"
    "s_add_u32 m0, s9, 0x14000\n"
    "s_nop 0\n"
    "buffer_load_dwordx4 v2, s[52:55], s10 offen sc1 lds\n"
    "ds_read_b128 a[196:199], v13 offset:32768\n"
    "s_waitcnt lgkmcnt(6)\n"
    "v_mfma_f32_32x32x16_bf16 v[64:79], v[128:131], a[112:115], v[64:79]\n"
    "ds_read_b128 v[120:123], v173 offset:18432\n"
    "buffer_load_dwordx4 v2, s[52:55], s10 offen offset:1024 sc1 lds\n"
    "ds_read_b128 a[200:203], v14 offset:32768\n"
    "v_mfma_f32_32x32x16_bf16 v[64:79], v[132:135], a[116:119], v[64:79]\n"
    "ds_read_b128 v[124:127], v173 offset:19456\n"
    "buffer_load_dwordx4 v2, s[52:55], s10 offen offset:2048 sc1 lds\n"
    "ds_read_b128 a[204:207], v15 offset:32768\n"
    "s_waitcnt lgkmcnt(8)\n"
    "v_mfma_f32_32x32x16_bf16 v[64:79], v[136:139], a[120:123], v[64:79]\n"
    "ds_read_b128 v[128:131], v173 offset:20480\n"
    "ds_read_b128 a[208:211], v12 offset:36864\n"
    "v_mfma_f32_32x32x16_bf16 v[64:79], v[140:143], a[124:127], v[64:79]\n"
    "ds_read_b128 v[132:135], v173 offset:21504\n"
    "buffer_load_dwordx4 v2, s[52:55], s10 offen offset:3072 sc1 lds\n"
    "ds_read_b128 a[212:215], v13 offset:36864\n"
    "s_waitcnt lgkmcnt(9)\n"
    "v_mfma_f32_32x32x16_bf16 v[80:95], v[112:115], a[64:67], v[80:95]\n"
    "ds_read_b128 v[136:139], v173 offset:22528\n"
    "ds_read_b128 a[216:219], v14 offset:36864\n"
    "s_add_u32 m0, s72, 0x1000\n"
    "s_nop 0\n"
    "buffer_load_dwordx4 v11, s[36:39], s97 offen offset:3072 lds\n"
    "v_mfma_f32_32x32x16_bf16 v[80:95], v[116:119], a[68:71], v[80:95]\n"
    "ds_read_b128 v[140:143], v173 offset:23552\n"
    "s_mov_b32 s97, 0x481000\n"
    "s_mul_i32 s78, s69, 0x1c0\n"
    "s_add_u32 s79, s78, 0x180\n"
    "s_and_b32 s78, s78, 0xffffff00\n"
    "s_or_b32 s79, s79, 0xc0\n"
    "v_lshl_add_u32 v161, v1, 6, s78\n"
    "s_waitcnt lgkmcnt(8)\n"
    "v_mfma_f32_32x32x16_bf16 v[80:95], v[120:123], a[72:75], v[80:95]\n"
    "ds_read_b128 v[112:115], v173 offset:24576\n"
    "v_min_u32 v161, s79, v161\n"
    "v_lshlrev_b32 v162, 4, v5\n"
    "s_mul_i32 s80, s69, 0xe00\n"
    "s_add_u32 s80, s80, 0x4b0000\n"
    "s_mul_i32 s87, s65, 0x1c000\n"
    "s_mul_i32 s73, s68, 0x3800\n"
    "v_mfma_f32_32x32x16_bf16 v[80:95], v[124:127], a[76:79], v[80:95]\n"
    "ds_read_b128 v[116:119], v173 offset:25600\n"
    "s_add_u32 s87, s87, s73\n"
    "s_mul_i32 s73, s61, 0xe00\n"
    "s_add_u32 s87, s87, s73\n"
    "s_mov_b32 s50, 0x1c0000\n"
    "s_mov_b32 s51, 0x20000\n"
    "v_lshlrev_b32 v166, 7, v1\n"
    "s_waitcnt lgkmcnt(6)\n"
    "v_mfma_f32_32x32x16_bf16 v[80:95], v[128:131], a[80:83], v[80:95]\n"
    "ds_read_b128 v[120:123], v173 offset:26624\n"
    "ds_read_b128 a[220:223], v15 offset:36864\n"
    "buffer_load_dwordx4 v[144:147], v170, s[28:31], s74 offen\n"
    "v_mfma_f32_32x32x16_bf16 v[80:95], v[132:135], a[84:87], v[80:95]\n"
    "ds_read_b128 v[124:127], v173 offset:27648\n"
    "s_waitcnt lgkmcnt(5)\n"
    "v_mfma_f32_32x32x16_bf16 v[80:95], v[136:139], a[88:91], v[80:95]\n"
    "ds_read_b128 v[128:131], v173 offset:28672\n"
    "buffer_load_dwordx4 v[148:151], v170, s[28:31], s74 offen offset:32\n"
    "v_mfma_f32_32x32x16_bf16 v[80:95], v[140:143], a[92:95], v[80:95]\n"
    "ds_read_b128 v[132:135], v173 offset:29696\n"
    "s_waitcnt lgkmcnt(5)\n"
    "v_mfma_f32_32x32x16_bf16 v[80:95], v[112:115], a[96:99], v[80:95]\n"
    "ds_read_b128 v[136:139], v173 offset:30720\n"
    "v_mfma_f32_32x32x16_bf16 v[80:95], v[116:119], a[100:103], v[80:95]\n"
    "ds_read_b128 v[140:143], v173 offset:31744\n"
    "s_waitcnt lgkmcnt(4)\n"
    "v_mfma_f32_32x32x16_bf16 v[80:95], v[120:123], a[104:107], v[80:95]\n"
    "s_waitcnt vmcnt(14)\n"
    "s_barrier\n"
    "ds_read_b128 v[112:115], v172\n"
    "s_xor_b32 s10, s8, 0x2c000\n"
    "s_add_u32 m0, s9, 0x18000\n"
    "s_nop 0\n"
    "buffer_load_dwordx4 v2, s[52:55], s10 offen sc1 lds\n"
    "v_mfma_f32_32x32x16_bf16 v[80:95], v[124:127], a[108:111], v[80:95]\n"
    "ds_read_b128 v[116:119], v172 offset:1024\n"
    "s_mov_b64 exec, 0xfffffff\n"
    "buffer_load_dword v167, v166, s[48:51], s87 offen\n"
    "s_mov_b64 exec, -1\n"
    "s_waitcnt lgkmcnt(4)\n"
    "v_mfma_f32_32x32x16_bf16 v[80:95], v[128:131], a[112:115], v[80:95]\n"
    "ds_read_b128 v[120:123], v172 offset:2048\n"
    "v_mfma_f32_32x32x16_bf16 v[80:95], v[132:135], a[116:119], v[80:95]\n"
    "ds_read_b128 v[124:127], v172 offset:3072\n"
    "s_waitcnt lgkmcnt(4)\n"
    "v_mfma_f32_32x32x16_bf16 v[80:95], v[136:139], a[120:123], v[80:95]\n"
    "ds_read_b128 v[128:131], v172 offset:4096\n"
    "v_mfma_f32_32x32x16_bf16 v[80:95], v[140:143], a[124:127], v[80:95]\n"
    "ds_read_b128 v[132:135], v172 offset:5120\n"
    "buffer_load_dwordx4 v2, s[52:55], s10 offen offset:1024 sc1 lds\n"
    "s_waitcnt lgkmcnt(4)\n"
    "v_mfma_f32_32x32x16_bf16 v[64:79], v[112:115], a[128:131], v[64:79]\n"
    "ds_read_b128 v[136:139], v172 offset:6144\n"
    "v_mfma_f32_32x32x16_bf16 v[64:79], v[116:119], a[132:135], v[64:79]\n"
    "ds_read_b128 v[140:143], v172 offset:7168\n"
    "buffer_load_dwordx4 v2, s[52:55], s10 offen offset:2048 sc1 lds\n"
    "s_waitcnt lgkmcnt(4)\n"
    "v_mfma_f32_32x32x16_bf16 v[64:79], v[120:123], a[136:139], v[64:79]\n"
    "ds_read_b128 v[112:115], v172 offset:8192\n"
    "s_waitcnt vmcnt(6)\n"
    "ds_read_b128 a[224:227], v12\n"
    "v_mfma_f32_32x32x16_bf16 v[64:79], v[124:127], a[140:143], v[64:79]\n"
    "ds_read_b128 v[116:119], v172 offset:9216\n"
    "ds_read_b128 a[228:231], v13\n"
    "s_waitcnt lgkmcnt(6)\n"
    "v_mfma_f32_32x32x16_bf16 v[64:79], v[128:131], a[144:147], v[64:79]\n"
    "ds_read_b128 v[120:123], v172 offset:10240\n"
    "buffer_load_dwordx4 v2, s[52:55], s10 offen offset:3072 sc1 lds\n"
    "ds_read_b128 a[232:235], v14\n"
    "v_mfma_f32_32x32x16_bf16 v[64:79], v[132:135], a[148:151], v[64:79]\n"
    "ds_read_b128 v[124:127], v172 offset:11264\n"
    "ds_read_b128 a[236:239], v15\n"
    "s_waitcnt lgkmcnt(8)\n"
    "v_mfma_f32_32x32x16_bf16 v[64:79], v[136:139], a[152:155], v[64:79]\n"
    "ds_read_b128 v[128:131], v172 offset:12288\n"
    "ds_read_b128 a[240:243], v12 offset:4096\n"
    "v_mfma_f32_32x32x16_bf16 v[64:79], v[140:143], a[156:159], v[64:79]\n"
    "ds_read_b128 v[132:135], v172 offset:13312\n"
    "ds_read_b128 a[244:247], v13 offset:4096\n"
    "s_waitcnt lgkmcnt(9)\n"
    "v_mfma_f32_32x32x16_bf16 v[64:79], v[112:115], a[160:163], v[64:79]\n"
    "ds_read_b128 v[136:139], v172 offset:14336\n"
    "ds_read_b128 a[248:251], v14 offset:4096\n"
    "v_mfma_f32_32x32x16_bf16 v[64:79], v[116:119], a[164:167], v[64:79]\n"
    "ds_read_b128 v[140:143], v172 offset:15360\n"
    "ds_read_b128 a[252:255], v15 offset:4096\n"
    "s_waitcnt lgkmcnt(9)\n"
    "v_mfma_f32_32x32x16_bf16 v[64:79], v[120:123], a[168:171], v[64:79]\n"
    "s_barrier\n"
    "ds_read_b128 v[112:115], v172 offset:16384\n"
    "s_xor_b32 s10, s8, 0x4c000\n"
    "s_add_u32 m0, s9, 0x1c000\n"
    "s_nop 0\n"
    "buffer_load_dwordx4 v2, s[52:55], s10 offen sc1 lds\n"
    "v_mfma_f32_32x32x16_bf16 v[64:79], v[124:127], a[172:175], v[64:79]\n"
    "ds_read_b128 v[116:119], v172 offset:17408\n"
    "s_waitcnt lgkmcnt(7)\n"
    "v_mfma_f32_32x32x16_bf16 v[64:79], v[128:131], a[176:179], v[64:79]\n"
    "ds_read_b128 v[120:123], v172 offset:18432\n"
    "buffer_load_dwordx4 v2, s[52:55], s10 offen offset:1024 sc1 lds\n"
    "v_mfma_f32_32x32x16_bf16 v[64:79], v[132:135], a[180:183], v[64:79]\n"
    "ds_read_b128 v[124:127], v172 offset:19456\n"
    "s_waitcnt lgkmcnt(5)\n"
    "v_mfma_f32_32x32x16_bf16 v[64:79], v[136:139], a[184:187], v[64:79]\n"
    "ds_read_b128 v[128:131], v172 offset:20480\n"
    "s_waitcnt vmcnt(8)\n"
    "v_permlane32_swap_b32 v144, v146\n"
    "v_permlane32_swap_b32 v145, v147\n"
    "s_waitcnt vmcnt(7)\n"
    "v_permlane32_swap_b32 v148, v150\n"
    "v_permlane32_swap_b32 v149, v151\n"
    "v_mfma_f32_32x32x16_bf16 v[64:79], v[140:143], a[188:191], v[64:79]\n"
    "ds_read_b128 v[132:135], v172 offset:21504\n"
    "buffer_load_dwordx4 v2, s[52:55], s10 offen offset:2048 sc1 lds\n"
    "v_lshlrev_b32 v24, 16, v144\n"
    "v_and_b32 v25, 0xffff0000, v144\n"
    "v_lshlrev_b32 v26, 16, v145\n"
    "v_and_b32 v27, 0xffff0000, v145\n"
    "s_waitcnt lgkmcnt(4)\n"
    "v_mfma_f32_32x32x16_bf16 v[80:95], v[112:115], a[128:131], v[80:95]\n"
    "ds_read_b128 v[136:139], v172 offset:22528\n"
    "v_lshlrev_b32 v28, 16, v146\n"
    "v_and_b32 v29, 0xffff0000, v146\n"
    "v_lshlrev_b32 v30, 16, v147\n"
    "v_and_b32 v31, 0xffff0000, v147\n"
    "v_lshlrev_b32 v32, 16, v148\n"
    "v_and_b32 v33, 0xffff0000, v148\n"
    "v_mfma_f32_32x32x16_bf16 v[80:95], v[116:119], a[132:135], v[80:95]\n"
    "ds_read_b128 v[140:143], v172 offset:23552\n"
    "buffer_load_dwordx4 v2, s[52:55], s10 offen offset:3072 sc1 lds\n"
    "v_lshlrev_b32 v34, 16, v149\n"
    "v_and_b32 v35, 0xffff0000, v149\n"
    "v_lshlrev_b32 v36, 16, v150\n"
    "v_and_b32 v37, 0xffff0000, v150\n"
    "s_waitcnt lgkmcnt(4)\n"
    "v_mfma_f32_32x32x16_bf16 v[80:95], v[120:123], a[136:139], v[80:95]\n"
    "ds_read_b128 v[112:115], v172 offset:24576\n"
    "v_lshlrev_b32 v38, 16, v151\n"
    "v_and_b32 v39, 0xffff0000, v151\n"
    "v_mfma_f32_32x32x16_bf16 v[80:95], v[124:127], a[140:143], v[80:95]\n"
    "ds_read_b128 v[116:119], v172 offset:25600\n"
    "s_waitcnt lgkmcnt(4)\n"
    "v_mfma_f32_32x32x16_bf16 v[80:95], v[128:131], a[144:147], v[80:95]\n"
    "ds_read_b128 v[120:123], v172 offset:26624\n"
    "v_mfma_f32_32x32x16_bf16 v[80:95], v[132:135], a[148:151], v[80:95]\n"
    "ds_read_b128 v[124:127], v172 offset:27648\n"
    "s_waitcnt lgkmcnt(4)\n"
    "v_mfma_f32_32x32x16_bf16 v[80:95], v[136:139], a[152:155], v[80:95]\n"
    "ds_read_b128 v[128:131], v172 offset:28672\n"
    "v_mfma_f32_32x32x16_bf16 v[80:95], v[140:143], a[156:159], v[80:95]\n"
    "ds_read_b128 v[132:135], v172 offset:29696\n"
    "s_waitcnt lgkmcnt(4)\n"
    "v_mfma_f32_32x32x16_bf16 v[80:95], v[112:115], a[160:163], v[80:95]\n"
    "ds_read_b128 v[136:139], v172 offset:30720\n"
    "v_mfma_f32_32x32x16_bf16 v[80:95], v[116:119], a[164:167], v[80:95]\n"
    "ds_read_b128 v[140:143], v172 offset:31744\n"
    "s_waitcnt lgkmcnt(4)\n"
    "v_mfma_f32_32x32x16_bf16 v[80:95], v[120:123], a[168:171], v[80:95]\n"
    "s_waitcnt vmcnt(4)\n"
    "s_barrier\n"
    "ds_read_b128 v[112:115], v172 offset:32768\n"
    "s_xor_b32 s10, s8, 0x60000\n"
    "s_add_u32 m0, s9, 0x20000\n"
    "s_nop 0\n"
    "buffer_load_dwordx4 v2, s[52:55], s10 offen sc1 lds\n"
    "v_mfma_f32_32x32x16_bf16 v[80:95], v[124:127], a[172:175], v[80:95]\n"
    "ds_read_b128 v[116:119], v172 offset:33792\n"
    "s_waitcnt lgkmcnt(4)\n"
    "v_mfma_f32_32x32x16_bf16 v[80:95], v[128:131], a[176:179], v[80:95]\n"
    "ds_read_b128 v[120:123], v172 offset:34816\n"
    "buffer_load_dwordx4 v2, s[52:55], s10 offen offset:1024 sc1 lds\n"
    "v_mfma_f32_32x32x16_bf16 v[80:95], v[132:135], a[180:183], v[80:95]\n"
    "ds_read_b128 v[124:127], v172 offset:35840\n"
    "s_waitcnt lgkmcnt(4)\n"
    "v_mfma_f32_32x32x16_bf16 v[80:95], v[136:139], a[184:187], v[80:95]\n"
    "ds_read_b128 v[128:131], v172 offset:36864\n"
    "v_mfma_f32_32x32x16_bf16 v[80:95], v[140:143], a[188:191], v[80:95]\n"
    "ds_read_b128 v[132:135], v172 offset:37888\n"
    "buffer_load_dwordx4 v2, s[52:55], s10 offen offset:2048 sc1 lds\n"
    "s_waitcnt lgkmcnt(4)\n"
    "v_mfma_f32_32x32x16_bf16 v[64:79], v[112:115], a[192:195], v[64:79]\n"
    "ds_read_b128 v[136:139], v172 offset:38912\n"
    "buffer_load_dword v160, v161, s[52:55], s97 offen sc1\n"
    "v_mfma_f32_32x32x16_bf16 v[64:79], v[116:119], a[196:199], v[64:79]\n"
    "ds_read_b128 v[140:143], v172 offset:39936\n"
    "buffer_load_dwordx4 v2, s[52:55], s10 offen offset:3072 sc1 lds\n"
    "s_xor_b32 s10, s8, 0x68000\n"
    "s_waitcnt lgkmcnt(4)\n"
    "v_mfma_f32_32x32x16_bf16 v[64:79], v[120:123], a[200:203], v[64:79]\n"
    "ds_read_b128 v[112:115], v172 offset:40960\n"
    "v_mfma_f32_32x32x16_bf16 v[64:79], v[124:127], a[204:207], v[64:79]\n"
    "ds_read_b128 v[116:119], v172 offset:41984\n"
    "s_waitcnt lgkmcnt(4)\n"
    "v_mfma_f32_32x32x16_bf16 v[64:79], v[128:131], a[208:211], v[64:79]\n"
    "ds_read_b128 v[120:123], v172 offset:43008\n"
    "s_add_u32 m0, s9, 0x24000\n"
    "s_nop 0\n"
    "buffer_load_dwordx4 v2, s[52:55], s10 offen sc1 lds\n"
    "v_mfma_f32_32x32x16_bf16 v[64:79], v[132:135], a[212:215], v[64:79]\n"
    "ds_read_b128 v[124:127], v172 offset:44032\n"
    "s_waitcnt lgkmcnt(4)\n"
    "v_mfma_f32_32x32x16_bf16 v[64:79], v[136:139], a[216:219], v[64:79]\n"
    "ds_read_b128 v[128:131], v172 offset:45056\n"
    "buffer_load_dwordx4 v2, s[52:55], s10 offen offset:1024 sc1 lds\n"
    "v_mfma_f32_32x32x16_bf16 v[64:79], v[140:143], a[220:223], v[64:79]\n"
    "ds_read_b128 v[132:135], v172 offset:46080\n"
    "s_waitcnt lgkmcnt(4)\n"
    "v_mfma_f32_32x32x16_bf16 v[64:79], v[112:115], a[224:227], v[64:79]\n"
    "ds_read_b128 v[136:139], v172 offset:47104\n"
    "v_mfma_f32_32x32x16_bf16 v[64:79], v[116:119], a[228:231], v[64:79]\n"
    "ds_read_b128 v[140:143], v172 offset:48128\n"
    "buffer_load_dwordx4 v2, s[52:55], s10 offen offset:2048 sc1 lds\n"
    "s_waitcnt lgkmcnt(4)\n"
    "v_mfma_f32_32x32x16_bf16 v[64:79], v[120:123], a[232:235], v[64:79]\n"
    "s_waitcnt vmcnt(8)\n"
    "s_barrier\n"
    "ds_read_b128 v[112:115], v172 offset:49152\n"
    "v_mfma_f32_32x32x16_bf16 v[64:79], v[124:127], a[236:239], v[64:79]\n"
    "ds_read_b128 v[116:119], v172 offset:50176\n"
    "buffer_load_dwordx4 v2, s[52:55], s10 offen offset:3072 sc1 lds\n"
    "s_xor_b32 s10, s8, 0x64000\n"
    "s_waitcnt lgkmcnt(4)\n"
    "v_mfma_f32_32x32x16_bf16 v[64:79], v[128:131], a[240:243], v[64:79]\n"
    "ds_read_b128 v[120:123], v172 offset:51200\n"
    "v_mfma_f32_32x32x16_bf16 v[64:79], v[132:135], a[244:247], v[64:79]\n"
    "ds_read_b128 v[124:127], v172 offset:52224\n"
    "s_waitcnt lgkmcnt(4)\n"
    "v_mfma_f32_32x32x16_bf16 v[64:79], v[136:139], a[248:251], v[64:79]\n"
    "ds_read_b128 v[128:131], v172 offset:53248\n"
    "s_add_u32 m0, s9, 0x10000\n"
    "s_nop 0\n"
    "buffer_load_dwordx4 v2, s[52:55], s10 offen sc1 lds\n"
    "v_mfma_f32_32x32x16_bf16 v[64:79], v[140:143], a[252:255], v[64:79]\n"
    "ds_read_b128 v[132:135], v172 offset:54272\n"
    "s_waitcnt lgkmcnt(4)\n"
    "v_mfma_f32_32x32x16_bf16 v[80:95], v[112:115], a[192:195], v[80:95]\n"
    "ds_read_b128 v[136:139], v172 offset:55296\n"
    "buffer_load_dwordx4 v2, s[52:55], s10 offen offset:1024 sc1 lds\n"
    "v_mfma_f32_32x32x16_bf16 v[80:95], v[116:119], a[196:199], v[80:95]\n"
    "ds_read_b128 v[140:143], v172 offset:56320\n"
    "s_nop 4\n"
    "v_cvt_pk_bf16_f32 v40, v64, v65\n"
    "v_cvt_pk_bf16_f32 v41, v66, v67\n"
    "v_cvt_pk_bf16_f32 v42, v68, v69\n"
    "s_waitcnt lgkmcnt(4)\n"
    "v_mfma_f32_32x32x16_bf16 v[80:95], v[120:123], a[200:203], v[80:95]\n"
    "ds_read_b128 v[112:115], v172 offset:57344\n"
    "v_mfma_f32_32x32x16_bf16 v[80:95], v[124:127], a[204:207], v[80:95]\n"
    "ds_read_b128 v[116:119], v172 offset:58368\n"
    "buffer_store_dwordx4 v[40:43], v2, s[52:55], s11 offen sc1\n"
    "v_cvt_pk_bf16_f32 v44, v70, v71\n"
    "v_cvt_pk_bf16_f32 v45, v72, v73\n"
    "v_cvt_pk_bf16_f32 v46, v74, v75\n"
    "s_waitcnt lgkmcnt(4)\n"
    "v_mfma_f32_32x32x16_bf16 v[80:95], v[128:131], a[208:211], v[80:95]\n"
    "ds_read_b128 v[120:123], v172 offset:59392\n"
    "v_mfma_f32_32x32x16_bf16 v[80:95], v[132:135], a[212:215], v[80:95]\n"
    "ds_read_b128 v[124:127], v172 offset:60416\n"
    "buffer_store_dwordx4 v[44:47], v174, s[52:55], s11 offen sc1\n"
    "v_cvt_pk_bf16_f32 v48, v76, v77\n"
    "v_cvt_pk_bf16_f32 v49, v78, v79\n"
    "s_waitcnt lgkmcnt(4)\n"
    "v_mfma_f32_32x32x16_bf16 v[80:95], v[136:139], a[216:219], v[80:95]\n"
    "ds_read_b128 v[128:131], v172 offset:61440\n"
    "v_mfma_f32_32x32x16_bf16 v[80:95], v[140:143], a[220:223], v[80:95]\n"
    "ds_read_b128 v[132:135], v172 offset:62464\n"
    "s_waitcnt lgkmcnt(4)\n"
    "v_mfma_f32_32x32x16_bf16 v[80:95], v[112:115], a[224:227], v[80:95]\n"
    "ds_read_b128 v[136:139], v172 offset:63488\n"
    "buffer_store_dwordx4 v[48:51], v175, s[52:55], s11 offen sc1\n"
    "s_waitcnt vmcnt(10)\n"
    "v_cmp_ne_u32 vcc, s66, v160\n"
    "s_cbranch_vccnz Lslow10_%=\n"
    "Lback11_%=:\n"
    "s_add_u32 s89, s19, 0\n"
    "v_mfma_f32_32x32x16_bf16 v[80:95], v[116:119], a[228:231], v[80:95]\n"
    "ds_read_b128 v[140:143], v172 offset:64512\n"
    "s_waitcnt lgkmcnt(4)\n"
    "v_mfma_f32_32x32x16_bf16 v[80:95], v[120:123], a[232:235], v[80:95]\n"
    "s_waitcnt vmcnt(9)\n"
    "s_barrier\n"
    "ds_read_b128 v[112:115], v173\n"
    "buffer_load_dwordx4 v2, s[52:55], s10 offen offset:2048 sc1 lds\n"
    "v_mfma_f32_32x32x16_bf16 v[80:95], v[124:127], a[236:239], v[80:95]\n"
    "ds_read_b128 v[116:119], v173 offset:1024\n"
    "s_waitcnt lgkmcnt(4)\n"
    "v_mfma_f32_32x32x16_bf16 v[80:95], v[128:131], a[240:243], v[80:95]\n"
    "ds_read_b128 v[120:123], v173 offset:2048\n"
    "v_mfma_f32_32x32x16_bf16 v[80:95], v[132:135], a[244:247], v[80:95]\n"
    "ds_read_b128 v[124:127], v173 offset:3072\n"
    "buffer_load_dwordx4 v2, s[52:55], s10 offen offset:3072 sc1 lds\n"
    "s_xor_b32 s10, s8, 0x6c000\n"
    "s_waitcnt lgkmcnt(4)\n"
    "v_mfma_f32_32x32x16_bf16 v[80:95], v[136:139], a[248:251], v[80:95]\n"
    "ds_read_b128 v[128:131], v173 offset:4096\n"
    "v_mfma_f32_32x32x16_bf16 v[80:95], v[140:143], a[252:255], v[80:95]\n"
    "ds_read_b128 v[132:135], v173 offset:5120\n"
    "s_add_u32 m0, s9, 0x14000\n"
    "s_nop 0\n"
    "buffer_load_dwordx4 v2, s[52:55], s10 offen sc1 lds\n"
    "s_waitcnt lgkmcnt(4)\n"
    "v_mfma_f32_32x32x16_bf16 v[96:111], v[112:115], a[0:3], 0\n"
    "ds_read_b128 v[136:139], v173 offset:6144\n"
    "v_mfma_f32_32x32x16_bf16 v[96:111], v[116:119], a[4:7], v[96:111]\n"
    "ds_read_b128 v[140:143], v173 offset:7168\n"
    "s_nop 2\n"
    "v_cvt_pk_bf16_f32 v40, v80, v81\n"
    "v_cvt_pk_bf16_f32 v41, v82, v83\n"
    "v_cvt_pk_bf16_f32 v42, v84, v85\n"
    "s_waitcnt lgkmcnt(4)\n"
    "v_mfma_f32_32x32x16_bf16 v[96:111], v[120:123], a[8:11], v[96:111]\n"
    "ds_read_b128 v[112:115], v173 offset:8192\n"
    "buffer_store_dwordx4 v[40:43], v2, s[52:55], s12 offen sc1\n"
    "v_cvt_pk_bf16_f32 v44, v86, v87\n"
    "v_cvt_pk_bf16_f32 v45, v88, v89\n"
    "v_cvt_pk_bf16_f32 v46, v90, v91\n"
    "v_mfma_f32_32x32x16_bf16 v[96:111], v[124:127], a[12:15], v[96:111]\n"
    "ds_read_b128 v[116:119], v173 offset:9216\n"
    "s_waitcnt lgkmcnt(4)\n"
    "v_mfma_f32_32x32x16_bf16 v[96:111], v[128:131], a[16:19], v[96:111]\n"
    "ds_read_b128 v[120:123], v173 offset:10240\n"
    "buffer_store_dwordx4 v[44:47], v174, s[52:55], s12 offen sc1\n"
    "v_cvt_pk_bf16_f32 v48, v92, v93\n"
    "v_cvt_pk_bf16_f32 v49, v94, v95\n"
    "v_mfma_f32_32x32x16_bf16 v[96:111], v[132:135], a[20:23], v[96:111]\n"
    "ds_read_b128 v[124:127], v173 offset:11264\n"
    "s_waitcnt lgkmcnt(4)\n"
    "v_mfma_f32_32x32x16_bf16 v[96:111], v[136:139], a[24:27], v[96:111]\n"
    "ds_read_b128 v[128:131], v173 offset:12288\n"
    "v_mfma_f32_32x32x16_bf16 v[96:111], v[140:143], a[28:31], v[96:111]\n"
    "ds_read_b128 v[132:135], v173 offset:13312\n"
    "buffer_store_dwordx4 v[48:51], v175, s[52:55], s12 offen sc1\n"
    "s_waitcnt lgkmcnt(4)\n"
    "v_mfma_f32_32x32x16_bf16 v[96:111], v[112:115], a[32:35], v[96:111]\n"
    "ds_read_b128 v[136:139], v173 offset:14336\n"
    "v_mfma_f32_32x32x16_bf16 v[96:111], v[116:119], a[36:39], v[96:111]\n"
    "ds_read_b128 v[140:143], v173 offset:15360\n"
    "buffer_load_dwordx4 v2, s[52:55], s10 offen offset:1024 sc1 lds\n"
    "s_waitcnt lgkmcnt(4)\n"
    "v_mfma_f32_32x32x16_bf16 v[96:111], v[120:123], a[40:43], v[96:111]\n"
    "s_waitcnt vmcnt(12)\n"
    "s_barrier\n"
    "ds_read_b128 v[112:115], v173 offset:16384\n"
    "v_mfma_f32_32x32x16_bf16 v[96:111], v[124:127], a[44:47], v[96:111]\n"
    "ds_read_b128 v[116:119], v173 offset:17408\n"
    "s_waitcnt lgkmcnt(4)\n"
    "v_mfma_f32_32x32x16_bf16 v[96:111], v[128:131], a[48:51], v[96:111]\n"
    "ds_read_b128 v[120:123], v173 offset:18432\n"
    "buffer_load_dwordx4 v2, s[52:55], s10 offen offset:2048 sc1 lds\n"
    "v_mfma_f32_32x32x16_bf16 v[96:111], v[132:135], a[52:55], v[96:111]\n"
    "ds_read_b128 v[124:127], v173 offset:19456\n"
    "s_waitcnt lgkmcnt(4)\n"
    "v_mfma_f32_32x32x16_bf16 v[96:111], v[136:139], a[56:59], v[96:111]\n"
    "ds_read_b128 v[128:131], v173 offset:20480\n"
    "buffer_load_dwordx4 v2, s[52:55], s10 offen offset:3072 sc1 lds\n"
    "s_xor_b32 s10, s8, 0\n"
    "v_mfma_f32_32x32x16_bf16 v[96:111], v[140:143], a[60:63], v[96:111]\n"
    "ds_read_b128 v[132:135], v173 offset:21504\n"
    "s_waitcnt lgkmcnt(4)\n"
    "v_mfma_f32_32x32x16_bf16 v[96:111], v[112:115], a[64:67], v[96:111]\n"
    "ds_read_b128 v[136:139], v173 offset:22528\n"
    "v_mfma_f32_32x32x16_bf16 v[96:111], v[116:119], a[68:71], v[96:111]\n"
    "ds_read_b128 v[140:143], v173 offset:23552\n"
    "s_add_u32 m0, s9, 0x18000\n"
    "s_nop 0\n"
    "buffer_load_dwordx4 v2, s[52:55], s10 offen sc1 lds\n"
    "s_waitcnt lgkmcnt(4)\n"
    "v_mfma_f32_32x32x16_bf16 v[96:111], v[120:123], a[72:75], v[96:111]\n"
    "ds_read_b128 v[112:115], v173 offset:24576\n"
    "v_mfma_f32_32x32x16_bf16 v[96:111], v[124:127], a[76:79], v[96:111]\n"
    "ds_read_b128 v[116:119], v173 offset:25600\n"
    "buffer_load_dwordx4 v2, s[52:55], s10 offen offset:1024 sc1 lds\n"
    "s_waitcnt lgkmcnt(4)\n"
    "v_mfma_f32_32x32x16_bf16 v[96:111], v[128:131], a[80:83], v[96:111]\n"
    "ds_read_b128 v[120:123], v173 offset:26624\n"
    "v_mfma_f32_32x32x16_bf16 v[96:111], v[132:135], a[84:87], v[96:111]\n"
    "ds_read_b128 v[124:127], v173 offset:27648\n"
    "s_waitcnt lgkmcnt(4)\n"
    "v_mfma_f32_32x32x16_bf16 v[96:111], v[136:139], a[88:91], v[96:111]\n"
    "ds_read_b128 v[128:131], v173 offset:28672\n"
    "buffer_load_dwordx4 v2, s[52:55], s10 offen offset:2048 sc1 lds\n"
    "v_mfma_f32_32x32x16_bf16 v[96:111], v[140:143], a[92:95], v[96:111]\n"
    "ds_read_b128 v[132:135], v173 offset:29696\n"
    "s_waitcnt lgkmcnt(4)\n"
    "v_mfma_f32_32x32x16_bf16 v[96:111], v[112:115], a[96:99], v[96:111]\n"
    "ds_read_b128 v[136:139], v173 offset:30720\n"
    "buffer_load_dwordx4 v2, s[52:55], s10 offen offset:3072 sc1 lds\n"
    "s_xor_b32 s10, s8, 0x8000\n"
    "v_mfma_f32_32x32x16_bf16 v[96:111], v[116:119], a[100:103], v[96:111]\n"
    "ds_read_b128 v[140:143], v173 offset:31744\n"
    "s_waitcnt lgkmcnt(4)\n"
    "v_mfma_f32_32x32x16_bf16 v[96:111], v[120:123], a[104:107], v[96:111]\n"
    "s_waitcnt vmcnt(11)\n"
    "s_barrier\n"
    "ds_read_b128 v[112:115], v172\n"
    "v_mfma_f32_32x32x16_bf16 v[96:111], v[124:127], a[108:111], v[96:111]\n"
    "ds_read_b128 v[116:119], v172 offset:1024\n"
    "s_add_u32 m0, s9, 0x1c000\n"
    "s_nop 0\n"
    "buffer_load_dwordx4 v2, s[52:55], s10 offen sc1 lds\n"
    "s_waitcnt lgkmcnt(4)\n"
    "v_mfma_f32_32x32x16_bf16 v[96:111], v[128:131], a[112:115], v[96:111]\n"
    "ds_read_b128 v[120:123], v172 offset:2048\n"
    "v_mfma_f32_32x32x16_bf16 v[96:111], v[132:135], a[116:119], v[96:111]\n"
    "ds_read_b128 v[124:127], v172 offset:3072\n"
    "buffer_load_dwordx4 v2, s[52:55], s10 offen offset:1024 sc1 lds\n"
    "s_waitcnt lgkmcnt(4)\n"
    "v_mfma_f32_32x32x16_bf16 v[96:111], v[136:139], a[120:123], v[96:111]\n"
    "ds_read_b128 v[128:131], v172 offset:4096\n"
    "v_mfma_f32_32x32x16_bf16 v[96:111], v[140:143], a[124:127], v[96:111]\n"
    "ds_read_b128 v[132:135], v172 offset:5120\n"
    "s_waitcnt lgkmcnt(4)\n"
    "v_mfma_f32_32x32x16_bf16 v[96:111], v[112:115], a[128:131], v[96:111]\n"
    "ds_read_b128 v[136:139], v172 offset:6144\n"
    "buffer_load_dwordx4 v2, s[52:55], s10 offen offset:2048 sc1 lds\n"
    "v_mfma_f32_32x32x16_bf16 v[96:111], v[116:119], a[132:135], v[96:111]\n"
    "ds_read_b128 v[140:143], v172 offset:7168\n"
    "s_waitcnt lgkmcnt(4)\n"
    "v_mfma_f32_32x32x16_bf16 v[96:111], v[120:123], a[136:139], v[96:111]\n"
    "ds_read_b128 v[112:115], v172 offset:8192\n"
    "buffer_load_dwordx4 v2, s[52:55], s10 offen offset:3072 sc1 lds\n"
    "s_xor_b32 s10, s8, 0x4000\n"
    "v_mfma_f32_32x32x16_bf16 v[96:111], v[124:127], a[140:143], v[96:111]\n"
    "ds_read_b128 v[116:119], v172 offset:9216\n"
    "s_waitcnt lgkmcnt(4)\n"
    "v_mfma_f32_32x32x16_bf16 v[96:111], v[128:131], a[144:147], v[96:111]\n"
    "ds_read_b128 v[120:123], v172 offset:10240\n"
    "v_mfma_f32_32x32x16_bf16 v[96:111], v[132:135], a[148:151], v[96:111]\n"
    "ds_read_b128 v[124:127], v172 offset:11264\n"
    "s_add_u32 m0, s9, 0x20000\n"
    "s_nop 0\n"
    "buffer_load_dwordx4 v2, s[52:55], s10 offen sc1 lds\n"
    "s_waitcnt lgkmcnt(4)\n"
    "v_mfma_f32_32x32x16_bf16 v[96:111], v[136:139], a[152:155], v[96:111]\n"
    "ds_read_b128 v[128:131], v172 offset:12288\n"
    "v_mfma_f32_32x32x16_bf16 v[96:111], v[140:143], a[156:159], v[96:111]\n"
    "ds_read_b128 v[132:135], v172 offset:13312\n"
    "buffer_load_dwordx4 v2, s[52:55], s10 offen offset:1024 sc1 lds\n"
    "s_waitcnt lgkmcnt(4)\n"
    "v_mfma_f32_32x32x16_bf16 v[96:111], v[112:115], a[160:163], v[96:111]\n"
    "ds_read_b128 v[136:139], v172 offset:14336\n"
    "v_mfma_f32_32x32x16_bf16 v[96:111], v[116:119], a[164:167], v[96:111]\n"
    "ds_read_b128 v[140:143], v172 offset:15360\n"
    "s_waitcnt lgkmcnt(4)\n"
    "v_mfma_f32_32x32x16_bf16 v[96:111], v[120:123], a[168:171], v[96:111]\n"
    "s_waitcnt vmcnt(10)\n"
    "s_barrier\n"
    "ds_read_b128 v[112:115], v172 offset:16384\n"
    "buffer_load_dwordx4 v2, s[52:55], s10 offen offset:2048 sc1 lds\n"
    "v_mfma_f32_32x32x16_bf16 v[96:111], v[124:127], a[172:175], v[96:111]\n"
    "ds_read_b128 v[116:119], v172 offset:17408\n"
    "s_waitcnt lgkmcnt(4)\n"
    "v_mfma_f32_32x32x16_bf16 v[96:111], v[128:131], a[176:179], v[96:111]\n"
    "ds_read_b128 v[120:123], v172 offset:18432\n"
    "buffer_load_dwordx4 v2, s[52:55], s10 offen offset:3072 sc1 lds\n"
    "s_xor_b32 s10, s8, 0xc000\n"
    "v_mfma_f32_32x32x16_bf16 v[96:111], v[132:135], a[180:183], v[96:111]\n"
    "ds_read_b128 v[124:127], v172 offset:19456\n"
    "s_waitcnt lgkmcnt(4)\n"
    "v_mfma_f32_32x32x16_bf16 v[96:111], v[136:139], a[184:187], v[96:111]\n"
    "ds_read_b128 v[128:131], v172 offset:20480\n"
    "v_mfma_f32_32x32x16_bf16 v[96:111], v[140:143], a[188:191], v[96:111]\n"
    "ds_read_b128 v[132:135], v172 offset:21504\n"
    "s_add_u32 m0, s9, 0x24000\n"
    "s_nop 0\n"
    "buffer_load_dwordx4 v2, s[52:55], s10 offen sc1 lds\n"
    "s_waitcnt lgkmcnt(4)\n"
    "v_mfma_f32_32x32x16_bf16 v[96:111], v[112:115], a[192:195], v[96:111]\n"
    "ds_read_b128 v[136:139], v172 offset:22528\n"
    "v_mfma_f32_32x32x16_bf16 v[96:111], v[116:119], a[196:199], v[96:111]\n"
    "ds_read_b128 v[140:143], v172 offset:23552\n"
    "buffer_load_dwordx4 v2, s[52:55], s10 offen offset:1024 sc1 lds\n"
    "s_waitcnt lgkmcnt(4)\n"
    "v_mfma_f32_32x32x16_bf16 v[96:111], v[120:123], a[200:203], v[96:111]\n"
    "ds_read_b128 v[112:115], v172 offset:24576\n"
    "v_mfma_f32_32x32x16_bf16 v[96:111], v[124:127], a[204:207], v[96:111]\n"
    "ds_read_b128 v[116:119], v172 offset:25600\n"
    "s_waitcnt lgkmcnt(4)\n"
    "v_mfma_f32_32x32x16_bf16 v[96:111], v[128:131], a[208:211], v[96:111]\n"
    "ds_read_b128 v[120:123], v172 offset:26624\n"
    "buffer_load_dwordx4 v2, s[52:55], s10 offen offset:2048 sc1 lds\n"
    "v_mfma_f32_32x32x16_bf16 v[96:111], v[132:135], a[212:215], v[96:111]\n"
    "ds_read_b128 v[124:127], v172 offset:27648\n"
    "s_waitcnt lgkmcnt(4)\n"
    "v_mfma_f32_32x32x16_bf16 v[96:111], v[136:139], a[216:219], v[96:111]\n"
    "ds_read_b128 v[128:131], v172 offset:28672\n"
    "buffer_load_dwordx4 v2, s[52:55], s10 offen offset:3072 sc1 lds\n"
    "v_mfma_f32_32x32x16_bf16 v[96:111], v[140:143], a[220:223], v[96:111]\n"
    "ds_read_b128 v[132:135], v172 offset:29696\n"
    "s_waitcnt lgkmcnt(4)\n"
    "v_mfma_f32_32x32x16_bf16 v[96:111], v[112:115], a[224:227], v[96:111]\n"
    "ds_read_b128 v[136:139], v172 offset:30720\n"
    "v_mfma_f32_32x32x16_bf16 v[96:111], v[116:119], a[228:231], v[96:111]\n"
    "ds_read_b128 v[140:143], v172 offset:31744\n"
    "buffer_load_dwordx4 v[64:67], v2, s[52:55], s16 offen sc1\n"
    "s_waitcnt lgkmcnt(4)\n"
    "v_mfma_f32_32x32x16_bf16 v[96:111], v[120:123], a[232:235], v[96:111]\n"
    "s_waitcnt vmcnt(13)\n"
    "s_barrier\n"
    "ds_read_b128 v[112:115], v172 offset:32768\n"
    "v_mfma_f32_32x32x16_bf16 v[96:111], v[124:127], a[236:239], v[96:111]\n"
    "ds_read_b128 v[116:119], v172 offset:33792\n"
    "buffer_load_dwordx4 v[68:71], v174, s[52:55], s16 offen sc1\n"
    "s_waitcnt lgkmcnt(4)\n"
    "v_mfma_f32_32x32x16_bf16 v[96:111], v[128:131], a[240:243], v[96:111]\n"
    "ds_read_b128 v[120:123], v172 offset:34816\n"
    "v_mfma_f32_32x32x16_bf16 v[96:111], v[132:135], a[244:247], v[96:111]\n"
    "ds_read_b128 v[124:127], v172 offset:35840\n"
    "s_waitcnt lgkmcnt(4)\n"
    "v_mfma_f32_32x32x16_bf16 v[96:111], v[136:139], a[248:251], v[96:111]\n"
    "ds_read_b128 v[128:131], v172 offset:36864\n"
    "buffer_load_dwordx4 v[72:75], v175, s[52:55], s16 offen sc1\n"
    "v_mfma_f32_32x32x16_bf16 v[96:111], v[140:143], a[252:255], v[96:111]\n"
    "ds_read_b128 v[132:135], v172 offset:37888\n"
    "s_waitcnt lgkmcnt(4)\n"
    "v_mfma_f32_32x32x16_bf16 v[24:39], v[112:115], a[0:3], v[24:39]\n"
    "ds_read_b128 v[136:139], v172 offset:38912\n"
    "buffer_load_dwordx4 v[80:83], v2, s[52:55], s17 offen sc1\n"
    "v_mfma_f32_32x32x16_bf16 v[24:39], v[116:119], a[4:7], v[24:39]\n"
    "ds_read_b128 v[140:143], v172 offset:39936\n"
    "s_nop 4\n"
    "v_cvt_pk_bf16_f32 v40, v96, v97\n"
    "v_cvt_pk_bf16_f32 v41, v98, v99\n"
    "v_cvt_pk_bf16_f32 v42, v100, v101\n"
    "s_waitcnt lgkmcnt(4)\n"
    "v_mfma_f32_32x32x16_bf16 v[24:39], v[120:123], a[8:11], v[24:39]\n"
    "ds_read_b128 v[112:115], v172 offset:40960\n"
    "v_mfma_f32_32x32x16_bf16 v[24:39], v[124:127], a[12:15], v[24:39]\n"
    "ds_read_b128 v[116:119], v172 offset:41984\n"
    "buffer_store_dwordx4 v[40:43], v2, s[52:55], s15 offen sc1\n"
    "v_cvt_pk_bf16_f32 v44, v102, v103\n"
    "v_cvt_pk_bf16_f32 v45, v104, v105\n"
    "v_cvt_pk_bf16_f32 v46, v106, v107\n"
    "s_waitcnt lgkmcnt(4)\n"
    "v_mfma_f32_32x32x16_bf16 v[24:39], v[128:131], a[16:19], v[24:39]\n"
    "ds_read_b128 v[120:123], v172 offset:43008\n"
    "v_mfma_f32_32x32x16_bf16 v[24:39], v[132:135], a[20:23], v[24:39]\n"
    "ds_read_b128 v[124:127], v172 offset:44032\n"
    "buffer_store_dwordx4 v[44:47], v174, s[52:55], s15 offen sc1\n"
    "v_cvt_pk_bf16_f32 v48, v108, v109\n"
    "v_cvt_pk_bf16_f32 v49, v110, v111\n"
    "s_waitcnt lgkmcnt(4)\n"
    "v_mfma_f32_32x32x16_bf16 v[24:39], v[136:139], a[24:27], v[24:39]\n"
    "ds_read_b128 v[128:131], v172 offset:45056\n"
    "v_mfma_f32_32x32x16_bf16 v[24:39], v[140:143], a[28:31], v[24:39]\n"
    "ds_read_b128 v[132:135], v172 offset:46080\n"
    "s_waitcnt lgkmcnt(4)\n"
    "v_mfma_f32_32x32x16_bf16 v[24:39], v[112:115], a[32:35], v[24:39]\n"
    "ds_read_b128 v[136:139], v172 offset:47104\n"
    "buffer_store_dwordx4 v[48:51], v175, s[52:55], s15 offen sc1\n"
    "v_mfma_f32_32x32x16_bf16 v[24:39], v[116:119], a[36:39], v[24:39]\n"
    "ds_read_b128 v[140:143], v172 offset:48128\n"
    "v_lshlrev_b32 v171, 2, v5\n"
    "s_mov_b32 exec_lo, 0xffff\n"
    "s_mov_b32 exec_hi, 0\n"
    "buffer_store_dword v4, v171, s[52:55], s81 offen sc1\n"
    "s_mov_b64 exec, -1\n"
    "s_waitcnt lgkmcnt(4)\n"
    "v_mfma_f32_32x32x16_bf16 v[24:39], v[120:123], a[40:43], v[24:39]\n"
    "s_waitcnt vmcnt(16)\n"
    "s_barrier\n"
    "ds_read_b128 v[112:115], v172 offset:49152\n"
    "buffer_load_dwordx4 v[84:87], v174, s[52:55], s17 offen sc1\n"
    "v_and_b32 v170, 15, v1\n"
    "v_lshlrev_b32 v170, 6, v170\n"
    "v_mfma_f32_32x32x16_bf16 v[24:39], v[124:127], a[44:47], v[24:39]\n"
    "ds_read_b128 v[116:119], v172 offset:50176\n"
    "s_waitcnt lgkmcnt(4)\n"
    "v_mfma_f32_32x32x16_bf16 v[24:39], v[128:131], a[48:51], v[24:39]\n"
    "ds_read_b128 v[120:123], v172 offset:51200\n"
    "v_mfma_f32_32x32x16_bf16 v[24:39], v[132:135], a[52:55], v[24:39]\n"
    "ds_read_b128 v[124:127], v172 offset:52224\n"
    "buffer_load_dwordx4 v[88:91], v175, s[52:55], s17 offen sc1\n"
    "s_waitcnt lgkmcnt(4)\n"
    "v_mfma_f32_32x32x16_bf16 v[24:39], v[136:139], a[56:59], v[24:39]\n"
    "ds_read_b128 v[128:131], v172 offset:53248\n"
    "v_mfma_f32_32x32x16_bf16 v[24:39], v[140:143], a[60:63], v[24:39]\n"
    "ds_read_b128 v[132:135], v172 offset:54272\n"
    "s_add_u32 m0, s72, 0\n"
    "s_nop 0\n"
    "buffer_load_dwordx4 v2, s[52:55], s89 offen sc1 lds\n"
    "s_waitcnt lgkmcnt(4)\n"
    "v_mfma_f32_32x32x16_bf16 v[24:39], v[112:115], a[64:67], v[24:39]\n"
    "ds_read_b128 v[136:139], v172 offset:55296\n"
    "v_mfma_f32_32x32x16_bf16 v[24:39], v[116:119], a[68:71], v[24:39]\n"
    "ds_read_b128 v[140:143], v172 offset:56320\n"
    "s_waitcnt lgkmcnt(4)\n"
    "v_mfma_f32_32x32x16_bf16 v[24:39], v[120:123], a[72:75], v[24:39]\n"
    "ds_read_b128 v[112:115], v172 offset:57344\n"
    "buffer_load_dwordx4 v2, s[52:55], s89 offen offset:1024 sc1 lds\n"
    "v_mfma_f32_32x32x16_bf16 v[24:39], v[124:127], a[76:79], v[24:39]\n"
    "ds_read_b128 v[116:119], v172 offset:58368\n"
    "s_waitcnt lgkmcnt(4)\n"
    "v_mfma_f32_32x32x16_bf16 v[24:39], v[128:131], a[80:83], v[24:39]\n"
    "ds_read_b128 v[120:123], v172 offset:59392\n"
    "buffer_load_dwordx4 v2, s[52:55], s89 offen offset:2048 sc1 lds\n"
    "v_mfma_f32_32x32x16_bf16 v[24:39], v[132:135], a[84:87], v[24:39]\n"
    "ds_read_b128 v[124:127], v172 offset:60416\n"
    "s_waitcnt lgkmcnt(4)\n"
    "v_mfma_f32_32x32x16_bf16 v[24:39], v[136:139], a[88:91], v[24:39]\n"
    "ds_read_b128 v[128:131], v172 offset:61440\n"
    "v_mfma_f32_32x32x16_bf16 v[24:39], v[140:143], a[92:95], v[24:39]\n"
    "ds_read_b128 v[132:135], v172 offset:62464\n"
    "buffer_load_dwordx4 v2, s[52:55], s89 offen offset:3072 sc1 lds\n"
    "s_add_u32 s89, s19, 0x1000\n"
    "s_waitcnt lgkmcnt(4)\n"
    "v_mfma_f32_32x32x16_bf16 v[24:39], v[112:115], a[96:99], v[24:39]\n"
    "ds_read_b128 v[136:139], v172 offset:63488\n"
    "v_mfma_f32_32x32x16_bf16 v[24:39], v[116:119], a[100:103], v[24:39]\n"
    "ds_read_b128 v[140:143], v172 offset:64512\n"
    "s_add_u32 m0, s72, 0x1000\n"
    "s_nop 0\n"
    "buffer_load_dwordx4 v2, s[52:55], s89 offen sc1 lds\n"
    "s_waitcnt lgkmcnt(4)\n"
    "v_mfma_f32_32x32x16_bf16 v[24:39], v[120:123], a[104:107], v[24:39]\n"
    "s_waitcnt vmcnt(19)\n"
    "s_barrier\n"
    "ds_read_b128 v[112:115], v173\n"
    "v_mfma_f32_32x32x16_bf16 v[24:39], v[124:127], a[108:111], v[24:39]\n"
    "ds_read_b128 v[116:119], v173 offset:1024\n"
    "s_waitcnt lgkmcnt(4)\n"
    "v_mfma_f32_32x32x16_bf16 v[24:39], v[128:131], a[112:115], v[24:39]\n"
    "ds_read_b128 v[120:123], v173 offset:2048\n"
    "s_lshr_b32 s79, s9, 2\n"
    "s_add_u32 s80, s80, s79\n"
    "s_add_u32 m0, s79, 0x19000\n"
    "s_nop 0\n"
    "buffer_load_dwordx4 v2, s[52:55], s80 offen sc1 lds\n"
    "v_mfma_f32_32x32x16_bf16 v[24:39], v[132:135], a[116:119], v[24:39]\n"
    "ds_read_b128 v[124:127], v173 offset:3072\n"
    "s_waitcnt vmcnt(15)\n"
    "v_xor_b32 v168, s66, v67\n"
    "s_waitcnt vmcnt(14)\n"
    "v_bitop3_b32 v168, v71, s66, v168 bitop3:0xbe\n"
    "s_waitcnt vmcnt(13)\n"
    "v_bitop3_b32 v168, v75, s66, v168 bitop3:0xbe\n"
    "v_cmp_ne_u32 vcc, 0, v168\n"
    "s_cbranch_vccnz Lslow13_%=\n"
    "Lback14_%=:\n"
    "s_waitcnt lgkmcnt(4)\n"
    "v_mfma_f32_32x32x16_bf16 v[24:39], v[136:139], a[120:123], v[24:39]\n"
    "ds_read_b128 v[128:131], v173 offset:4096\n"
    "s_add_u32 m0, s72, 0x1000\n"
    "s_nop 0\n"
    "buffer_load_dwordx4 v2, s[52:55], s89 offen offset:1024 sc1 lds\n"
    "v_mov_b32 v67, 0\n"
    "v_mov_b32 v71, 0\n"
    "v_mfma_f32_32x32x16_bf16 v[24:39], v[140:143], a[124:127], v[24:39]\n"
    "ds_read_b128 v[132:135], v173 offset:5120\n"
    "v_mov_b32 v75, 0\n"
    "s_waitcnt lgkmcnt(4)\n"
    "v_mfma_f32_32x32x16_bf16 v[24:39], v[112:115], a[128:131], v[24:39]\n"
    "ds_read_b128 v[136:139], v173 offset:6144\n"
    "v_mfma_f32_32x32x16_bf16 v[24:39], v[52:55], v[64:67], v[24:39]\n"
    "v_mfma_f32_32x32x16_bf16 v[24:39], v[116:119], a[132:135], v[24:39]\n"
    "ds_read_b128 v[140:143], v173 offset:7168\n"
    "buffer_load_dwordx4 v2, s[52:55], s89 offen offset:2048 sc1 lds\n"
    "s_waitcnt lgkmcnt(4)\n"
    "v_mfma_f32_32x32x16_bf16 v[24:39], v[120:123], a[136:139], v[24:39]\n"
    "ds_read_b128 v[112:115], v173 offset:8192\n"
    "v_mfma_f32_32x32x16_bf16 v[24:39], v[56:59], v[68:71], v[24:39]\n"
    "v_mfma_f32_32x32x16_bf16 v[24:39], v[124:127], a[140:143], v[24:39]\n"
    "ds_read_b128 v[116:119], v173 offset:9216\n"
    "buffer_load_dwordx4 v2, s[52:55], s89 offen offset:3072 sc1 lds\n"
    "s_add_u32 s89, s19, 0x8000\n"
    "s_waitcnt lgkmcnt(4)\n"
    "v_mfma_f32_32x32x16_bf16 v[24:39], v[128:131], a[144:147], v[24:39]\n"
    "ds_read_b128 v[120:123], v173 offset:10240\n"
    "v_mfma_f32_32x32x16_bf16 v[24:39], v[60:63], v[72:75], v[24:39]\n"
    "v_mfma_f32_32x32x16_bf16 v[24:39], v[132:135], a[148:151], v[24:39]\n"
    "ds_read_b128 v[124:127], v173 offset:11264\n"
    "s_waitcnt lgkmcnt(4)\n"
    "v_mfma_f32_32x32x16_bf16 v[24:39], v[136:139], a[152:155], v[24:39]\n"
    "ds_read_b128 v[128:131], v173 offset:12288\n"
    "s_add_u32 m0, s72, 0x8000\n"
    "s_nop 0\n"
    "buffer_load_dwordx4 v2, s[52:55], s89 offen sc1 lds\n"
    "v_mfma_f32_32x32x16_bf16 v[24:39], v[140:143], a[156:159], v[24:39]\n"
    "ds_read_b128 v[132:135], v173 offset:13312\n"
    "s_waitcnt lgkmcnt(4)\n"
    "v_mfma_f32_32x32x16_bf16 v[24:39], v[112:115], a[160:163], v[24:39]\n"
    "ds_read_b128 v[136:139], v173 offset:14336\n"
    "buffer_load_dwordx4 v2, s[52:55], s89 offen offset:1024 sc1 lds\n"
    "v_mfma_f32_32x32x16_bf16 v[24:39], v[116:119], a[164:167], v[24:39]\n"
    "ds_read_b128 v[140:143], v173 offset:15360\n"
    "s_waitcnt lgkmcnt(4)\n"
    "v_mfma_f32_32x32x16_bf16 v[24:39], v[120:123], a[168:171], v[24:39]\n"
    "s_barrier\n"
    "ds_read_b128 v[112:115], v173 offset:16384\n"
    "v_mfma_f32_32x32x16_bf16 v[24:39], v[124:127], a[172:175], v[24:39]\n"
    "ds_read_b128 v[116:119], v173 offset:17408\n"
    "buffer_load_dwordx4 v2, s[52:55], s89 offen offset:2048 sc1 lds\n"
    "s_waitcnt lgkmcnt(4)\n"
    "v_mfma_f32_32x32x16_bf16 v[24:39], v[128:131], a[176:179], v[24:39]\n"
    "ds_read_b128 v[120:123], v173 offset:18432\n"
    "s_waitcnt vmcnt(18)\n"
    "v_xor_b32 v168, s66, v83\n"
    "s_waitcnt vmcnt(13)\n"
    "v_bitop3_b32 v168, v87, s66, v168 bitop3:0xbe\n"
    "s_waitcnt vmcnt(12)\n"
    "v_bitop3_b32 v168, v91, s66, v168 bitop3:0xbe\n"
    "v_cmp_ne_u32 vcc, 0, v168\n"
    "s_cbranch_vccnz Lslow16_%=\n"
    "Lback17_%=:\n"
    "v_mfma_f32_32x32x16_bf16 v[24:39], v[132:135], a[180:183], v[24:39]\n"
    "ds_read_b128 v[124:127], v173 offset:19456\n"
    "buffer_load_dwordx4 v[96:99], v2, s[52:55], s18 offen sc1\n"
    "v_mov_b32 v83, 0\n"
    "v_mov_b32 v87, 0\n"
    "v_mov_b32 v91, 0\n"
    "s_waitcnt lgkmcnt(4)\n"
    "v_mfma_f32_32x32x16_bf16 v[24:39], v[136:139], a[184:187], v[24:39]\n"
    "ds_read_b128 v[128:131], v173 offset:20480\n"
    "v_mfma_f32_32x32x16_bf16 v[24:39], v[52:55], v[80:83], v[24:39]\n"
    "v_mfma_f32_32x32x16_bf16 v[24:39], v[140:143], a[188:191], v[24:39]\n"
    "ds_read_b128 v[132:135], v173 offset:21504\n"
    "v_mfma_f32_32x32x16_bf16 v[24:39], v[56:59], v[84:87], v[24:39]\n"
    "s_waitcnt lgkmcnt(4)\n"
    "v_mfma_f32_32x32x16_bf16 v[24:39], v[112:115], a[192:195], v[24:39]\n"
    "ds_read_b128 v[136:139], v173 offset:22528\n"
    "buffer_load_dwordx4 v[100:103], v174, s[52:55], s18 offen sc1\n"
    "buffer_load_dword v171, v170, s[52:55], s82 offen sc1\n"
    "v_mfma_f32_32x32x16_bf16 v[24:39], v[116:119], a[196:199], v[24:39]\n"
    "ds_read_b128 v[140:143], v173 offset:23552\n"
    "v_mfma_f32_32x32x16_bf16 v[24:39], v[60:63], v[88:91], v[24:39]\n"
    "s_waitcnt lgkmcnt(4)\n"
    "v_mfma_f32_32x32x16_bf16 v[24:39], v[120:123], a[200:203], v[24:39]\n"
    "ds_read_b128 v[112:115], v173 offset:24576\n"
    "buffer_load_dwordx4 v[104:107], v175, s[52:55], s18 offen sc1\n"
    "v_mfma_f32_32x32x16_bf16 v[24:39], v[124:127], a[204:207], v[24:39]\n"
    "ds_read_b128 v[116:119], v173 offset:25600\n"
    "s_waitcnt lgkmcnt(4)\n"
    "v_mfma_f32_32x32x16_bf16 v[24:39], v[128:131], a[208:211], v[24:39]\n"
    "ds_read_b128 v[120:123], v173 offset:26624\n"
    "v_mfma_f32_32x32x16_bf16 v[24:39], v[132:135], a[212:215], v[24:39]\n"
    "ds_read_b128 v[124:127], v173 offset:27648\n"
    "buffer_load_dwordx4 v2, s[52:55], s89 offen offset:3072 sc1 lds\n"
    "s_add_u32 s89, s19, 0x9000\n"
    "s_waitcnt lgkmcnt(4)\n"
    "v_mfma_f32_32x32x16_bf16 v[24:39], v[136:139], a[216:219], v[24:39]\n"
    "ds_read_b128 v[128:131], v173 offset:28672\n"
    "v_mfma_f32_32x32x16_bf16 v[24:39], v[140:143], a[220:223], v[24:39]\n"
    "ds_read_b128 v[132:135], v173 offset:29696\n"
    "s_add_u32 m0, s72, 0x9000\n"
    "s_nop 0\n"
    "buffer_load_dwordx4 v2, s[52:55], s89 offen sc1 lds\n"
    "s_waitcnt lgkmcnt(4)\n"
    "v_mfma_f32_32x32x16_bf16 v[24:39], v[112:115], a[224:227], v[24:39]\n"
    "ds_read_b128 v[136:139], v173 offset:30720\n"
    "v_mfma_f32_32x32x16_bf16 v[24:39], v[116:119], a[228:231], v[24:39]\n"
    "ds_read_b128 v[140:143], v173 offset:31744\n"
    "s_waitcnt lgkmcnt(4)\n"
    "v_mfma_f32_32x32x16_bf16 v[24:39], v[120:123], a[232:235], v[24:39]\n"
    "buffer_load_dwordx4 v2, s[52:55], s89 offen offset:1024 sc1 lds\n"
    "v_mfma_f32_32x32x16_bf16 v[24:39], v[124:127], a[236:239], v[24:39]\n"
    "s_waitcnt lgkmcnt(2)\n"
    "v_mfma_f32_32x32x16_bf16 v[24:39], v[128:131], a[240:243], v[24:39]\n"
    "buffer_load_dwordx4 v2, s[52:55], s89 offen offset:2048 sc1 lds\n"
    "v_mfma_f32_32x32x16_bf16 v[24:39], v[132:135], a[244:247], v[24:39]\n"
    "s_waitcnt lgkmcnt(0)\n"
    "v_mfma_f32_32x32x16_bf16 v[24:39], v[136:139], a[248:251], v[24:39]\n"
    "v_mfma_f32_32x32x16_bf16 v[24:39], v[140:143], a[252:255], v[24:39]\n"
    "buffer_load_dwordx4 v2, s[52:55], s89 offen offset:3072 sc1 lds\n"
    "s_add_u32 s89, s19, 0x10000\n"
    "s_waitcnt vmcnt(8)\n"
    "v_xor_b32 v168, s66, v99\n"
    "s_waitcnt vmcnt(7)\n"
    "v_bitop3_b32 v168, v103, s66, v168 bitop3:0xbe\n"
    "s_waitcnt vmcnt(5)\n"
    "v_bitop3_b32 v168, v107, s66, v168 bitop3:0xbe\n"
    "v_cmp_ne_u32 vcc, 0, v168\n"
    "s_cbranch_vccnz Lslow19_%=\n"
    "Lback20_%=:\n"
    "v_mov_b32 v99, 0\n"
    "v_mov_b32 v103, 0\n"
    "v_mov_b32 v107, 0\n"
    "v_mfma_f32_32x32x16_bf16 v[24:39], v[52:55], v[96:99], v[24:39]\n"
    "v_mfma_f32_32x32x16_bf16 v[24:39], v[56:59], v[100:103], v[24:39]\n"
    "v_mfma_f32_32x32x16_bf16 v[24:39], v[60:63], v[104:107], v[24:39]\n"
    "s_nop 11\n"
    "v_cvt_pk_bf16_f32 v155, v30, v31\n"
    "v_cvt_pk_bf16_f32 v152, v24, v25\n"
    "v_cvt_pk_bf16_f32 v153, v26, v27\n"
    "v_cvt_pk_bf16_f32 v154, v28, v29\n"
    "buffer_store_dwordx4 v[152:155], v2, s[52:55], s16 offen sc1\n"
    "v_cvt_pk_bf16_f32 v159, v38, v39\n"
    "v_cvt_pk_bf16_f32 v156, v32, v33\n"
    "v_cvt_pk_bf16_f32 v157, v34, v35\n"
    "v_cvt_pk_bf16_f32 v158, v36, v37\n"
    "buffer_store_dwordx4 v[156:159], v174, s[52:55], s16 offen sc1\n"
    "v_pk_mul_f32 v[80:81], v[24:25], v[24:25]\n"
    "v_pk_mul_f32 v[82:83], v[26:27], v[26:27]\n"
    "v_pk_add_f32 v[84:85], v[24:25], v[26:27]\n"
    "v_pk_add_f32 v[86:87], v[28:29], v[30:31]\n"
    "v_pk_fma_f32 v[80:81], v[28:29], v[28:29], v[80:81]\n"
    "v_pk_fma_f32 v[82:83], v[30:31], v[30:31], v[82:83]\n"
    "v_pk_fma_f32 v[80:81], v[32:33], v[32:33], v[80:81]\n"
    "v_pk_add_f32 v[84:85], v[84:85], v[32:33]\n"
    "v_pk_fma_f32 v[82:83], v[34:35], v[34:35], v[82:83]\n"
    "v_pk_add_f32 v[86:87], v[86:87], v[34:35]\n"
    "v_pk_fma_f32 v[80:81], v[36:37], v[36:37], v[80:81]\n"
    "v_pk_add_f32 v[84:85], v[84:85], v[36:37]\n"
    "v_pk_fma_f32 v[82:83], v[38:39], v[38:39], v[82:83]\n"
    "v_pk_add_f32 v[86:87], v[86:87], v[38:39]\n"
    "v_pk_add_f32 v[80:81], v[80:81], v[82:83]\n"
    "v_pk_add_f32 v[84:85], v[84:85], v[86:87]\n"
    "v_add_f32 v165, v80, v81\n"
    "v_add_f32 v164, v84, v85\n"
    "s_nop 1\n"
    "v_permlane32_swap_b32 v164, v165\n"
    "v_add_f32 v164, v164, v165\n"
    "v_mov_b32 v165, s66\n"
    "v_mov_b32 v166, v164\n"
    "v_mov_b32 v167, s66\n"
    "s_nop 0\n"
    "v_permlane32_swap_b32 v164, v166\n"
    "s_mov_b32 exec_hi, 0\n"
    "buffer_store_dwordx4 v[164:167], v2, s[52:55], s83 offen sc1\n"
    "s_mov_b32 exec_hi, -1\n"
    "s_add_u32 m0, s72, 0x10000\n"
    "s_nop 0\n"
    "buffer_load_dwordx4 v2, s[52:55], s89 offen sc1 lds\n"
    "buffer_load_dwordx4 v2, s[52:55], s89 offen offset:1024 sc1 lds\n"
    "buffer_load_dwordx4 v2, s[52:55], s89 offen offset:2048 sc1 lds\n"
    "buffer_load_dwordx4 v2, s[52:55], s89 offen offset:3072 sc1 lds\n"
    "s_add_u32 s89, s19, 0x11000\n"
    "s_add_u32 m0, s72, 0x11000\n"
    "s_nop 0\n"
    "buffer_load_dwordx4 v2, s[52:55], s89 offen sc1 lds\n"
    "buffer_load_dwordx4 v2, s[52:55], s89 offen offset:1024 sc1 lds\n"
    "buffer_load_dwordx4 v2, s[52:55], s89 offen offset:2048 sc1 lds\n"
    "buffer_load_dwordx4 v2, s[52:55], s89 offen offset:3072 sc1 lds\n"
    "v_cmp_ne_u32 vcc, s66, v171\n"
    "s_cbranch_vccnz Lslow22_%=\n"
    "Lback23_%=:\n"
    "s_barrier\n"
    "s_branch Ljoin2_%=\n"
    "Ly2_%=:\n"
    "s_xor_b32 s96, s27, 0x100\n"
    "s_add_u32 s97, s96, 0x80\n"
    "s_add_u32 m0, s72, 0x8000\n"
    "s_nop 0\n"
    "buffer_load_dwordx4 v8, s[36:39], s96 offen lds\n"
    "buffer_load_dwordx4 v9, s[36:39], s96 offen offset:1024 lds\n"
    "buffer_load_dwordx4 v10, s[36:39], s96 offen offset:2048 lds\n"
    "buffer_load_dwordx4 v11, s[36:39], s96 offen offset:3072 lds\n"
    "s_add_u32 m0, s72, 0x9000\n"
    "s_nop 0\n"
    "buffer_load_dwordx4 v8, s[36:39], s97 offen lds\n"
    "buffer_load_dwordx4 v9, s[36:39], s97 offen offset:1024 lds\n"
    "buffer_load_dwordx4 v10, s[36:39], s97 offen offset:2048 lds\n"
    "buffer_load_dwordx4 v11, s[36:39], s97 offen offset:3072 lds\n"
    "s_lshl_b32 s77, s65, 3\n"
    "s_add_u32 s77, s77, s68\n"
    "s_cmp_lt_u32 s77, 0x70\n"
    "s_cselect_b32 s78, -1, 0\n"
    "s_mul_i32 s82, s77, 0x93\n"
    "s_lshr_b32 s82, s82, 12\n"
    "s_mul_i32 s83, s82, 28\n"
    "s_sub_u32 s83, s77, s83\n"
    "s_lshl_b32 s84, s82, 2\n"
    "s_add_u32 s84, s84, s61\n"
    "s_mov_b32 s42, 0\n"
    "s_mov_b32 s43, 0x20000\n"
    "s_mov_b32 s46, 0\n"
    "s_mov_b32 s47, 0x20000\n"
    "s_and_b32 s42, s78, 0x1c0000\n"
    "s_mov_b32 s46, s42\n"
    "v_lshrrev_b32 v54, 3, v1\n"
    "v_mul_u32_u24 v51, 0xe00, v54\n"
    "v_and_b32 v52, 7, v1\n"
    "v_lshlrev_b32 v52, 4, v52\n"
    "v_lshlrev_b32 v53, 1, v1\n"
    "v_and_b32 v53, 32, v53\n"
    "v_xor_b32 v63, v52, v53\n"
    "v_add_u32 v62, v51, v63\n"
    "v_xor_b32 v66, 64, v62\n"
    "v_lshlrev_b32 v53, 1, v53\n"
    "v_xor_b32 v52, v52, v53\n"
    "v_lshl_add_u32 v70, v54, 10, v52\n"
    "v_add_u32 v63, 0x6c00, v62\n"
    "v_add_u32 v67, 0x6c00, v66\n"
    "v_add_u32 v71, 0x1c00, v70\n"
    "v_add_u32 v64, 0xd800, v62\n"
    "v_add_u32 v68, 0xd800, v66\n"
    "v_add_u32 v72, 0x3800, v70\n"
    "v_add_u32 v65, 0x14400, v62\n"
    "v_add_u32 v69, 0x14400, v66\n"
    "v_add_u32 v73, 0x5400, v70\n"
    "s_mul_i32 s85, s84, 0x1c000\n"
    "s_lshl_b32 s73, s83, 7\n"
    "s_add_u32 s85, s85, s73\n"
    "s_lshl_b32 s86, s83, 8\n"
    "s_lshl_b32 s73, s82, 6\n"
    "s_add_u32 s86, s86, s73\n"
    "s_add_u32 s86, s86, 0x481000\n"
    "s_lshl_b32 s90, s83, 17\n"
    "s_lshl_b32 s73, s84, 11\n"
    "s_add_u32 s90, s90, s73\n"
    "s_add_u32 s90, s90, 0x4c0000\n"
    "s_lshl_b32 s91, s83, 11\n"
    "s_lshl_b32 s73, s61, 9\n"
    "s_add_u32 s91, s91, s73\n"
    "s_lshl_b32 s73, s82, 2\n"
    "s_add_u32 s91, s91, s73\n"
    "s_add_u32 s91, s91, 0x4b0000\n"
    "s_and_b32 s80, s77, 7\n"
    "s_lshr_b32 s79, s77, 3\n"
    "s_lshl_b32 s79, s79, 2\n"
    "s_add_u32 s79, s79, s61\n"
    "s_lshl_b32 s81, s79, 15\n"
    "s_lshl_b32 s73, s80, 7\n"
    "s_add_u32 s81, s81, s73\n"
    "s_lshr_b32 s74, s80, 1\n"
    "s_and_b32 s75, s80, 1\n"
    "s_mul_i32 s92, s74, 0x70000\n"
    "s_lshl_b32 s73, s79, 13\n"
    "s_add_u32 s92, s92, s73\n"
    "s_lshl_b32 s73, s75, 11\n"
    "s_add_u32 s92, s92, s73\n"
    "s_add_u32 s92, s92, 0x840000\n"
    "s_mov_b32 s50, 0\n"
    "s_mov_b32 s51, 0x20000\n"
    "s_and_b32 s50, s78, 0x1c0000\n"
    "v_bfe_u32 v52, v1, 2, 2\n"
    "v_lshl_or_b32 v52, v6, 2, v52\n"
    "v_and_b32 v53, 3, v1\n"
    "v_lshlrev_b32 v53, 3, v53\n"
    "v_lshl_or_b32 v53, v52, 7, v53\n"
    "v_add_u32 v53, s72, v53\n"
    "v_bfe_u32 v54, v1, 4, 1\n"
    "v_bfe_u32 v51, v1, 3, 1\n"
    "v_lshl_or_b32 v52, v51, 1, v54\n"
    "v_lshl_or_b32 v78, v52, 5, v53\n"
    "v_or_b32 v78, 0x18000, v78\n"
    "v_xor_b32 v79, 64, v78\n"
    "v_lshl_or_b32 v52, v54, 1, v51\n"
    "v_lshl_or_b32 v52, v52, 5, v53\n"
    "v_lshl_or_b32 v52, v54, 12, v52\n"
    "v_xor_b32 v74, 0x20000, v52\n"
    "v_mov_b32 v224, 0\n"
    "v_xor_b32 v75, 0x20020, v52\n"
    "v_mov_b32 v80, 0\n"
    "v_xor_b32 v76, 0x20040, v52\n"
    "v_mov_b32 v81, 0\n"
    "v_xor_b32 v77, 0x20060, v52\n"
    "v_mov_b32 v82, 0\n"
    "s_add_u32 m0, s72, 0x20000\n"
    "s_nop 0\n"
    "buffer_load_dwordx4 v62, s[40:43], s85 offen nt lds\n"
    "buffer_load_dwordx4 v63, s[40:43], s85 offen offset:1024 nt lds\n"
    "buffer_load_dwordx4 v64, s[40:43], s85 offen offset:2048 nt lds\n"
    "buffer_load_dwordx4 v65, s[40:43], s85 offen offset:3072 nt lds\n"
    "s_add_u32 m0, s72, 0x21000\n"
    "s_nop 0\n"
    "buffer_load_dwordx4 v66, s[44:47], s85 offen nt lds\n"
    "buffer_load_dwordx4 v67, s[44:47], s85 offen offset:1024 nt lds\n"
    "buffer_load_dwordx4 v68, s[44:47], s85 offen offset:2048 nt lds\n"
    "buffer_load_dwordx4 v69, s[44:47], s85 offen offset:3072 nt lds\n"
    "s_waitcnt vmcnt(18)\n"
    "ds_read_b128 a[0:3], v12\n"
    "ds_read_b128 a[4:7], v13\n"
    "ds_read_b128 a[8:11], v14\n"
    "ds_read_b128 a[12:15], v15\n"
    "ds_read_b128 a[16:19], v12 offset:4096\n"
    "ds_read_b128 a[20:23], v13 offset:4096\n"
    "ds_read_b128 a[24:27], v14 offset:4096\n"
    "ds_read_b128 a[28:31], v15 offset:4096\n"
    "s_xor_b32 s96, s27, 0x200\n"
    "s_add_u32 s97, s96, 0x80\n"
    "s_add_u32 m0, s72, 0x10000\n"
    "s_nop 0\n"
    "buffer_load_dwordx4 v8, s[36:39], s96 offen lds\n"
    "buffer_load_dwordx4 v9, s[36:39], s96 offen offset:1024 lds\n"
    "buffer_load_dwordx4 v10, s[36:39], s96 offen offset:2048 lds\n"
    "buffer_load_dwordx4 v11, s[36:39], s96 offen offset:3072 lds\n"
    "s_add_u32 m0, s72, 0x11000\n"
    "s_nop 0\n"
    "buffer_load_dwordx4 v8, s[36:39], s97 offen lds\n"
    "buffer_load_dwordx4 v9, s[36:39], s97 offen offset:1024 lds\n"
    "buffer_load_dwordx4 v10, s[36:39], s97 offen offset:2048 lds\n"
    "buffer_load_dwordx4 v11, s[36:39], s97 offen offset:3072 lds\n"
    "s_waitcnt vmcnt(16)\n"
    "ds_read_b128 a[32:35], v12 offset:32768\n"
    "ds_read_b128 a[36:39], v13 offset:32768\n"
    "ds_read_b128 a[40:43], v14 offset:32768\n"
    "ds_read_b128 a[44:47], v15 offset:32768\n"
    "ds_read_b128 a[48:51], v12 offset:36864\n"
    "ds_read_b128 a[52:55], v13 offset:36864\n"
    "ds_read_b128 a[56:59], v14 offset:36864\n"
    "ds_read_b128 a[60:63], v15 offset:36864\n"
    "s_waitcnt lgkmcnt(0)\n"
    "s_xor_b32 s96, s27, 0x300\n"
    "s_add_u32 s97, s96, 0x80\n"
    "s_add_u32 m0, s72, 0x8000\n"
    "s_nop 0\n"
    "buffer_load_dwordx4 v8, s[36:39], s96 offen lds\n"
    "buffer_load_dwordx4 v9, s[36:39], s96 offen offset:1024 lds\n"
    "buffer_load_dwordx4 v10, s[36:39], s96 offen offset:2048 lds\n"
    "buffer_load_dwordx4 v11, s[36:39], s96 offen offset:3072 lds\n"
    "s_add_u32 m0, s72, 0x9000\n"
    "s_nop 0\n"
    "buffer_load_dwordx4 v8, s[36:39], s97 offen lds\n"
    "buffer_load_dwordx4 v9, s[36:39], s97 offen offset:1024 lds\n"
    "buffer_load_dwordx4 v10, s[36:39], s97 offen offset:2048 lds\n"
    "buffer_load_dwordx4 v11, s[36:39], s97 offen offset:3072 lds\n"
    "s_waitcnt vmcnt(16)\n"
    "ds_read_b64_tr_b16 v[176:177], v74\n"
    "ds_read_b64_tr_b16 v[178:179], v74 offset:1024\n"
    "ds_read_b64_tr_b16 v[180:181], v74 offset:2048\n"
    "ds_read_b64_tr_b16 v[182:183], v74 offset:3072\n"
    "ds_read_b64_tr_b16 v[184:185], v75\n"
    "ds_read_b64_tr_b16 v[186:187], v75 offset:1024\n"
    "ds_read_b64_tr_b16 v[188:189], v75 offset:2048\n"
    "ds_read_b64_tr_b16 v[190:191], v75 offset:3072\n"
    "ds_read_b64_tr_b16 v[192:193], v76\n"
    "ds_read_b64_tr_b16 v[194:195], v76 offset:1024\n"
    "ds_read_b64_tr_b16 v[196:197], v76 offset:2048\n"
    "ds_read_b64_tr_b16 v[198:199], v76 offset:3072\n"
    "ds_read_b64_tr_b16 v[200:201], v77\n"
    "ds_read_b64_tr_b16 v[202:203], v77 offset:1024\n"
    "ds_read_b64_tr_b16 v[204:205], v77 offset:2048\n"
    "ds_read_b64_tr_b16 v[206:207], v77 offset:3072\n"
    "s_mov_b64 s[40:41], s[52:53]\n"
    "s_and_b32 s42, s78, 0x1600000\n"
    "s_add_u32 s74, s72, 0x20000\n"
    "v_lshl_add_u32 v52, v5, 2, s74\n"
    "s_lshl_b32 s74, s61, 7\n"
    "s_add_u32 s74, s74, 0x20000\n"
    "v_lshl_add_u32 v53, v5, 2, s74\n"
    "s_waitcnt lgkmcnt(15)\n"
    "v_dot2c_f32_bf16 v224, s67, v176\n"
    "s_waitcnt lgkmcnt(11)\n"
    "v_dot2c_f32_bf16 v80, s67, v184\n"
    "v_dot2c_f32_bf16 v224, s67, v177\n"
    "v_dot2c_f32_bf16 v80, s67, v185\n"
    "v_dot2c_f32_bf16 v224, s67, v178\n"
    "s_waitcnt lgkmcnt(10)\n"
    "v_dot2c_f32_bf16 v80, s67, v186\n"
    "v_dot2c_f32_bf16 v224, s67, v179\n"
    "v_dot2c_f32_bf16 v80, s67, v187\n"
    "v_dot2c_f32_bf16 v224, s67, v180\n"
    "s_waitcnt lgkmcnt(9)\n"
    "v_dot2c_f32_bf16 v80, s67, v188\n"
    "v_dot2c_f32_bf16 v224, s67, v181\n"
    "v_dot2c_f32_bf16 v80, s67, v189\n"
    "v_dot2c_f32_bf16 v224, s67, v182\n"
    "s_waitcnt lgkmcnt(8)\n"
    "v_dot2c_f32_bf16 v80, s67, v190\n"
    "v_dot2c_f32_bf16 v224, s67, v183\n"
    "v_dot2c_f32_bf16 v80, s67, v191\n"
    "s_waitcnt lgkmcnt(7)\n"
    "v_dot2c_f32_bf16 v81, s67, v192\n"
    "s_waitcnt lgkmcnt(3)\n"
    "v_dot2c_f32_bf16 v82, s67, v200\n"
    "v_dot2c_f32_bf16 v81, s67, v193\n"
    "v_dot2c_f32_bf16 v82, s67, v201\n"
    "v_dot2c_f32_bf16 v81, s67, v194\n"
    "s_waitcnt lgkmcnt(2)\n"
    "v_dot2c_f32_bf16 v82, s67, v202\n"
    "v_dot2c_f32_bf16 v81, s67, v195\n"
    "v_dot2c_f32_bf16 v82, s67, v203\n"
    "v_dot2c_f32_bf16 v81, s67, v196\n"
    "s_waitcnt lgkmcnt(1)\n"
    "v_dot2c_f32_bf16 v82, s67, v204\n"
    "v_dot2c_f32_bf16 v81, s67, v197\n"
    "v_dot2c_f32_bf16 v82, s67, v205\n"
    "v_dot2c_f32_bf16 v81, s67, v198\n"
    "s_waitcnt lgkmcnt(0)\n"
    "v_dot2c_f32_bf16 v82, s67, v206\n"
    "v_dot2c_f32_bf16 v81, s67, v199\n"
    "v_dot2c_f32_bf16 v82, s67, v207\n"
    "v_mov_b32 v51, v224\n"
    "s_nop 1\n"
    "v_permlane32_swap_b32 v224, v51\n"
    "v_add_f32 v224, v224, v51\n"
    "ds_write_b32 v52, v224\n"
    "v_mov_b32 v51, v80\n"
    "s_nop 1\n"
    "v_permlane32_swap_b32 v80, v51\n"
    "v_add_f32 v80, v80, v51\n"
    "ds_write_b32 v52, v80 offset:128\n"
    "v_mov_b32 v51, v81\n"
    "s_nop 1\n"
    "v_permlane32_swap_b32 v81, v51\n"
    "v_add_f32 v81, v81, v51\n"
    "ds_write_b32 v52, v81 offset:256\n"
    "v_mov_b32 v51, v82\n"
    "s_nop 1\n"
    "v_permlane32_swap_b32 v82, v51\n"
    "v_add_f32 v82, v82, v51\n"
    "ds_write_b32 v52, v82 offset:384\n"
    "s_waitcnt lgkmcnt(0)\n"
    "s_barrier\n"
    "ds_read_b32 v224, v53\n"
    "ds_read_b32 v80, v53 offset:8192\n"
    "ds_read_b32 v81, v53 offset:16384\n"
    "ds_read_b32 v82, v53 offset:24576\n"
    "s_waitcnt lgkmcnt(1)\n"
    "v_add_f32 v224, v224, v81\n"
    "s_waitcnt lgkmcnt(0)\n"
    "v_add_f32 v80, v80, v82\n"
    "v_add_f32 v224, v224, v80\n"
    "v_cvt_pk_bf16_f32 v51, v224, v224\n"
    "v_and_b32 v51, 0xffff0000, v51\n"
    "v_sub_f32 v51, v224, v51\n"
    "v_cvt_pk_bf16_f32 v224, v224, v51\n"
    "s_xor_b32 s96, s27, 0x400\n"
    "s_add_u32 s97, s96, 0x80\n"
    "s_add_u32 m0, s72, 0\n"
    "s_nop 0\n"
    "buffer_load_dwordx4 v8, s[36:39], s96 offen lds\n"
    "buffer_load_dwordx4 v9, s[36:39], s96 offen offset:1024 lds\n"
    "buffer_load_dwordx4 v10, s[36:39], s96 offen offset:2048 lds\n"
    "buffer_load_dwordx4 v11, s[36:39], s96 offen offset:3072 lds\n"
    "s_add_u32 m0, s72, 0x1000\n"
    "s_nop 0\n"
    "buffer_load_dwordx4 v8, s[36:39], s97 offen lds\n"
    "buffer_load_dwordx4 v9, s[36:39], s97 offen offset:1024 lds\n"
    "buffer_load_dwordx4 v10, s[36:39], s97 offen offset:2048 lds\n"
    "buffer_load_dwordx4 v11, s[36:39], s97 offen offset:3072 lds\n"
    "s_waitcnt vmcnt(16)\n"
    "ds_read_b128 a[64:67], v16\n"
    "ds_read_b128 a[68:71], v17\n"
    "ds_read_b128 a[72:75], v18\n"
    "ds_read_b128 a[76:79], v19\n"
    "ds_read_b128 a[80:83], v16 offset:4096\n"
    "ds_read_b128 a[84:87], v17 offset:4096\n"
    "ds_read_b128 a[88:91], v18 offset:4096\n"
    "ds_read_b128 a[92:95], v19 offset:4096\n"
    "s_waitcnt vmcnt(8)\n"
    "ds_read_b128 a[96:99], v12 offset:32768\n"
    "ds_read_b128 a[100:103], v13 offset:32768\n"
    "ds_read_b128 a[104:107], v14 offset:32768\n"
    "ds_read_b128 a[108:111], v15 offset:32768\n"
    "ds_read_b128 a[112:115], v12 offset:36864\n"
    "ds_read_b128 a[116:119], v13 offset:36864\n"
    "ds_read_b128 a[120:123], v14 offset:36864\n"
    "ds_read_b128 a[124:127], v15 offset:36864\n"
    "s_waitcnt lgkmcnt(0)\n"
    "s_xor_b32 s96, s27, 0x500\n"
    "s_add_u32 s97, s96, 0x80\n"
    "s_add_u32 m0, s72, 0x8000\n"
    "s_nop 0\n"
    "buffer_load_dwordx4 v8, s[36:39], s96 offen lds\n"
    "buffer_load_dwordx4 v9, s[36:39], s96 offen offset:1024 lds\n"
    "buffer_load_dwordx4 v10, s[36:39], s96 offen offset:2048 lds\n"
    "buffer_load_dwordx4 v11, s[36:39], s96 offen offset:3072 lds\n"
    "s_add_u32 m0, s72, 0x9000\n"
    "s_nop 0\n"
    "buffer_load_dwordx4 v8, s[36:39], s97 offen lds\n"
    "buffer_load_dwordx4 v9, s[36:39], s97 offen offset:1024 lds\n"
    "buffer_load_dwordx4 v10, s[36:39], s97 offen offset:2048 lds\n"
    "buffer_load_dwordx4 v11, s[36:39], s97 offen offset:3072 lds\n"
    "v_add_u32 v172, 0x10000, v2\n"
    "v_add_u32 v173, 0x20000, v2\n"
    "s_and_b32 s76, s64, 2\n"
    "s_lshr_b32 s74, s63, 1\n"
    "s_and_b32 s75, s63, 1\n"
    "s_lshl_b32 s8, s74, 21\n"
    "s_lshl_b32 s73, s64, 19\n"
    "s_add_u32 s8, s8, s73\n"
    "s_lshl_b32 s73, s63, 17\n"
    "s_add_u32 s8, s8, s73\n"
    "s_lshl_b32 s73, s75, 16\n"
    "s_add_u32 s8, s8, s73\n"
    "s_lshl_b32 s73, s76, 14\n"
    "s_add_u32 s8, s8, s73\n"
    "s_lshl_b32 s73, s61, 12\n"
    "s_add_u32 s8, s8, s73\n"
    "s_lshl_b32 s9, s61, 12\n"
    "s_lshl_b32 s77, s74, 12\n"
    "s_lshl_b32 s73, s64, 10\n"
    "s_add_u32 s77, s77, s73\n"
    "s_lshl_b32 s73, s75, 9\n"
    "s_add_u32 s77, s77, s73\n"
    "s_add_u32 s77, s77, 0x488000\n"
    "s_cmp_lg_u32 s61, 0\n"
    "s_cbranch_scc1 Lskip25_%=\n"
    "v_and_b32 v168, 7, v1\n"
    "v_lshlrev_b32 v168, 6, v168\n"
    "s_mov_b32 s76, 0x30d40\n"
    "Lpoll27_%=:\n"
    "s_nop 0\n"
    "buffer_load_dword v169, v168, s[52:55], s77 offen sc1\n"
    "s_waitcnt vmcnt(0)\n"
    "v_cmp_ne_u32 vcc, s66, v169\n"
    "s_cbranch_vccz Ldone26_%=\n"
    "s_sleep 1\n"
    "s_sub_u32 s76, s76, 1\n"
    "s_cbranch_scc0 Lpoll27_%=\n"
    "Ldone26_%=:\n"
    "s_nop 0\n"
    "Lskip25_%=:\n"
    "s_nop 0\n"
    "s_barrier\n"
    "s_xor_b32 s10, s8, 0x20000\n"
    "s_add_u32 m0, s9, 0x18000\n"
    "s_nop 0\n"
    "buffer_load_dwordx4 v2, s[52:55], s10 offen sc1 lds\n"
    "buffer_load_dwordx4 v2, s[52:55], s10 offen offset:1024 sc1 lds\n"
    "buffer_load_dwordx4 v2, s[52:55], s10 offen offset:2048 sc1 lds\n"
    "buffer_load_dwordx4 v2, s[52:55], s10 offen offset:3072 sc1 lds\n"
    "s_xor_b32 s10, s8, 0x40000\n"
    "s_add_u32 m0, s9, 0x1c000\n"
    "s_nop 0\n"
    "buffer_load_dwordx4 v2, s[52:55], s10 offen sc1 lds\n"
    "buffer_load_dwordx4 v2, s[52:55], s10 offen offset:1024 sc1 lds\n"
    "buffer_load_dwordx4 v2, s[52:55], s10 offen offset:2048 sc1 lds\n"
    "buffer_load_dwordx4 v2, s[52:55], s10 offen offset:3072 sc1 lds\n"
    "s_xor_b32 s10, s8, 0x24000\n"
    "s_add_u32 m0, s9, 0x20000\n"
    "s_nop 0\n"
    "buffer_load_dwordx4 v2, s[52:55], s10 offen sc1 lds\n"
    "buffer_load_dwordx4 v2, s[52:55], s10 offen offset:1024 sc1 lds\n"
    "buffer_load_dwordx4 v2, s[52:55], s10 offen offset:2048 sc1 lds\n"
    "buffer_load_dwordx4 v2, s[52:55], s10 offen offset:3072 sc1 lds\n"
    "s_or_b32 s19, s64, 1\n"
    "s_xor_b32 s19, s61, s19\n"
    "s_lshl_b32 s19, s19, 13\n"
    "s_mul_i32 s73, s69, 0x38000\n"
    "s_add_u32 s19, s19, s73\n"
    "s_add_u32 s19, s19, 0x4c0000\n"
    "v_lshlrev_b32 v170, 10, v5\n"
    "v_lshl_or_b32 v170, v6, 4, v170\n"
    "s_lshl_b32 s74, s62, 17\n"
    "s_lshl_b32 s73, s61, 15\n"
    "s_add_u32 s74, s74, s73\n"
    "s_lshl_b32 s73, s64, 8\n"
    "s_add_u32 s74, s74, s73\n"
    "s_lshl_b32 s73, s63, 6\n"
    "s_add_u32 s74, s74, s73\n"
    "s_waitcnt vmcnt(8)\n"
    "s_barrier\n"
    "ds_read_b128 v[112:115], v172 offset:32768\n"
    "ds_read_b128 v[116:119], v172 offset:33792\n"
    "ds_read_b128 v[120:123], v172 offset:34816\n"
    "ds_read_b128 v[124:127], v172 offset:35840\n"
    "ds_read_b128 v[128:131], v172 offset:36864\n"
    "ds_read_b128 v[132:135], v172 offset:37888\n"
    "s_waitcnt lgkmcnt(5)\n"
    "v_mfma_f32_32x32x16_bf16 v[64:79], v[112:115], a[0:3], 0\n"
    "ds_read_b128 v[136:139], v172 offset:38912\n"
    "s_mov_b32 exec_hi, 0\n"
    "buffer_store_dword v224, v2, s[40:43], s91 offen sc1\n"
    "s_mov_b32 exec_hi, -1\n"
    "ds_read_b128 a[128:131], v12\n"
    "s_waitcnt lgkmcnt(6)\n"
    "v_mfma_f32_32x32x16_bf16 v[64:79], v[116:119], a[4:7], v[64:79]\n"
    "ds_read_b128 v[140:143], v172 offset:39936\n"
    "ds_read_b128 a[132:135], v13\n"
    "s_waitcnt lgkmcnt(6)\n"
    "v_mfma_f32_32x32x16_bf16 v[64:79], v[120:123], a[8:11], v[64:79]\n"
    "ds_read_b128 v[112:115], v172 offset:40960\n"
    "ds_read_b128 a[136:139], v14\n"
    "v_mfma_f32_32x32x16_bf16 v[64:79], v[124:127], a[12:15], v[64:79]\n"
    "ds_read_b128 v[116:119], v172 offset:41984\n"
    "buffer_store_dwordx4 v[176:179], v2, s[40:43], s90 offen sc1\n"
    "ds_read_b128 a[140:143], v15\n"
    "s_waitcnt lgkmcnt(8)\n"
    "v_mfma_f32_32x32x16_bf16 v[64:79], v[128:131], a[16:19], v[64:79]\n"
    "ds_read_b128 v[120:123], v172 offset:43008\n"
    "ds_read_b128 a[144:147], v12 offset:4096\n"
    "v_mov_b32 v43, s66\n"
    "v_mov_b32 v47, s66\n"
    "v_mov_b32 v50, 0\n"
    "v_mov_b32 v51, s66\n"
    "v_mov_b32 v55, 0\n"
    "v_mfma_f32_32x32x16_bf16 v[64:79], v[132:135], a[20:23], v[64:79]\n"
    "ds_read_b128 v[124:127], v172 offset:44032\n"
    "ds_read_b128 a[148:151], v13 offset:4096\n"
    "v_mov_b32 v59, 0\n"
    "v_mov_b32 v62, 0\n"
    "v_mov_b32 v63, 0\n"
    "v_lshrrev_b32 v169, 3, v5\n"
    "v_bfe_u32 v171, v5, 1, 1\n"
    "s_waitcnt lgkmcnt(9)\n"
    "v_mfma_f32_32x32x16_bf16 v[64:79], v[136:139], a[24:27], v[64:79]\n"
    "ds_read_b128 v[128:131], v172 offset:45056\n"
    "buffer_store_dwordx4 v[180:183], v2, s[40:43], s90 offen offset:1024 sc1\n"
    "s_add_u32 s90, s90, 0x8000\n"
    "ds_read_b128 a[152:155], v14 offset:4096\n"
    "v_lshl_or_b32 v169, v169, 1, v171\n"
    "v_bfe_u32 v171, v5, 2, 1\n"
    "v_mfma_f32_32x32x16_bf16 v[64:79], v[140:143], a[28:31], v[64:79]\n"
    "ds_read_b128 v[132:135], v172 offset:46080\n"
    "ds_read_b128 a[156:159], v15 offset:4096\n"
    "s_waitcnt lgkmcnt(0)\n"
    "s_xor_b32 s96, s27, 0x600\n"
    "s_add_u32 s97, s96, 0x80\n"
    "s_add_u32 m0, s72, 0\n"
    "s_nop 0\n"
    "buffer_load_dwordx4 v8, s[36:39], s96 offen lds\n"
    "v_mfma_f32_32x32x16_bf16 v[64:79], v[112:115], a[32:35], v[64:79]\n"
    "ds_read_b128 v[136:139], v172 offset:47104\n"
    "ds_read_b128 a[160:163], v12 offset:32768\n"
    "buffer_load_dwordx4 v9, s[36:39], s96 offen offset:1024 lds\n"
    "v_xor_b32 v171, v171, v6\n"
    "v_lshl_or_b32 v169, v171, 3, v169\n"
    "v_and_b32 v171, 1, v5\n"
    "v_mfma_f32_32x32x16_bf16 v[64:79], v[116:119], a[36:39], v[64:79]\n"
    "ds_read_b128 v[140:143], v172 offset:48128\n"
    "buffer_store_dwordx4 v[184:187], v2, s[40:43], s90 offen sc1\n"
    "ds_read_b128 a[164:167], v13 offset:32768\n"
    "v_lshlrev_b32 v171, 4, v171\n"
    "v_mov_b32 v52, 0x3f80\n"
    "v_lshlrev_b32 v171, v171, v52\n"
    "v_mfma_f32_32x32x16_bf16 v[64:79], v[120:123], a[40:43], v[64:79]\n"
    "s_waitcnt vmcnt(10)\n"
    "s_barrier\n"
    "ds_read_b128 v[112:115], v172 offset:49152\n"
    "s_xor_b32 s10, s8, 0x44000\n"
    "ds_read_b128 a[168:171], v14 offset:32768\n"
    "v_mfma_f32_32x32x16_bf16 v[64:79], v[124:127], a[44:47], v[64:79]\n"
    "ds_read_b128 v[116:119], v172 offset:50176\n"
    "s_add_u32 m0, s9, 0x24000\n"
    "s_nop 0\n"
    "buffer_load_dwordx4 v2, s[52:55], s10 offen sc1 lds\n"
    "ds_read_b128 a[172:175], v15 offset:32768\n"
    "v_mfma_f32_32x32x16_bf16 v[64:79], v[128:131], a[48:51], v[64:79]\n"
    "ds_read_b128 v[120:123], v172 offset:51200\n"
    "buffer_load_dwordx4 v2, s[52:55], s10 offen offset:1024 sc1 lds\n"
    "ds_read_b128 a[176:179], v12 offset:36864\n"
    "v_cmp_eq_u32 vcc, 0, v169\n"
    "s_nop 1\n"
    "v_cndmask_b32 v52, 0, v171, vcc\n"
    "v_mfma_f32_32x32x16_bf16 v[64:79], v[132:135], a[52:55], v[64:79]\n"
    "ds_read_b128 v[124:127], v172 offset:52224\n"
    "buffer_load_dwordx4 v2, s[52:55], s10 offen offset:2048 sc1 lds\n"
    "ds_read_b128 a[180:183], v13 offset:36864\n"
    "v_cmp_eq_u32 vcc, 1, v169\n"
    "s_nop 1\n"
    "v_cndmask_b32 v53, 0, v171, vcc\n"
    "s_waitcnt lgkmcnt(9)\n"
    "v_mfma_f32_32x32x16_bf16 v[64:79], v[136:139], a[56:59], v[64:79]\n"
    "ds_read_b128 v[128:131], v172 offset:53248\n"
    "ds_read_b128 a[184:187], v14 offset:36864\n"
    "v_cmp_eq_u32 vcc, 2, v169\n"
    "s_nop 1\n"
    "v_cndmask_b32 v54, 0, v171, vcc\n"
    "v_mfma_f32_32x32x16_bf16 v[64:79], v[140:143], a[60:63], v[64:79]\n"
    "ds_read_b128 v[132:135], v172 offset:54272\n"
    "buffer_load_dwordx4 v2, s[52:55], s10 offen offset:3072 sc1 lds\n"
    "ds_read_b128 a[188:191], v15 offset:36864\n"
    "v_cmp_eq_u32 vcc, 3, v169\n"
    "s_nop 1\n"
    "v_cndmask_b32 v56, 0, v171, vcc\n"
    "s_waitcnt lgkmcnt(9)\n"
    "v_mfma_f32_32x32x16_bf16 v[80:95], v[112:115], a[0:3], 0\n"
    "ds_read_b128 v[136:139], v172 offset:55296\n"
    "buffer_store_dwordx4 v[188:191], v2, s[40:43], s90 offen offset:1024 sc1\n"
    "s_add_u32 s90, s90, 0x8000\n"
    "v_cmp_eq_u32 vcc, 4, v169\n"
    "s_nop 1\n"
    "v_cndmask_b32 v57, 0, v171, vcc\n"
    "v_mfma_f32_32x32x16_bf16 v[80:95], v[116:119], a[4:7], v[80:95]\n"
    "ds_read_b128 v[140:143], v172 offset:56320\n"
    "buffer_store_dwordx4 v[192:195], v2, s[40:43], s90 offen sc1\n"
    "v_cmp_eq_u32 vcc, 5, v169\n"
    "s_nop 1\n"
    "v_cndmask_b32 v58, 0, v171, vcc\n"
    "s_waitcnt lgkmcnt(7)\n"
    "v_mfma_f32_32x32x16_bf16 v[80:95], v[120:123], a[8:11], v[80:95]\n"
    "ds_read_b128 v[112:115], v172 offset:57344\n"
    "v_cmp_eq_u32 vcc, 6, v169\n"
    "s_nop 1\n"
    "v_cndmask_b32 v60, 0, v171, vcc\n"
    "v_cmp_eq_u32 vcc, 7, v169\n"
    "s_nop 1\n"
    "v_cndmask_b32 v61, 0, v171, vcc\n"
    "v_mfma_f32_32x32x16_bf16 v[80:95], v[124:127], a[12:15], v[80:95]\n"
    "ds_read_b128 v[116:119], v172 offset:58368\n"
    "buffer_store_dwordx4 v[196:199], v2, s[40:43], s90 offen offset:1024 sc1\n"
    "s_add_u32 s90, s90, 0x8000\n"
    "s_lshl_b32 s70, s62, 2\n"
    "s_add_u32 s70, s70, s61\n"
    "s_lshl_b32 s71, s64, 2\n"
    "s_waitcnt lgkmcnt(5)\n"
    "v_mfma_f32_32x32x16_bf16 v[80:95], v[128:131], a[16:19], v[80:95]\n"
    "ds_read_b128 v[120:123], v172 offset:59392\n"
    "s_add_u32 m0, s72, 0\n"
    "s_nop 0\n"
    "buffer_load_dwordx4 v10, s[36:39], s96 offen offset:2048 lds\n"
    "s_add_u32 s71, s71, s63\n"
    "s_mul_i32 s85, s70, 0x30000\n"
    "v_mfma_f32_32x32x16_bf16 v[80:95], v[132:135], a[20:23], v[80:95]\n"
    "ds_read_b128 v[124:127], v172 offset:60416\n"
    "buffer_store_dwordx4 v[200:203], v2, s[40:43], s90 offen sc1\n"
    "s_add_u32 s85, s85, 0xa00000\n"
    "s_lshl_b32 s16, s71, 10\n"
    "s_add_u32 s16, s16, s85\n"
    "s_add_u32 s17, s16, 0xc000\n"
    "s_waitcnt lgkmcnt(4)\n"
    "v_mfma_f32_32x32x16_bf16 v[80:95], v[136:139], a[24:27], v[80:95]\n"
    "ds_read_b128 v[128:131], v172 offset:61440\n"
    "buffer_load_dwordx4 v11, s[36:39], s96 offen offset:3072 lds\n"
    "s_add_u32 s18, s16, 0x18000\n"
    "s_xor_b32 s11, s16, 0x400\n"
    "s_xor_b32 s12, s17, 0x800\n"
    "s_xor_b32 s15, s18, 0xc00\n"
    "v_mfma_f32_32x32x16_bf16 v[80:95], v[140:143], a[28:31], v[80:95]\n"
    "ds_read_b128 v[132:135], v172 offset:62464\n"
    "v_add_u32 v174, 0x4000, v2\n"
    "v_add_u32 v175, 0x8000, v2\n"
    "s_lshl_b32 s82, s70, 10\n"
    "s_add_u32 s82, s82, 0x490000\n"
    "s_lshl_b32 s81, s71, 6\n"
    "s_add_u32 s81, s81, s82\n"
    "s_waitcnt lgkmcnt(4)\n"
    "v_mfma_f32_32x32x16_bf16 v[80:95], v[112:115], a[32:35], v[80:95]\n"
    "ds_read_b128 v[136:139], v172 offset:63488\n"
    "buffer_store_dwordx4 v[204:207], v2, s[40:43], s90 offen offset:1024 sc1\n"
    "s_add_u32 s90, s90, 0x8000\n"
    "s_lshl_b32 s83, s70, 13\n"
    "s_lshl_b32 s73, s71, 9\n"
    "s_add_u32 s83, s83, s73\n"
    "v_mfma_f32_32x32x16_bf16 v[80:95], v[116:119], a[36:39], v[80:95]\n"
    "ds_read_b128 v[140:143], v172 offset:64512\n"
    "s_add_u32 m0, s72, 0x1000\n"
    "s_nop 0\n"
    "buffer_load_dwordx4 v8, s[36:39], s97 offen lds\n"
    "s_add_u32 s83, s83, 0x400000\n"
    "s_waitcnt lgkmcnt(4)\n"
    "v_mfma_f32_32x32x16_bf16 v[80:95], v[120:123], a[40:43], v[80:95]\n"
    "s_waitcnt vmcnt(18)\n"
    "s_barrier\n"
    "ds_read_b128 v[112:115], v173\n"
    "s_xor_b32 s10, s8, 0x28000\n"
    "s_add_u32 m0, s9, 0x10000\n"
    "s_nop 0\n"
    "buffer_load_dwordx4 v2, s[52:55], s10 offen sc1 lds\n"
    "v_mfma_f32_32x32x16_bf16 v[80:95], v[124:127], a[44:47], v[80:95]\n"
    "ds_read_b128 v[116:119], v173 offset:1024\n"
    "s_waitcnt lgkmcnt(4)\n"
    "v_mfma_f32_32x32x16_bf16 v[80:95], v[128:131], a[48:51], v[80:95]\n"
    "ds_read_b128 v[120:123], v173 offset:2048\n"
    "buffer_load_dwordx4 v2, s[52:55], s10 offen offset:1024 sc1 lds\n"
    "v_mfma_f32_32x32x16_bf16 v[80:95], v[132:135], a[52:55], v[80:95]\n"
    "ds_read_b128 v[124:127], v173 offset:3072\n"
    "buffer_load_dwordx4 v2, s[52:55], s10 offen offset:2048 sc1 lds\n"
    "s_waitcnt lgkmcnt(4)\n"
    "v_mfma_f32_32x32x16_bf16 v[80:95], v[136:139], a[56:59], v[80:95]\n"
    "ds_read_b128 v[128:131], v173 offset:4096\n"
    "buffer_load_dwordx4 v2, s[52:55], s10 offen offset:3072 sc1 lds\n"
    "v_mfma_f32_32x32x16_bf16 v[80:95], v[140:143], a[60:63], v[80:95]\n"
    "ds_read_b128 v[132:135], v173 offset:5120\n"
    "s_waitcnt lgkmcnt(4)\n"
    "v_mfma_f32_32x32x16_bf16 v[64:79], v[112:115], a[64:67], v[64:79]\n"
    "ds_read_b128 v[136:139], v173 offset:6144\n"
    "s_add_u32 m0, s72, 0x1000\n"
    "s_nop 0\n"
    "buffer_load_dwordx4 v9, s[36:39], s97 offen offset:1024 lds\n"
    "v_mfma_f32_32x32x16_bf16 v[64:79], v[116:119], a[68:71], v[64:79]\n"
    "ds_read_b128 v[140:143], v173 offset:7168\n"
    "buffer_load_dwordx4 v10, s[36:39], s97 offen offset:2048 lds\n"
    "s_waitcnt lgkmcnt(4)\n"
    "v_mfma_f32_32x32x16_bf16 v[64:79], v[120:123], a[72:75], v[64:79]\n"
    "ds_read_b128 v[112:115], v173 offset:8192\n"
    "buffer_load_dwordx4 v11, s[36:39], s97 offen offset:3072 lds\n"
    "s_xor_b32 s96, s27, 0x700\n"
    "s_add_u32 s97, s96, 0x80\n"
    "v_mfma_f32_32x32x16_bf16 v[64:79], v[124:127], a[76:79], v[64:79]\n"
    "ds_read_b128 v[116:119], v173 offset:9216\n"
    "s_add_u32 m0, s72, 0x8000\n"
    "s_nop 0\n"
    "buffer_load_dwordx4 v8, s[36:39], s96 offen lds\n"
    "s_waitcnt lgkmcnt(4)\n"
    "v_mfma_f32_32x32x16_bf16 v[64:79], v[128:131], a[80:83], v[64:79]\n"
    "ds_read_b128 v[120:123], v173 offset:10240\n"
    "v_mfma_f32_32x32x16_bf16 v[64:79], v[132:135], a[84:87], v[64:79]\n"
    "ds_read_b128 v[124:127], v173 offset:11264\n"
    "buffer_load_dwordx4 v9, s[36:39], s96 offen offset:1024 lds\n"
    "s_waitcnt lgkmcnt(4)\n"
    "v_mfma_f32_32x32x16_bf16 v[64:79], v[136:139], a[88:91], v[64:79]\n"
    "ds_read_b128 v[128:131], v173 offset:12288\n"
    "buffer_load_dwordx4 v10, s[36:39], s96 offen offset:2048 lds\n"
    "v_mfma_f32_32x32x16_bf16 v[64:79], v[140:143], a[92:95], v[64:79]\n"
    "ds_read_b128 v[132:135], v173 offset:13312\n"
    "buffer_load_dwordx4 v11, s[36:39], s96 offen offset:3072 lds\n"
    "s_waitcnt lgkmcnt(4)\n"
    "v_mfma_f32_32x32x16_bf16 v[64:79], v[112:115], a[96:99], v[64:79]\n"
    "ds_read_b128 v[136:139], v173 offset:14336\n"
    "v_mfma_f32_32x32x16_bf16 v[64:79], v[116:119], a[100:103], v[64:79]\n"
    "ds_read_b128 v[140:143], v173 offset:15360\n"
    "s_add_u32 m0, s72, 0x9000\n"
    "s_nop 0\n"
    "buffer_load_dwordx4 v8, s[36:39], s97 offen lds\n"
    "s_waitcnt lgkmcnt(4)\n"
    "v_mfma_f32_32x32x16_bf16 v[64:79], v[120:123], a[104:107], v[64:79]\n"
    "s_waitcnt vmcnt(20)\n"
    "s_waitcnt vmcnt(13)\n"
    "s_barrier\n"
    "ds_read_b128 v[112:115], v173 offset:16384\n"
    "s_xor_b32 s10, s8, 0x48000\n"
    "s_add_u32 m0, s9, 0x14000\n"
    "s_nop 0\n"
    "buffer_load_dwordx4 v2, s[52:55], s10 offen sc1 lds\n"
    "v_mfma_f32_32x32x16_bf16 v[64:79], v[124:127], a[108:111], v[64:79]\n"
    "ds_read_b128 v[116:119], v173 offset:17408\n"
    "buffer_load_dwordx4 v2, s[52:55], s10 offen offset:1024 sc1 lds\n"
    "s_waitcnt lgkmcnt(4)\n"
    "v_mfma_f32_32x32x16_bf16 v[64:79], v[128:131], a[112:115], v[64:79]\n"
    "ds_read_b128 v[120:123], v173 offset:18432\n"
    "v_lshlrev_b32 v168, 2, v5\n"
    "s_and_b32 exec_lo, s24, 0xffff\n"
    "s_mov_b32 exec_hi, 0\n"
    "buffer_store_dword v4, v168, s[40:43], s86 offen sc1\n"
    "s_mov_b64 exec, -1\n"
    "v_mfma_f32_32x32x16_bf16 v[64:79], v[132:135], a[116:119], v[64:79]\n"
    "ds_read_b128 v[124:127], v173 offset:19456\n"
    "buffer_load_dwordx4 v2, s[52:55], s10 offen offset:2048 sc1 lds\n"
    "s_waitcnt lgkmcnt(4)\n"
    "v_mfma_f32_32x32x16_bf16 v[64:79], v[136:139], a[120:123], v[64:79]\n"
    "ds_read_b128 v[128:131], v173 offset:20480\n"
    "buffer_load_dwordx4 v2, s[52:55], s10 offen offset:3072 sc1 lds\n"
    "v_mfma_f32_32x32x16_bf16 v[64:79], v[140:143], a[124:127], v[64:79]\n"
    "ds_read_b128 v[132:135], v173 offset:21504\n"
    "s_add_u32 m0, s72, 0x9000\n"
    "s_nop 0\n"
    "buffer_load_dwordx4 v9, s[36:39], s97 offen offset:1024 lds\n"
    "s_waitcnt lgkmcnt(4)\n"
    "v_mfma_f32_32x32x16_bf16 v[80:95], v[112:115], a[64:67], v[80:95]\n"
    "ds_read_b128 v[136:139], v173 offset:22528\n"
    "buffer_load_dwordx4 v10, s[36:39], s97 offen offset:2048 lds\n"
    "v_mfma_f32_32x32x16_bf16 v[80:95], v[116:119], a[68:71], v[80:95]\n"
    "ds_read_b128 v[140:143], v173 offset:23552\n"
    "s_waitcnt lgkmcnt(4)\n"
    "v_mfma_f32_32x32x16_bf16 v[80:95], v[120:123], a[72:75], v[80:95]\n"
    "ds_read_b128 v[112:115], v173 offset:24576\n"
    "buffer_load_dwordx4 v11, s[36:39], s97 offen offset:3072 lds\n"
    "v_mfma_f32_32x32x16_bf16 v[80:95], v[124:127], a[76:79], v[80:95]\n"
    "ds_read_b128 v[116:119], v173 offset:25600\n"
    "s_mov_b32 s97, 0x481000\n"
    "s_mul_i32 s78, s69, 0x1c0\n"
    "s_add_u32 s79, s78, 0x180\n"
    "s_and_b32 s78, s78, 0xffffff00\n"
    "s_or_b32 s79, s79, 0xc0\n"
    "v_lshl_add_u32 v161, v1, 6, s78\n"
    "s_waitcnt lgkmcnt(4)\n"
    "v_mfma_f32_32x32x16_bf16 v[80:95], v[128:131], a[80:83], v[80:95]\n"
    "ds_read_b128 v[120:123], v173 offset:26624\n"
    "v_min_u32 v161, s79, v161\n"
    "v_lshlrev_b32 v162, 4, v5\n"
    "s_mul_i32 s80, s69, 0xe00\n"
    "s_add_u32 s80, s80, 0x4b0000\n"
    "s_waitcnt vmcnt(12)\n"
    "ds_read_b128 a[192:195], v12\n"
    "v_mfma_f32_32x32x16_bf16 v[80:95], v[132:135], a[84:87], v[80:95]\n"
    "ds_read_b128 v[124:127], v173 offset:27648\n"
    "ds_read_b128 a[196:199], v13\n"
    "buffer_load_dwordx4 v[144:147], v170, s[28:31], s74 offen\n"
    "s_waitcnt lgkmcnt(6)\n"
    "v_mfma_f32_32x32x16_bf16 v[80:95], v[136:139], a[88:91], v[80:95]\n"
    "ds_read_b128 v[128:131], v173 offset:28672\n"
    "ds_read_b128 a[200:203], v14\n"
    "v_mfma_f32_32x32x16_bf16 v[80:95], v[140:143], a[92:95], v[80:95]\n"
    "ds_read_b128 v[132:135], v173 offset:29696\n"
    "ds_read_b128 a[204:207], v15\n"
    "buffer_load_dwordx4 v[148:151], v170, s[28:31], s74 offen offset:32\n"
    "s_waitcnt lgkmcnt(8)\n"
    "v_mfma_f32_32x32x16_bf16 v[80:95], v[112:115], a[96:99], v[80:95]\n"
    "ds_read_b128 v[136:139], v173 offset:30720\n"
    "ds_read_b128 a[208:211], v12 offset:4096\n"
    "v_mfma_f32_32x32x16_bf16 v[80:95], v[116:119], a[100:103], v[80:95]\n"
    "ds_read_b128 v[140:143], v173 offset:31744\n"
    "ds_read_b128 a[212:215], v13 offset:4096\n"
    "s_waitcnt lgkmcnt(9)\n"
    "v_mfma_f32_32x32x16_bf16 v[80:95], v[120:123], a[104:107], v[80:95]\n"
    "s_barrier\n"
    "ds_read_b128 v[112:115], v172\n"
    "s_xor_b32 s10, s8, 0x2c000\n"
    "s_add_u32 m0, s9, 0x18000\n"
    "s_nop 0\n"
    "buffer_load_dwordx4 v2, s[52:55], s10 offen sc1 lds\n"
    "v_mfma_f32_32x32x16_bf16 v[80:95], v[124:127], a[108:111], v[80:95]\n"
    "ds_read_b128 v[116:119], v172 offset:1024\n"
    "ds_read_b128 a[216:219], v14 offset:4096\n"
    "s_waitcnt lgkmcnt(8)\n"
    "v_mfma_f32_32x32x16_bf16 v[80:95], v[128:131], a[112:115], v[80:95]\n"
    "ds_read_b128 v[120:123], v172 offset:2048\n"
    "buffer_load_dwordx4 v2, s[52:55], s10 offen offset:1024 sc1 lds\n"
    "ds_read_b128 a[220:223], v15 offset:4096\n"
    "v_mfma_f32_32x32x16_bf16 v[80:95], v[132:135], a[116:119], v[80:95]\n"
    "ds_read_b128 v[124:127], v172 offset:3072\n"
    "s_waitcnt lgkmcnt(7)\n"
    "v_mfma_f32_32x32x16_bf16 v[80:95], v[136:139], a[120:123], v[80:95]\n"
    "ds_read_b128 v[128:131], v172 offset:4096\n"
    "v_mfma_f32_32x32x16_bf16 v[80:95], v[140:143], a[124:127], v[80:95]\n"
    "ds_read_b128 v[132:135], v172 offset:5120\n"
    "buffer_load_dwordx4 v2, s[52:55], s10 offen offset:2048 sc1 lds\n"
    "s_waitcnt lgkmcnt(6)\n"
    "v_mfma_f32_32x32x16_bf16 v[64:79], v[112:115], a[128:131], v[64:79]\n"
    "ds_read_b128 v[136:139], v172 offset:6144\n"
    "v_mfma_f32_32x32x16_bf16 v[64:79], v[116:119], a[132:135], v[64:79]\n"
    "ds_read_b128 v[140:143], v172 offset:7168\n"
    "buffer_load_dwordx4 v2, s[52:55], s10 offen offset:3072 sc1 lds\n"
    "s_waitcnt lgkmcnt(4)\n"
    "v_mfma_f32_32x32x16_bf16 v[64:79], v[120:123], a[136:139], v[64:79]\n"
    "ds_read_b128 v[112:115], v172 offset:8192\n"
    "v_mfma_f32_32x32x16_bf16 v[64:79], v[124:127], a[140:143], v[64:79]\n"
    "ds_read_b128 v[116:119], v172 offset:9216\n"
    "s_waitcnt lgkmcnt(4)\n"
    "v_mfma_f32_32x32x16_bf16 v[64:79], v[128:131], a[144:147], v[64:79]\n"
    "ds_read_b128 v[120:123], v172 offset:10240\n"
    "s_waitcnt vmcnt(6)\n"
    "ds_read_b128 a[224:227], v12 offset:32768\n"
    "v_mfma_f32_32x32x16_bf16 v[64:79], v[132:135], a[148:151], v[64:79]\n"
    "ds_read_b128 v[124:127], v172 offset:11264\n"
    "ds_read_b128 a[228:231], v13 offset:32768\n"
    "s_waitcnt lgkmcnt(6)\n"
    "v_mfma_f32_32x32x16_bf16 v[64:79], v[136:139], a[152:155], v[64:79]\n"
    "ds_read_b128 v[128:131], v172 offset:12288\n"
    "ds_read_b128 a[232:235], v14 offset:32768\n"
    "v_mfma_f32_32x32x16_bf16 v[64:79], v[140:143], a[156:159], v[64:79]\n"
    "ds_read_b128 v[132:135], v172 offset:13312\n"
    "ds_read_b128 a[236:239], v15 offset:32768\n"
    "s_waitcnt lgkmcnt(8)\n"
    "v_mfma_f32_32x32x16_bf16 v[64:79], v[112:115], a[160:163], v[64:79]\n"
    "ds_read_b128 v[136:139], v172 offset:14336\n"
    "ds_read_b128 a[240:243], v12 offset:36864\n"
    "v_mfma_f32_32x32x16_bf16 v[64:79], v[116:119], a[164:167], v[64:79]\n"
    "ds_read_b128 v[140:143], v172 offset:15360\n"
    "ds_read_b128 a[244:247], v13 offset:36864\n"
    "s_waitcnt lgkmcnt(9)\n"
    "v_mfma_f32_32x32x16_bf16 v[64:79], v[120:123], a[168:171], v[64:79]\n"
    "s_barrier\n"
    "ds_read_b128 v[112:115], v172 offset:16384\n"
    "s_xor_b32 s10, s8, 0x4c000\n"
    "s_add_u32 m0, s9, 0x1c000\n"
    "s_nop 0\n"
    "buffer_load_dwordx4 v2, s[52:55], s10 offen sc1 lds\n"
    "v_mfma_f32_32x32x16_bf16 v[64:79], v[124:127], a[172:175], v[64:79]\n"
    "ds_read_b128 v[116:119], v172 offset:17408\n"
    "ds_read_b128 a[248:251], v14 offset:36864\n"
    "s_waitcnt lgkmcnt(8)\n"
    "v_mfma_f32_32x32x16_bf16 v[64:79], v[128:131], a[176:179], v[64:79]\n"
    "ds_read_b128 v[120:123], v172 offset:18432\n"
    "buffer_load_dwordx4 v2, s[52:55], s10 offen offset:1024 sc1 lds\n"
    "ds_read_b128 a[252:255], v15 offset:36864\n"
    "v_mfma_f32_32x32x16_bf16 v[64:79], v[132:135], a[180:183], v[64:79]\n"
    "ds_read_b128 v[124:127], v172 offset:19456\n"
    "s_waitcnt lgkmcnt(7)\n"
    "v_mfma_f32_32x32x16_bf16 v[64:79], v[136:139], a[184:187], v[64:79]\n"
    "ds_read_b128 v[128:131], v172 offset:20480\n"
    "v_mfma_f32_32x32x16_bf16 v[64:79], v[140:143], a[188:191], v[64:79]\n"
    "ds_read_b128 v[132:135], v172 offset:21504\n"
    "buffer_load_dwordx4 v2, s[52:55], s10 offen offset:2048 sc1 lds\n"
    "s_waitcnt vmcnt(8)\n"
    "v_permlane32_swap_b32 v144, v146\n"
    "v_permlane32_swap_b32 v145, v147\n"
    "s_waitcnt lgkmcnt(6)\n"
    "v_mfma_f32_32x32x16_bf16 v[80:95], v[112:115], a[128:131], v[80:95]\n"
    "ds_read_b128 v[136:139], v172 offset:22528\n"
    "s_waitcnt vmcnt(7)\n"
    "v_permlane32_swap_b32 v148, v150\n"
    "v_permlane32_swap_b32 v149, v151\n"
    "v_lshlrev_b32 v24, 16, v144\n"
    "v_and_b32 v25, 0xffff0000, v144\n"
    "v_lshlrev_b32 v26, 16, v145\n"
    "v_mfma_f32_32x32x16_bf16 v[80:95], v[116:119], a[132:135], v[80:95]\n"
    "ds_read_b128 v[140:143], v172 offset:23552\n"
    "buffer_load_dwordx4 v2, s[52:55], s10 offen offset:3072 sc1 lds\n"
    "v_and_b32 v27, 0xffff0000, v145\n"
    "v_lshlrev_b32 v28, 16, v146\n"
    "v_and_b32 v29, 0xffff0000, v146\n"
    "v_lshlrev_b32 v30, 16, v147\n"
    "s_waitcnt lgkmcnt(4)\n"
    "v_mfma_f32_32x32x16_bf16 v[80:95], v[120:123], a[136:139], v[80:95]\n"
    "ds_read_b128 v[112:115], v172 offset:24576\n"
    "v_and_b32 v31, 0xffff0000, v147\n"
    "v_lshlrev_b32 v32, 16, v148\n"
    "v_and_b32 v33, 0xffff0000, v148\n"
    "v_lshlrev_b32 v34, 16, v149\n"
    "v_and_b32 v35, 0xffff0000, v149\n"
    "v_lshlrev_b32 v36, 16, v150\n"
    "v_mfma_f32_32x32x16_bf16 v[80:95], v[124:127], a[140:143], v[80:95]\n"
    "ds_read_b128 v[116:119], v172 offset:25600\n"
    "v_and_b32 v37, 0xffff0000, v150\n"
    "v_lshlrev_b32 v38, 16, v151\n"
    "v_and_b32 v39, 0xffff0000, v151\n"
    "s_waitcnt lgkmcnt(4)\n"
    "v_mfma_f32_32x32x16_bf16 v[80:95], v[128:131], a[144:147], v[80:95]\n"
    "ds_read_b128 v[120:123], v172 offset:26624\n"
    "v_mfma_f32_32x32x16_bf16 v[80:95], v[132:135], a[148:151], v[80:95]\n"
    "ds_read_b128 v[124:127], v172 offset:27648\n"
    "s_waitcnt lgkmcnt(4)\n"
    "v_mfma_f32_32x32x16_bf16 v[80:95], v[136:139], a[152:155], v[80:95]\n"
    "ds_read_b128 v[128:131], v172 offset:28672\n"
    "v_mfma_f32_32x32x16_bf16 v[80:95], v[140:143], a[156:159], v[80:95]\n"
    "ds_read_b128 v[132:135], v172 offset:29696\n"
    "s_waitcnt lgkmcnt(4)\n"
    "v_mfma_f32_32x32x16_bf16 v[80:95], v[112:115], a[160:163], v[80:95]\n"
    "ds_read_b128 v[136:139], v172 offset:30720\n"
    "v_mfma_f32_32x32x16_bf16 v[80:95], v[116:119], a[164:167], v[80:95]\n"
    "ds_read_b128 v[140:143], v172 offset:31744\n"
    "s_waitcnt lgkmcnt(4)\n"
    "v_mfma_f32_32x32x16_bf16 v[80:95], v[120:123], a[168:171], v[80:95]\n"
    "s_waitcnt vmcnt(4)\n"
    "s_barrier\n"
    "ds_read_b128 v[112:115], v172 offset:32768\n"
    "s_xor_b32 s10, s8, 0x60000\n"
    "s_add_u32 m0, s9, 0x20000\n"
    "s_nop 0\n"
    "buffer_load_dwordx4 v2, s[52:55], s10 offen sc1 lds\n"
    "v_mfma_f32_32x32x16_bf16 v[80:95], v[124:127], a[172:175], v[80:95]\n"
    "ds_read_b128 v[116:119], v172 offset:33792\n"
    "s_waitcnt lgkmcnt(4)\n"
    "v_mfma_f32_32x32x16_bf16 v[80:95], v[128:131], a[176:179], v[80:95]\n"
    "ds_read_b128 v[120:123], v172 offset:34816\n"
    "buffer_load_dwordx4 v2, s[52:55], s10 offen offset:1024 sc1 lds\n"
    "v_mfma_f32_32x32x16_bf16 v[80:95], v[132:135], a[180:183], v[80:95]\n"
    "ds_read_b128 v[124:127], v172 offset:35840\n"
    "s_waitcnt lgkmcnt(4)\n"
    "v_mfma_f32_32x32x16_bf16 v[80:95], v[136:139], a[184:187], v[80:95]\n"
    "ds_read_b128 v[128:131], v172 offset:36864\n"
    "v_mfma_f32_32x32x16_bf16 v[80:95], v[140:143], a[188:191], v[80:95]\n"
    "ds_read_b128 v[132:135], v172 offset:37888\n"
    "buffer_load_dwordx4 v2, s[52:55], s10 offen offset:2048 sc1 lds\n"
    "s_waitcnt lgkmcnt(4)\n"
    "v_mfma_f32_32x32x16_bf16 v[64:79], v[112:115], a[192:195], v[64:79]\n"
    "ds_read_b128 v[136:139], v172 offset:38912\n"
    "buffer_load_dword v160, v161, s[52:55], s97 offen sc1\n"
    "v_mfma_f32_32x32x16_bf16 v[64:79], v[116:119], a[196:199], v[64:79]\n"
    "ds_read_b128 v[140:143], v172 offset:39936\n"
    "buffer_load_dwordx4 v2, s[52:55], s10 offen offset:3072 sc1 lds\n"
    "s_xor_b32 s10, s8, 0x64000\n"
    "s_waitcnt lgkmcnt(4)\n"
    "v_mfma_f32_32x32x16_bf16 v[64:79], v[120:123], a[200:203], v[64:79]\n"
    "ds_read_b128 v[112:115], v172 offset:40960\n"
    "v_mfma_f32_32x32x16_bf16 v[64:79], v[124:127], a[204:207], v[64:79]\n"
    "ds_read_b128 v[116:119], v172 offset:41984\n"
    "s_waitcnt lgkmcnt(4)\n"
    "v_mfma_f32_32x32x16_bf16 v[64:79], v[128:131], a[208:211], v[64:79]\n"
    "ds_read_b128 v[120:123], v172 offset:43008\n"
    "s_add_u32 m0, s9, 0x24000\n"
    "s_nop 0\n"
    "buffer_load_dwordx4 v2, s[52:55], s10 offen sc1 lds\n"
    "v_mfma_f32_32x32x16_bf16 v[64:79], v[132:135], a[212:215], v[64:79]\n"
    "ds_read_b128 v[124:127], v172 offset:44032\n"
    "s_waitcnt lgkmcnt(4)\n"
    "v_mfma_f32_32x32x16_bf16 v[64:79], v[136:139], a[216:219], v[64:79]\n"
    "ds_read_b128 v[128:131], v172 offset:45056\n"
    "buffer_load_dwordx4 v2, s[52:55], s10 offen offset:1024 sc1 lds\n"
    "v_mfma_f32_32x32x16_bf16 v[64:79], v[140:143], a[220:223], v[64:79]\n"
    "ds_read_b128 v[132:135], v172 offset:46080\n"
    "s_waitcnt lgkmcnt(4)\n"
    "v_mfma_f32_32x32x16_bf16 v[64:79], v[112:115], a[224:227], v[64:79]\n"
    "ds_read_b128 v[136:139], v172 offset:47104\n"
    "v_mfma_f32_32x32x16_bf16 v[64:79], v[116:119], a[228:231], v[64:79]\n"
    "ds_read_b128 v[140:143], v172 offset:48128\n"
    "buffer_load_dwordx4 v2, s[52:55], s10 offen offset:2048 sc1 lds\n"
    "s_waitcnt lgkmcnt(4)\n"
    "v_mfma_f32_32x32x16_bf16 v[64:79], v[120:123], a[232:235], v[64:79]\n"
    "s_waitcnt vmcnt(8)\n"
    "s_barrier\n"
    "ds_read_b128 v[112:115], v172 offset:49152\n"
    "v_mfma_f32_32x32x16_bf16 v[64:79], v[124:127], a[236:239], v[64:79]\n"
    "ds_read_b128 v[116:119], v172 offset:50176\n"
    "buffer_load_dwordx4 v2, s[52:55], s10 offen offset:3072 sc1 lds\n"
    "s_xor_b32 s10, s8, 0x68000\n"
    "s_waitcnt lgkmcnt(4)\n"
    "v_mfma_f32_32x32x16_bf16 v[64:79], v[128:131], a[240:243], v[64:79]\n"
    "ds_read_b128 v[120:123], v172 offset:51200\n"
    "v_mfma_f32_32x32x16_bf16 v[64:79], v[132:135], a[244:247], v[64:79]\n"
    "ds_read_b128 v[124:127], v172 offset:52224\n"
    "s_waitcnt lgkmcnt(4)\n"
    "v_mfma_f32_32x32x16_bf16 v[64:79], v[136:139], a[248:251], v[64:79]\n"
    "ds_read_b128 v[128:131], v172 offset:53248\n"
    "s_add_u32 m0, s9, 0x10000\n"
    "s_nop 0\n"
    "buffer_load_dwordx4 v2, s[52:55], s10 offen sc1 lds\n"
    "v_mfma_f32_32x32x16_bf16 v[64:79], v[140:143], a[252:255], v[64:79]\n"
    "ds_read_b128 v[132:135], v172 offset:54272\n"
    "s_waitcnt lgkmcnt(4)\n"
    "v_mfma_f32_32x32x16_bf16 v[80:95], v[112:115], a[192:195], v[80:95]\n"
    "ds_read_b128 v[136:139], v172 offset:55296\n"
    "buffer_load_dwordx4 v2, s[52:55], s10 offen offset:1024 sc1 lds\n"
    "v_mfma_f32_32x32x16_bf16 v[80:95], v[116:119], a[196:199], v[80:95]\n"
    "ds_read_b128 v[140:143], v172 offset:56320\n"
    "s_nop 4\n"
    "v_cvt_pk_bf16_f32 v40, v64, v65\n"
    "v_cvt_pk_bf16_f32 v41, v66, v67\n"
    "v_cvt_pk_bf16_f32 v42, v68, v69\n"
    "s_waitcnt lgkmcnt(4)\n"
    "v_mfma_f32_32x32x16_bf16 v[80:95], v[120:123], a[200:203], v[80:95]\n"
    "ds_read_b128 v[112:115], v172 offset:57344\n"
    "v_mfma_f32_32x32x16_bf16 v[80:95], v[124:127], a[204:207], v[80:95]\n"
    "ds_read_b128 v[116:119], v172 offset:58368\n"
    "buffer_store_dwordx4 v[40:43], v2, s[52:55], s11 offen sc1\n"
    "v_cvt_pk_bf16_f32 v44, v70, v71\n"
    "v_cvt_pk_bf16_f32 v45, v72, v73\n"
    "v_cvt_pk_bf16_f32 v46, v74, v75\n"
    "s_waitcnt lgkmcnt(4)\n"
    "v_mfma_f32_32x32x16_bf16 v[80:95], v[128:131], a[208:211], v[80:95]\n"
    "ds_read_b128 v[120:123], v172 offset:59392\n"
    "v_mfma_f32_32x32x16_bf16 v[80:95], v[132:135], a[212:215], v[80:95]\n"
    "ds_read_b128 v[124:127], v172 offset:60416\n"
    "buffer_store_dwordx4 v[44:47], v174, s[52:55], s11 offen sc1\n"
    "v_cvt_pk_bf16_f32 v48, v76, v77\n"
    "v_cvt_pk_bf16_f32 v49, v78, v79\n"
    "s_waitcnt lgkmcnt(4)\n"
    "v_mfma_f32_32x32x16_bf16 v[80:95], v[136:139], a[216:219], v[80:95]\n"
    "ds_read_b128 v[128:131], v172 offset:61440\n"
    "v_mfma_f32_32x32x16_bf16 v[80:95], v[140:143], a[220:223], v[80:95]\n"
    "ds_read_b128 v[132:135], v172 offset:62464\n"
    "s_waitcnt lgkmcnt(4)\n"
    "v_mfma_f32_32x32x16_bf16 v[80:95], v[112:115], a[224:227], v[80:95]\n"
    "ds_read_b128 v[136:139], v172 offset:63488\n"
    "buffer_store_dwordx4 v[48:51], v175, s[52:55], s11 offen sc1\n"
    "s_waitcnt vmcnt(10)\n"
    "v_cmp_ne_u32 vcc, s66, v160\n"
    "s_cbranch_vccnz Lslow28_%=\n"
    "Lback29_%=:\n"
    "s_add_u32 s89, s19, 0\n"
    "v_mfma_f32_32x32x16_bf16 v[80:95], v[116:119], a[228:231], v[80:95]\n"
    "ds_read_b128 v[140:143], v172 offset:64512\n"
    "s_waitcnt lgkmcnt(4)\n"
    "v_mfma_f32_32x32x16_bf16 v[80:95], v[120:123], a[232:235], v[80:95]\n"
    "s_waitcnt vmcnt(9)\n"
    "s_barrier\n"
    "ds_read_b128 v[112:115], v173\n"
    "buffer_load_dwordx4 v2, s[52:55], s10 offen offset:2048 sc1 lds\n"
    "v_mfma_f32_32x32x16_bf16 v[80:95], v[124:127], a[236:239], v[80:95]\n"
    "ds_read_b128 v[116:119], v173 offset:1024\n"
    "s_waitcnt lgkmcnt(4)\n"
    "v_mfma_f32_32x32x16_bf16 v[80:95], v[128:131], a[240:243], v[80:95]\n"
    "ds_read_b128 v[120:123], v173 offset:2048\n"
    "v_mfma_f32_32x32x16_bf16 v[80:95], v[132:135], a[244:247], v[80:95]\n"
    "ds_read_b128 v[124:127], v173 offset:3072\n"
    "buffer_load_dwordx4 v2, s[52:55], s10 offen offset:3072 sc1 lds\n"
    "s_xor_b32 s10, s8, 0x6c000\n"
    "s_waitcnt lgkmcnt(4)\n"
    "v_mfma_f32_32x32x16_bf16 v[80:95], v[136:139], a[248:251], v[80:95]\n"
    "ds_read_b128 v[128:131], v173 offset:4096\n"
    "v_mfma_f32_32x32x16_bf16 v[80:95], v[140:143], a[252:255], v[80:95]\n"
    "ds_read_b128 v[132:135], v173 offset:5120\n"
    "s_add_u32 m0, s9, 0x14000\n"
    "s_nop 0\n"
    "buffer_load_dwordx4 v2, s[52:55], s10 offen sc1 lds\n"
    "s_waitcnt lgkmcnt(4)\n"
    "v_mfma_f32_32x32x16_bf16 v[96:111], v[112:115], a[0:3], 0\n"
    "ds_read_b128 v[136:139], v173 offset:6144\n"
    "v_mfma_f32_32x32x16_bf16 v[96:111], v[116:119], a[4:7], v[96:111]\n"
    "ds_read_b128 v[140:143], v173 offset:7168\n"
    "s_nop 2\n"
    "v_cvt_pk_bf16_f32 v40, v80, v81\n"
    "v_cvt_pk_bf16_f32 v41, v82, v83\n"
    "v_cvt_pk_bf16_f32 v42, v84, v85\n"
    "s_waitcnt lgkmcnt(4)\n"
    "v_mfma_f32_32x32x16_bf16 v[96:111], v[120:123], a[8:11], v[96:111]\n"
    "ds_read_b128 v[112:115], v173 offset:8192\n"
    "buffer_store_dwordx4 v[40:43], v2, s[52:55], s12 offen sc1\n"
    "v_cvt_pk_bf16_f32 v44, v86, v87\n"
    "v_cvt_pk_bf16_f32 v45, v88, v89\n"
    "v_cvt_pk_bf16_f32 v46, v90, v91\n"
    "v_mfma_f32_32x32x16_bf16 v[96:111], v[124:127], a[12:15], v[96:111]\n"
    "ds_read_b128 v[116:119], v173 offset:9216\n"
    "s_waitcnt lgkmcnt(4)\n"
    "v_mfma_f32_32x32x16_bf16 v[96:111], v[128:131], a[16:19], v[96:111]\n"
    "ds_read_b128 v[120:123], v173 offset:10240\n"
    "buffer_store_dwordx4 v[44:47], v174, s[52:55], s12 offen sc1\n"
    "v_cvt_pk_bf16_f32 v48, v92, v93\n"
    "v_cvt_pk_bf16_f32 v49, v94, v95\n"
    "v_mfma_f32_32x32x16_bf16 v[96:111], v[132:135], a[20:23], v[96:111]\n"
    "ds_read_b128 v[124:127], v173 offset:11264\n"
    "s_waitcnt lgkmcnt(4)\n"
    "v_mfma_f32_32x32x16_bf16 v[96:111], v[136:139], a[24:27], v[96:111]\n"
    "ds_read_b128 v[128:131], v173 offset:12288\n"
    "v_mfma_f32_32x32x16_bf16 v[96:111], v[140:143], a[28:31], v[96:111]\n"
    "ds_read_b128 v[132:135], v173 offset:13312\n"
    "buffer_store_dwordx4 v[48:51], v175, s[52:55], s12 offen sc1\n"
    "s_waitcnt lgkmcnt(4)\n"
    "v_mfma_f32_32x32x16_bf16 v[96:111], v[112:115], a[32:35], v[96:111]\n"
    "ds_read_b128 v[136:139], v173 offset:14336\n"
    "v_mfma_f32_32x32x16_bf16 v[96:111], v[116:119], a[36:39], v[96:111]\n"
    "ds_read_b128 v[140:143], v173 offset:15360\n"
    "buffer_load_dwordx4 v2, s[52:55], s10 offen offset:1024 sc1 lds\n"
    "s_waitcnt lgkmcnt(4)\n"
    "v_mfma_f32_32x32x16_bf16 v[96:111], v[120:123], a[40:43], v[96:111]\n"
    "s_waitcnt vmcnt(12)\n"
    "s_barrier\n"
    "ds_read_b128 v[112:115], v173 offset:16384\n"
    "v_mfma_f32_32x32x16_bf16 v[96:111], v[124:127], a[44:47], v[96:111]\n"
    "ds_read_b128 v[116:119], v173 offset:17408\n"
    "s_waitcnt lgkmcnt(4)\n"
    "v_mfma_f32_32x32x16_bf16 v[96:111], v[128:131], a[48:51], v[96:111]\n"
    "ds_read_b128 v[120:123], v173 offset:18432\n"
    "buffer_load_dwordx4 v2, s[52:55], s10 offen offset:2048 sc1 lds\n"
    "v_mfma_f32_32x32x16_bf16 v[96:111], v[132:135], a[52:55], v[96:111]\n"
    "ds_read_b128 v[124:127], v173 offset:19456\n"
    "s_waitcnt lgkmcnt(4)\n"
    "v_mfma_f32_32x32x16_bf16 v[96:111], v[136:139], a[56:59], v[96:111]\n"
    "ds_read_b128 v[128:131], v173 offset:20480\n"
    "buffer_load_dwordx4 v2, s[52:55], s10 offen offset:3072 sc1 lds\n"
    "s_xor_b32 s10, s8, 0\n"
    "v_mfma_f32_32x32x16_bf16 v[96:111], v[140:143], a[60:63], v[96:111]\n"
    "ds_read_b128 v[132:135], v173 offset:21504\n"
    "s_waitcnt lgkmcnt(4)\n"
    "v_mfma_f32_32x32x16_bf16 v[96:111], v[112:115], a[64:67], v[96:111]\n"
    "ds_read_b128 v[136:139], v173 offset:22528\n"
    "v_mfma_f32_32x32x16_bf16 v[96:111], v[116:119], a[68:71], v[96:111]\n"
    "ds_read_b128 v[140:143], v173 offset:23552\n"
    "s_add_u32 m0, s9, 0x18000\n"
    "s_nop 0\n"
    "buffer_load_dwordx4 v2, s[52:55], s10 offen sc1 lds\n"
    "s_waitcnt lgkmcnt(4)\n"
    "v_mfma_f32_32x32x16_bf16 v[96:111], v[120:123], a[72:75], v[96:111]\n"
    "ds_read_b128 v[112:115], v173 offset:24576\n"
    "v_mfma_f32_32x32x16_bf16 v[96:111], v[124:127], a[76:79], v[96:111]\n"
    "ds_read_b128 v[116:119], v173 offset:25600\n"
    "buffer_load_dwordx4 v2, s[52:55], s10 offen offset:1024 sc1 lds\n"
    "s_waitcnt lgkmcnt(4)\n"
    "v_mfma_f32_32x32x16_bf16 v[96:111], v[128:131], a[80:83], v[96:111]\n"
    "ds_read_b128 v[120:123], v173 offset:26624\n"
    "v_mfma_f32_32x32x16_bf16 v[96:111], v[132:135], a[84:87], v[96:111]\n"
    "ds_read_b128 v[124:127], v173 offset:27648\n"
    "s_waitcnt lgkmcnt(4)\n"
    "v_mfma_f32_32x32x16_bf16 v[96:111], v[136:139], a[88:91], v[96:111]\n"
    "ds_read_b128 v[128:131], v173 offset:28672\n"
    "buffer_load_dwordx4 v2, s[52:55], s10 offen offset:2048 sc1 lds\n"
    "v_mfma_f32_32x32x16_bf16 v[96:111], v[140:143], a[92:95], v[96:111]\n"
    "ds_read_b128 v[132:135], v173 offset:29696\n"
    "s_waitcnt lgkmcnt(4)\n"
    "v_mfma_f32_32x32x16_bf16 v[96:111], v[112:115], a[96:99], v[96:111]\n"
    "ds_read_b128 v[136:139], v173 offset:30720\n"
    "buffer_load_dwordx4 v2, s[52:55], s10 offen offset:3072 sc1 lds\n"
    "s_xor_b32 s10, s8, 0x4000\n"
    "v_mfma_f32_32x32x16_bf16 v[96:111], v[116:119], a[100:103], v[96:111]\n"
    "ds_read_b128 v[140:143], v173 offset:31744\n"
    "s_waitcnt lgkmcnt(4)\n"
    "v_mfma_f32_32x32x16_bf16 v[96:111], v[120:123], a[104:107], v[96:111]\n"
    "s_waitcnt vmcnt(11)\n"
    "s_barrier\n"
    "ds_read_b128 v[112:115], v172\n"
    "v_mfma_f32_32x32x16_bf16 v[96:111], v[124:127], a[108:111], v[96:111]\n"
    "ds_read_b128 v[116:119], v172 offset:1024\n"
    "s_add_u32 m0, s9, 0x1c000\n"
    "s_nop 0\n"
    "buffer_load_dwordx4 v2, s[52:55], s10 offen sc1 lds\n"
    "s_waitcnt lgkmcnt(4)\n"
    "v_mfma_f32_32x32x16_bf16 v[96:111], v[128:131], a[112:115], v[96:111]\n"
    "ds_read_b128 v[120:123], v172 offset:2048\n"
    "v_mfma_f32_32x32x16_bf16 v[96:111], v[132:135], a[116:119], v[96:111]\n"
    "ds_read_b128 v[124:127], v172 offset:3072\n"
    "buffer_load_dwordx4 v2, s[52:55], s10 offen offset:1024 sc1 lds\n"
    "s_waitcnt lgkmcnt(4)\n"
    "v_mfma_f32_32x32x16_bf16 v[96:111], v[136:139], a[120:123], v[96:111]\n"
    "ds_read_b128 v[128:131], v172 offset:4096\n"
    "v_mfma_f32_32x32x16_bf16 v[96:111], v[140:143], a[124:127], v[96:111]\n"
    "ds_read_b128 v[132:135], v172 offset:5120\n"
    "s_waitcnt lgkmcnt(4)\n"
    "v_mfma_f32_32x32x16_bf16 v[96:111], v[112:115], a[128:131], v[96:111]\n"
    "ds_read_b128 v[136:139], v172 offset:6144\n"
    "buffer_load_dwordx4 v2, s[52:55], s10 offen offset:2048 sc1 lds\n"
    "v_mfma_f32_32x32x16_bf16 v[96:111], v[116:119], a[132:135], v[96:111]\n"
    "ds_read_b128 v[140:143], v172 offset:7168\n"
    "s_waitcnt lgkmcnt(4)\n"
    "v_mfma_f32_32x32x16_bf16 v[96:111], v[120:123], a[136:139], v[96:111]\n"
    "ds_read_b128 v[112:115], v172 offset:8192\n"
    "buffer_load_dwordx4 v2, s[52:55], s10 offen offset:3072 sc1 lds\n"
    "s_xor_b32 s10, s8, 0x8000\n"
    "v_mfma_f32_32x32x16_bf16 v[96:111], v[124:127], a[140:143], v[96:111]\n"
    "ds_read_b128 v[116:119], v172 offset:9216\n"
    "s_waitcnt lgkmcnt(4)\n"
    "v_mfma_f32_32x32x16_bf16 v[96:111], v[128:131], a[144:147], v[96:111]\n"
    "ds_read_b128 v[120:123], v172 offset:10240\n"
    "v_mfma_f32_32x32x16_bf16 v[96:111], v[132:135], a[148:151], v[96:111]\n"
    "ds_read_b128 v[124:127], v172 offset:11264\n"
    "s_add_u32 m0, s9, 0x20000\n"
    "s_nop 0\n"
    "buffer_load_dwordx4 v2, s[52:55], s10 offen sc1 lds\n"
    "s_waitcnt lgkmcnt(4)\n"
    "v_mfma_f32_32x32x16_bf16 v[96:111], v[136:139], a[152:155], v[96:111]\n"
    "ds_read_b128 v[128:131], v172 offset:12288\n"
    "v_mfma_f32_32x32x16_bf16 v[96:111], v[140:143], a[156:159], v[96:111]\n"
    "ds_read_b128 v[132:135], v172 offset:13312\n"
    "buffer_load_dwordx4 v2, s[52:55], s10 offen offset:1024 sc1 lds\n"
    "s_waitcnt lgkmcnt(4)\n"
    "v_mfma_f32_32x32x16_bf16 v[96:111], v[112:115], a[160:163], v[96:111]\n"
    "ds_read_b128 v[136:139], v172 offset:14336\n"
    "v_mfma_f32_32x32x16_bf16 v[96:111], v[116:119], a[164:167], v[96:111]\n"
    "ds_read_b128 v[140:143], v172 offset:15360\n"
    "s_waitcnt lgkmcnt(4)\n"
    "v_mfma_f32_32x32x16_bf16 v[96:111], v[120:123], a[168:171], v[96:111]\n"
    "s_waitcnt vmcnt(10)\n"
    "s_barrier\n"
    "ds_read_b128 v[112:115], v172 offset:16384\n"
    "buffer_load_dwordx4 v2, s[52:55], s10 offen offset:2048 sc1 lds\n"
    "v_mfma_f32_32x32x16_bf16 v[96:111], v[124:127], a[172:175], v[96:111]\n"
    "ds_read_b128 v[116:119], v172 offset:17408\n"
    "s_waitcnt lgkmcnt(4)\n"
    "v_mfma_f32_32x32x16_bf16 v[96:111], v[128:131], a[176:179], v[96:111]\n"
    "ds_read_b128 v[120:123], v172 offset:18432\n"
    "buffer_load_dwordx4 v2, s[52:55], s10 offen offset:3072 sc1 lds\n"
    "s_xor_b32 s10, s8, 0xc000\n"
    "v_mfma_f32_32x32x16_bf16 v[96:111], v[132:135], a[180:183], v[96:111]\n"
    "ds_read_b128 v[124:127], v172 offset:19456\n"
    "s_waitcnt lgkmcnt(4)\n"
    "v_mfma_f32_32x32x16_bf16 v[96:111], v[136:139], a[184:187], v[96:111]\n"
    "ds_read_b128 v[128:131], v172 offset:20480\n"
    "v_mfma_f32_32x32x16_bf16 v[96:111], v[140:143], a[188:191], v[96:111]\n"
    "ds_read_b128 v[132:135], v172 offset:21504\n"
    "s_add_u32 m0, s9, 0x24000\n"
    "s_nop 0\n"
    "buffer_load_dwordx4 v2, s[52:55], s10 offen sc1 lds\n"
    "s_waitcnt lgkmcnt(4)\n"
    "v_mfma_f32_32x32x16_bf16 v[96:111], v[112:115], a[192:195], v[96:111]\n"
    "ds_read_b128 v[136:139], v172 offset:22528\n"
    "v_mfma_f32_32x32x16_bf16 v[96:111], v[116:119], a[196:199], v[96:111]\n"
    "ds_read_b128 v[140:143], v172 offset:23552\n"
    "buffer_load_dwordx4 v2, s[52:55], s10 offen offset:1024 sc1 lds\n"
    "s_waitcnt lgkmcnt(4)\n"
    "v_mfma_f32_32x32x16_bf16 v[96:111], v[120:123], a[200:203], v[96:111]\n"
    "ds_read_b128 v[112:115], v172 offset:24576\n"
    "v_mfma_f32_32x32x16_bf16 v[96:111], v[124:127], a[204:207], v[96:111]\n"
    "ds_read_b128 v[116:119], v172 offset:25600\n"
    "s_waitcnt lgkmcnt(4)\n"
    "v_mfma_f32_32x32x16_bf16 v[96:111], v[128:131], a[208:211], v[96:111]\n"
    "ds_read_b128 v[120:123], v172 offset:26624\n"
    "buffer_load_dwordx4 v2, s[52:55], s10 offen offset:2048 sc1 lds\n"
    "v_mfma_f32_32x32x16_bf16 v[96:111], v[132:135], a[212:215], v[96:111]\n"
    "ds_read_b128 v[124:127], v172 offset:27648\n"
    "s_waitcnt lgkmcnt(4)\n"
    "v_mfma_f32_32x32x16_bf16 v[96:111], v[136:139], a[216:219], v[96:111]\n"
    "ds_read_b128 v[128:131], v172 offset:28672\n"
    "buffer_load_dwordx4 v2, s[52:55], s10 offen offset:3072 sc1 lds\n"
    "v_mfma_f32_32x32x16_bf16 v[96:111], v[140:143], a[220:223], v[96:111]\n"
    "ds_read_b128 v[132:135], v172 offset:29696\n"
    "s_waitcnt lgkmcnt(4)\n"
    "v_mfma_f32_32x32x16_bf16 v[96:111], v[112:115], a[224:227], v[96:111]\n"
    "ds_read_b128 v[136:139], v172 offset:30720\n"
    "v_mfma_f32_32x32x16_bf16 v[96:111], v[116:119], a[228:231], v[96:111]\n"
    "ds_read_b128 v[140:143], v172 offset:31744\n"
    "buffer_load_dwordx4 v[64:67], v2, s[52:55], s16 offen sc1\n"
    "s_waitcnt lgkmcnt(4)\n"
    "v_mfma_f32_32x32x16_bf16 v[96:111], v[120:123], a[232:235], v[96:111]\n"
    "s_waitcnt vmcnt(13)\n"
    "s_barrier\n"
    "ds_read_b128 v[112:115], v172 offset:32768\n"
    "v_mfma_f32_32x32x16_bf16 v[96:111], v[124:127], a[236:239], v[96:111]\n"
    "ds_read_b128 v[116:119], v172 offset:33792\n"
    "buffer_load_dwordx4 v[68:71], v174, s[52:55], s16 offen sc1\n"
    "s_waitcnt lgkmcnt(4)\n"
    "v_mfma_f32_32x32x16_bf16 v[96:111], v[128:131], a[240:243], v[96:111]\n"
    "ds_read_b128 v[120:123], v172 offset:34816\n"
    "v_mfma_f32_32x32x16_bf16 v[96:111], v[132:135], a[244:247], v[96:111]\n"
    "ds_read_b128 v[124:127], v172 offset:35840\n"
    "s_waitcnt lgkmcnt(4)\n"
    "v_mfma_f32_32x32x16_bf16 v[96:111], v[136:139], a[248:251], v[96:111]\n"
    "ds_read_b128 v[128:131], v172 offset:36864\n"
    "buffer_load_dwordx4 v[72:75], v175, s[52:55], s16 offen sc1\n"
    "v_mfma_f32_32x32x16_bf16 v[96:111], v[140:143], a[252:255], v[96:111]\n"
    "ds_read_b128 v[132:135], v172 offset:37888\n"
    "s_waitcnt lgkmcnt(4)\n"
    "v_mfma_f32_32x32x16_bf16 v[24:39], v[112:115], a[0:3], v[24:39]\n"
    "ds_read_b128 v[136:139], v172 offset:38912\n"
    "buffer_load_dwordx4 v[80:83], v2, s[52:55], s17 offen sc1\n"
    "v_mfma_f32_32x32x16_bf16 v[24:39], v[116:119], a[4:7], v[24:39]\n"
    "ds_read_b128 v[140:143], v172 offset:39936\n"
    "s_nop 4\n"
    "v_cvt_pk_bf16_f32 v40, v96, v97\n"
    "v_cvt_pk_bf16_f32 v41, v98, v99\n"
    "v_cvt_pk_bf16_f32 v42, v100, v101\n"
    "s_waitcnt lgkmcnt(4)\n"
    "v_mfma_f32_32x32x16_bf16 v[24:39], v[120:123], a[8:11], v[24:39]\n"
    "ds_read_b128 v[112:115], v172 offset:40960\n"
    "v_mfma_f32_32x32x16_bf16 v[24:39], v[124:127], a[12:15], v[24:39]\n"
    "ds_read_b128 v[116:119], v172 offset:41984\n"
    "buffer_store_dwordx4 v[40:43], v2, s[52:55], s15 offen sc1\n"
    "v_cvt_pk_bf16_f32 v44, v102, v103\n"
    "v_cvt_pk_bf16_f32 v45, v104, v105\n"
    "v_cvt_pk_bf16_f32 v46, v106, v107\n"
    "s_waitcnt lgkmcnt(4)\n"
    "v_mfma_f32_32x32x16_bf16 v[24:39], v[128:131], a[16:19], v[24:39]\n"
    "ds_read_b128 v[120:123], v172 offset:43008\n"
    "v_mfma_f32_32x32x16_bf16 v[24:39], v[132:135], a[20:23], v[24:39]\n"
    "ds_read_b128 v[124:127], v172 offset:44032\n"
    "buffer_store_dwordx4 v[44:47], v174, s[52:55], s15 offen sc1\n"
    "v_cvt_pk_bf16_f32 v48, v108, v109\n"
    "v_cvt_pk_bf16_f32 v49, v110, v111\n"
    "s_waitcnt lgkmcnt(4)\n"
    "v_mfma_f32_32x32x16_bf16 v[24:39], v[136:139], a[24:27], v[24:39]\n"
    "ds_read_b128 v[128:131], v172 offset:45056\n"
    "v_mfma_f32_32x32x16_bf16 v[24:39], v[140:143], a[28:31], v[24:39]\n"
    "ds_read_b128 v[132:135], v172 offset:46080\n"
    "s_waitcnt lgkmcnt(4)\n"
    "v_mfma_f32_32x32x16_bf16 v[24:39], v[112:115], a[32:35], v[24:39]\n"
    "ds_read_b128 v[136:139], v172 offset:47104\n"
    "buffer_store_dwordx4 v[48:51], v175, s[52:55], s15 offen sc1\n"
    "v_mfma_f32_32x32x16_bf16 v[24:39], v[116:119], a[36:39], v[24:39]\n"
    "ds_read_b128 v[140:143], v172 offset:48128\n"
    "v_lshlrev_b32 v171, 2, v5\n"
    "s_mov_b32 exec_lo, 0xffff\n"
    "s_mov_b32 exec_hi, 0\n"
    "buffer_store_dword v4, v171, s[52:55], s81 offen sc1\n"
    "s_mov_b64 exec, -1\n"
    "s_waitcnt lgkmcnt(4)\n"
    "v_mfma_f32_32x32x16_bf16 v[24:39], v[120:123], a[40:43], v[24:39]\n"
    "s_waitcnt vmcnt(16)\n"
    "s_barrier\n"
    "ds_read_b128 v[112:115], v172 offset:49152\n"
    "buffer_load_dwordx4 v[84:87], v174, s[52:55], s17 offen sc1\n"
    "v_and_b32 v170, 15, v1\n"
    "v_lshlrev_b32 v170, 6, v170\n"
    "v_mfma_f32_32x32x16_bf16 v[24:39], v[124:127], a[44:47], v[24:39]\n"
    "ds_read_b128 v[116:119], v172 offset:50176\n"
    "s_waitcnt lgkmcnt(4)\n"
    "v_mfma_f32_32x32x16_bf16 v[24:39], v[128:131], a[48:51], v[24:39]\n"
    "ds_read_b128 v[120:123], v172 offset:51200\n"
    "v_mfma_f32_32x32x16_bf16 v[24:39], v[132:135], a[52:55], v[24:39]\n"
    "ds_read_b128 v[124:127], v172 offset:52224\n"
    "buffer_load_dwordx4 v[88:91], v175, s[52:55], s17 offen sc1\n"
    "s_waitcnt lgkmcnt(4)\n"
    "v_mfma_f32_32x32x16_bf16 v[24:39], v[136:139], a[56:59], v[24:39]\n"
    "ds_read_b128 v[128:131], v172 offset:53248\n"
    "v_mfma_f32_32x32x16_bf16 v[24:39], v[140:143], a[60:63], v[24:39]\n"
    "ds_read_b128 v[132:135], v172 offset:54272\n"
    "s_add_u32 m0, s72, 0\n"
    "s_nop 0\n"
    "buffer_load_dwordx4 v2, s[52:55], s89 offen sc1 lds\n"
    "s_waitcnt lgkmcnt(4)\n"
    "v_mfma_f32_32x32x16_bf16 v[24:39], v[112:115], a[64:67], v[24:39]\n"
    "ds_read_b128 v[136:139], v172 offset:55296\n"
    "v_mfma_f32_32x32x16_bf16 v[24:39], v[116:119], a[68:71], v[24:39]\n"
    "ds_read_b128 v[140:143], v172 offset:56320\n"
    "s_waitcnt lgkmcnt(4)\n"
    "v_mfma_f32_32x32x16_bf16 v[24:39], v[120:123], a[72:75], v[24:39]\n"
    "ds_read_b128 v[112:115], v172 offset:57344\n"
    "buffer_load_dwordx4 v2, s[52:55], s89 offen offset:1024 sc1 lds\n"
    "v_mfma_f32_32x32x16_bf16 v[24:39], v[124:127], a[76:79], v[24:39]\n"
    "ds_read_b128 v[116:119], v172 offset:58368\n"
    "s_waitcnt lgkmcnt(4)\n"
    "v_mfma_f32_32x32x16_bf16 v[24:39], v[128:131], a[80:83], v[24:39]\n"
    "ds_read_b128 v[120:123], v172 offset:59392\n"
    "buffer_load_dwordx4 v2, s[52:55], s89 offen offset:2048 sc1 lds\n"
    "v_mfma_f32_32x32x16_bf16 v[24:39], v[132:135], a[84:87], v[24:39]\n"
    "ds_read_b128 v[124:127], v172 offset:60416\n"
    "s_waitcnt lgkmcnt(4)\n"
    "v_mfma_f32_32x32x16_bf16 v[24:39], v[136:139], a[88:91], v[24:39]\n"
    "ds_read_b128 v[128:131], v172 offset:61440\n"
    "v_mfma_f32_32x32x16_bf16 v[24:39], v[140:143], a[92:95], v[24:39]\n"
    "ds_read_b128 v[132:135], v172 offset:62464\n"
    "buffer_load_dwordx4 v2, s[52:55], s89 offen offset:3072 sc1 lds\n"
    "s_add_u32 s89, s19, 0x1000\n"
    "s_waitcnt lgkmcnt(4)\n"
    "v_mfma_f32_32x32x16_bf16 v[24:39], v[112:115], a[96:99], v[24:39]\n"
    "ds_read_b128 v[136:139], v172 offset:63488\n"
    "v_mfma_f32_32x32x16_bf16 v[24:39], v[116:119], a[100:103], v[24:39]\n"
    "ds_read_b128 v[140:143], v172 offset:64512\n"
    "s_add_u32 m0, s72, 0x1000\n"
    "s_nop 0\n"
    "buffer_load_dwordx4 v2, s[52:55], s89 offen sc1 lds\n"
    "s_waitcnt lgkmcnt(4)\n"
    "v_mfma_f32_32x32x16_bf16 v[24:39], v[120:123], a[104:107], v[24:39]\n"
    "s_waitcnt vmcnt(19)\n"
    "s_barrier\n"
    "ds_read_b128 v[112:115], v173\n"
    "v_mfma_f32_32x32x16_bf16 v[24:39], v[124:127], a[108:111], v[24:39]\n"
    "ds_read_b128 v[116:119], v173 offset:1024\n"
    "s_waitcnt lgkmcnt(4)\n"
    "v_mfma_f32_32x32x16_bf16 v[24:39], v[128:131], a[112:115], v[24:39]\n"
    "ds_read_b128 v[120:123], v173 offset:2048\n"
    "s_lshr_b32 s79, s9, 2\n"
    "s_add_u32 s80, s80, s79\n"
    "s_add_u32 m0, s79, 0x19000\n"
    "s_nop 0\n"
    "buffer_load_dwordx4 v2, s[52:55], s80 offen sc1 lds\n"
    "v_mfma_f32_32x32x16_bf16 v[24:39], v[132:135], a[116:119], v[24:39]\n"
    "ds_read_b128 v[124:127], v173 offset:3072\n"
    "s_waitcnt vmcnt(15)\n"
    "v_xor_b32 v168, s66, v67\n"
    "s_waitcnt vmcnt(14)\n"
    "v_bitop3_b32 v168, v71, s66, v168 bitop3:0xbe\n"
    "s_waitcnt vmcnt(13)\n"
    "v_bitop3_b32 v168, v75, s66, v168 bitop3:0xbe\n"
    "v_cmp_ne_u32 vcc, 0, v168\n"
    "s_cbranch_vccnz Lslow31_%=\n"
    "Lback32_%=:\n"
    "s_waitcnt lgkmcnt(4)\n"
    "v_mfma_f32_32x32x16_bf16 v[24:39], v[136:139], a[120:123], v[24:39]\n"
    "ds_read_b128 v[128:131], v173 offset:4096\n"
    "s_add_u32 m0, s72, 0x1000\n"
    "s_nop 0\n"
    "buffer_load_dwordx4 v2, s[52:55], s89 offen offset:1024 sc1 lds\n"
    "v_mov_b32 v67, 0\n"
    "v_mov_b32 v71, 0\n"
    "v_mfma_f32_32x32x16_bf16 v[24:39], v[140:143], a[124:127], v[24:39]\n"
    "ds_read_b128 v[132:135], v173 offset:5120\n"
    "v_mov_b32 v75, 0\n"
    "s_waitcnt lgkmcnt(4)\n"
    "v_mfma_f32_32x32x16_bf16 v[24:39], v[112:115], a[128:131], v[24:39]\n"
    "ds_read_b128 v[136:139], v173 offset:6144\n"
    "v_mfma_f32_32x32x16_bf16 v[24:39], v[52:55], v[64:67], v[24:39]\n"
    "v_mfma_f32_32x32x16_bf16 v[24:39], v[116:119], a[132:135], v[24:39]\n"
    "ds_read_b128 v[140:143], v173 offset:7168\n"
    "buffer_load_dwordx4 v2, s[52:55], s89 offen offset:2048 sc1 lds\n"
    "s_waitcnt lgkmcnt(4)\n"
    "v_mfma_f32_32x32x16_bf16 v[24:39], v[120:123], a[136:139], v[24:39]\n"
    "ds_read_b128 v[112:115], v173 offset:8192\n"
    "v_mfma_f32_32x32x16_bf16 v[24:39], v[56:59], v[68:71], v[24:39]\n"
    "v_mfma_f32_32x32x16_bf16 v[24:39], v[124:127], a[140:143], v[24:39]\n"
    "ds_read_b128 v[116:119], v173 offset:9216\n"
    "buffer_load_dwordx4 v2, s[52:55], s89 offen offset:3072 sc1 lds\n"
    "s_add_u32 s89, s19, 0x8000\n"
    "s_waitcnt lgkmcnt(4)\n"
    "v_mfma_f32_32x32x16_bf16 v[24:39], v[128:131], a[144:147], v[24:39]\n"
    "ds_read_b128 v[120:123], v173 offset:10240\n"
    "v_mfma_f32_32x32x16_bf16 v[24:39], v[60:63], v[72:75], v[24:39]\n"
    "v_mfma_f32_32x32x16_bf16 v[24:39], v[132:135], a[148:151], v[24:39]\n"
    "ds_read_b128 v[124:127], v173 offset:11264\n"
    "s_waitcnt lgkmcnt(4)\n"
    "v_mfma_f32_32x32x16_bf16 v[24:39], v[136:139], a[152:155], v[24:39]\n"
    "ds_read_b128 v[128:131], v173 offset:12288\n"
    "s_add_u32 m0, s72, 0x8000\n"
    "s_nop 0\n"
    "buffer_load_dwordx4 v2, s[52:55], s89 offen sc1 lds\n"
    "v_mfma_f32_32x32x16_bf16 v[24:39], v[140:143], a[156:159], v[24:39]\n"
    "ds_read_b128 v[132:135], v173 offset:13312\n"
    "s_waitcnt lgkmcnt(4)\n"
    "v_mfma_f32_32x32x16_bf16 v[24:39], v[112:115], a[160:163], v[24:39]\n"
    "ds_read_b128 v[136:139], v173 offset:14336\n"
    "buffer_load_dwordx4 v2, s[52:55], s89 offen offset:1024 sc1 lds\n"
    "v_mfma_f32_32x32x16_bf16 v[24:39], v[116:119], a[164:167], v[24:39]\n"
    "ds_read_b128 v[140:143], v173 offset:15360\n"
    "s_waitcnt lgkmcnt(4)\n"
    "v_mfma_f32_32x32x16_bf16 v[24:39], v[120:123], a[168:171], v[24:39]\n"
    "s_barrier\n"
    "ds_read_b128 v[112:115], v173 offset:16384\n"
    "v_mfma_f32_32x32x16_bf16 v[24:39], v[124:127], a[172:175], v[24:39]\n"
    "ds_read_b128 v[116:119], v173 offset:17408\n"
    "buffer_load_dwordx4 v2, s[52:55], s89 offen offset:2048 sc1 lds\n"
    "s_waitcnt lgkmcnt(4)\n"
    "v_mfma_f32_32x32x16_bf16 v[24:39], v[128:131], a[176:179], v[24:39]\n"
    "ds_read_b128 v[120:123], v173 offset:18432\n"
    "s_waitcnt vmcnt(18)\n"
    "v_xor_b32 v168, s66, v83\n"
    "s_waitcnt vmcnt(13)\n"
    "v_bitop3_b32 v168, v87, s66, v168 bitop3:0xbe\n"
    "s_waitcnt vmcnt(12)\n"
    "v_bitop3_b32 v168, v91, s66, v168 bitop3:0xbe\n"
    "v_cmp_ne_u32 vcc, 0, v168\n"
    "s_cbranch_vccnz Lslow34_%=\n"
    "Lback35_%=:\n"
    "v_mfma_f32_32x32x16_bf16 v[24:39], v[132:135], a[180:183], v[24:39]\n"
    "ds_read_b128 v[124:127], v173 offset:19456\n"
    "buffer_load_dwordx4 v[96:99], v2, s[52:55], s18 offen sc1\n"
    "v_mov_b32 v83, 0\n"
    "v_mov_b32 v87, 0\n"
    "v_mov_b32 v91, 0\n"
    "s_waitcnt lgkmcnt(4)\n"
    "v_mfma_f32_32x32x16_bf16 v[24:39], v[136:139], a[184:187], v[24:39]\n"
    "ds_read_b128 v[128:131], v173 offset:20480\n"
    "v_mfma_f32_32x32x16_bf16 v[24:39], v[52:55], v[80:83], v[24:39]\n"
    "v_mfma_f32_32x32x16_bf16 v[24:39], v[140:143], a[188:191], v[24:39]\n"
    "ds_read_b128 v[132:135], v173 offset:21504\n"
    "v_mfma_f32_32x32x16_bf16 v[24:39], v[56:59], v[84:87], v[24:39]\n"
    "s_waitcnt lgkmcnt(4)\n"
    "v_mfma_f32_32x32x16_bf16 v[24:39], v[112:115], a[192:195], v[24:39]\n"
    "ds_read_b128 v[136:139], v173 offset:22528\n"
    "buffer_load_dwordx4 v[100:103], v174, s[52:55], s18 offen sc1\n"
    "buffer_load_dword v171, v170, s[52:55], s82 offen sc1\n"
    "v_mfma_f32_32x32x16_bf16 v[24:39], v[116:119], a[196:199], v[24:39]\n"
    "ds_read_b128 v[140:143], v173 offset:23552\n"
    "v_mfma_f32_32x32x16_bf16 v[24:39], v[60:63], v[88:91], v[24:39]\n"
    "s_waitcnt lgkmcnt(4)\n"
    "v_mfma_f32_32x32x16_bf16 v[24:39], v[120:123], a[200:203], v[24:39]\n"
    "ds_read_b128 v[112:115], v173 offset:24576\n"
    "buffer_load_dwordx4 v[104:107], v175, s[52:55], s18 offen sc1\n"
    "v_mfma_f32_32x32x16_bf16 v[24:39], v[124:127], a[204:207], v[24:39]\n"
    "ds_read_b128 v[116:119], v173 offset:25600\n"
    "s_waitcnt lgkmcnt(4)\n"
    "v_mfma_f32_32x32x16_bf16 v[24:39], v[128:131], a[208:211], v[24:39]\n"
    "ds_read_b128 v[120:123], v173 offset:26624\n"
    "v_mfma_f32_32x32x16_bf16 v[24:39], v[132:135], a[212:215], v[24:39]\n"
    "ds_read_b128 v[124:127], v173 offset:27648\n"
    "buffer_load_dwordx4 v2, s[52:55], s89 offen offset:3072 sc1 lds\n"
    "s_add_u32 s89, s19, 0x9000\n"
    "s_waitcnt lgkmcnt(4)\n"
    "v_mfma_f32_32x32x16_bf16 v[24:39], v[136:139], a[216:219], v[24:39]\n"
    "ds_read_b128 v[128:131], v173 offset:28672\n"
    "v_mfma_f32_32x32x16_bf16 v[24:39], v[140:143], a[220:223], v[24:39]\n"
    "ds_read_b128 v[132:135], v173 offset:29696\n"
    "s_add_u32 m0, s72, 0x9000\n"
    "s_nop 0\n"
    "buffer_load_dwordx4 v2, s[52:55], s89 offen sc1 lds\n"
    "s_waitcnt lgkmcnt(4)\n"
    "v_mfma_f32_32x32x16_bf16 v[24:39], v[112:115], a[224:227], v[24:39]\n"
    "ds_read_b128 v[136:139], v173 offset:30720\n"
    "v_mfma_f32_32x32x16_bf16 v[24:39], v[116:119], a[228:231], v[24:39]\n"
    "ds_read_b128 v[140:143], v173 offset:31744\n"
    "s_waitcnt lgkmcnt(4)\n"
    "v_mfma_f32_32x32x16_bf16 v[24:39], v[120:123], a[232:235], v[24:39]\n"
    "buffer_load_dwordx4 v2, s[52:55], s89 offen offset:1024 sc1 lds\n"
    "v_mfma_f32_32x32x16_bf16 v[24:39], v[124:127], a[236:239], v[24:39]\n"
    "s_waitcnt lgkmcnt(2)\n"
    "v_mfma_f32_32x32x16_bf16 v[24:39], v[128:131], a[240:243], v[24:39]\n"
    "buffer_load_dwordx4 v2, s[52:55], s89 offen offset:2048 sc1 lds\n"
    "v_mfma_f32_32x32x16_bf16 v[24:39], v[132:135], a[244:247], v[24:39]\n"
    "s_waitcnt lgkmcnt(0)\n"
    "v_mfma_f32_32x32x16_bf16 v[24:39], v[136:139], a[248:251], v[24:39]\n"
    "v_mfma_f32_32x32x16_bf16 v[24:39], v[140:143], a[252:255], v[24:39]\n"
    "buffer_load_dwordx4 v2, s[52:55], s89 offen offset:3072 sc1 lds\n"
    "s_add_u32 s89, s19, 0x10000\n"
    "s_waitcnt vmcnt(8)\n"
    "v_xor_b32 v168, s66, v99\n"
    "s_waitcnt vmcnt(7)\n"
    "v_bitop3_b32 v168, v103, s66, v168 bitop3:0xbe\n"
    "s_waitcnt vmcnt(5)\n"
    "v_bitop3_b32 v168, v107, s66, v168 bitop3:0xbe\n"
    "v_cmp_ne_u32 vcc, 0, v168\n"
    "s_cbranch_vccnz Lslow37_%=\n"
    "Lback38_%=:\n"
    "v_mov_b32 v99, 0\n"
    "v_mov_b32 v103, 0\n"
    "v_mov_b32 v107, 0\n"
    "v_mfma_f32_32x32x16_bf16 v[24:39], v[52:55], v[96:99], v[24:39]\n"
    "v_mfma_f32_32x32x16_bf16 v[24:39], v[56:59], v[100:103], v[24:39]\n"
    "v_mfma_f32_32x32x16_bf16 v[24:39], v[60:63], v[104:107], v[24:39]\n"
    "s_nop 11\n"
    "v_cvt_pk_bf16_f32 v155, v30, v31\n"
    "v_cvt_pk_bf16_f32 v152, v24, v25\n"
    "v_cvt_pk_bf16_f32 v153, v26, v27\n"
    "v_cvt_pk_bf16_f32 v154, v28, v29\n"
    "buffer_store_dwordx4 v[152:155], v2, s[52:55], s16 offen sc1\n"
    "v_cvt_pk_bf16_f32 v159, v38, v39\n"
    "v_cvt_pk_bf16_f32 v156, v32, v33\n"
    "v_cvt_pk_bf16_f32 v157, v34, v35\n"
    "v_cvt_pk_bf16_f32 v158, v36, v37\n"
    "buffer_store_dwordx4 v[156:159], v174, s[52:55], s16 offen sc1\n"
    "v_pk_mul_f32 v[80:81], v[24:25], v[24:25]\n"
    "v_pk_mul_f32 v[82:83], v[26:27], v[26:27]\n"
    "v_pk_add_f32 v[84:85], v[24:25], v[26:27]\n"
    "v_pk_add_f32 v[86:87], v[28:29], v[30:31]\n"
    "v_pk_fma_f32 v[80:81], v[28:29], v[28:29], v[80:81]\n"
    "v_pk_fma_f32 v[82:83], v[30:31], v[30:31], v[82:83]\n"
    "v_pk_fma_f32 v[80:81], v[32:33], v[32:33], v[80:81]\n"
    "v_pk_add_f32 v[84:85], v[84:85], v[32:33]\n"
    "v_pk_fma_f32 v[82:83], v[34:35], v[34:35], v[82:83]\n"
    "v_pk_add_f32 v[86:87], v[86:87], v[34:35]\n"
    "v_pk_fma_f32 v[80:81], v[36:37], v[36:37], v[80:81]\n"
    "v_pk_add_f32 v[84:85], v[84:85], v[36:37]\n"
    "v_pk_fma_f32 v[82:83], v[38:39], v[38:39], v[82:83]\n"
    "v_pk_add_f32 v[86:87], v[86:87], v[38:39]\n"
    "v_pk_add_f32 v[80:81], v[80:81], v[82:83]\n"
    "v_pk_add_f32 v[84:85], v[84:85], v[86:87]\n"
    "v_add_f32 v165, v80, v81\n"
    "v_add_f32 v164, v84, v85\n"
    "s_nop 1\n"
    "v_permlane32_swap_b32 v164, v165\n"
    "v_add_f32 v164, v164, v165\n"
    "v_mov_b32 v165, s66\n"
    "v_mov_b32 v166, v164\n"
    "v_mov_b32 v167, s66\n"
    "s_nop 0\n"
    "v_permlane32_swap_b32 v164, v166\n"
    "s_mov_b32 exec_hi, 0\n"
    "buffer_store_dwordx4 v[164:167], v2, s[52:55], s83 offen sc1\n"
    "s_mov_b32 exec_hi, -1\n"
    "s_add_u32 m0, s72, 0x10000\n"
    "s_nop 0\n"
    "buffer_load_dwordx4 v2, s[52:55], s89 offen sc1 lds\n"
    "buffer_load_dwordx4 v2, s[52:55], s89 offen offset:1024 sc1 lds\n"
    "buffer_load_dwordx4 v2, s[52:55], s89 offen offset:2048 sc1 lds\n"
    "buffer_load_dwordx4 v2, s[52:55], s89 offen offset:3072 sc1 lds\n"
    "s_add_u32 s89, s19, 0x11000\n"
    "s_add_u32 m0, s72, 0x11000\n"
    "s_nop 0\n"
    "buffer_load_dwordx4 v2, s[52:55], s89 offen sc1 lds\n"
    "buffer_load_dwordx4 v2, s[52:55], s89 offen offset:1024 sc1 lds\n"
    "buffer_load_dwordx4 v2, s[52:55], s89 offen offset:2048 sc1 lds\n"
    "buffer_load_dwordx4 v2, s[52:55], s89 offen offset:3072 sc1 lds\n"
    "v_cmp_ne_u32 vcc, s66, v171\n"
    "s_cbranch_vccnz Lslow40_%=\n"
    "Lback41_%=:\n"
    "s_barrier\n"
    "Ljoin2_%=:\n"
    "v_add_u32 v13, 0x10000, v2\n"
    "v_add_u32 v14, 0x20000, v2\n"
    "v_add_u32 v15, 0x4000, v2\n"
    "s_mov_b32 s96, s85\n"
    "s_lshl_b32 s73, s64, 12\n"
    "s_add_u32 s96, s96, s73\n"
    "s_or_b32 s96, s96, 0x1000\n"
    "s_xor_b32 s8, s96, 0x1000\n"
    "s_xor_b32 s10, s96, 0x2000\n"
    "s_xor_b32 s75, s96, 0x3000\n"
    "buffer_load_dwordx4 a[128:131], v2, s[52:55], s96 offen sc1\n"
    "buffer_load_dwordx4 a[136:139], v2, s[52:55], s96 offen offset:1024 sc1\n"
    "buffer_load_dwordx4 a[144:147], v2, s[52:55], s96 offen offset:2048 sc1\n"
    "buffer_load_dwordx4 a[152:155], v2, s[52:55], s96 offen offset:3072 sc1\n"
    "buffer_load_dwordx4 a[132:135], v15, s[52:55], s96 offen sc1\n"
    "buffer_load_dwordx4 a[140:143], v15, s[52:55], s96 offen offset:1024 sc1\n"
    "buffer_load_dwordx4 a[148:151], v15, s[52:55], s96 offen offset:2048 sc1\n"
    "buffer_load_dwordx4 a[156:159], v15, s[52:55], s96 offen offset:3072 sc1\n"
    "s_add_u32 s89, s19, 0x18000\n"
    "s_add_u32 m0, s72, 0x18000\n"
    "s_nop 0\n"
    "buffer_load_dwordx4 v2, s[52:55], s89 offen sc1 lds\n"
    "buffer_load_dwordx4 v2, s[52:55], s89 offen offset:1024 sc1 lds\n"
    "buffer_load_dwordx4 v2, s[52:55], s89 offen offset:2048 sc1 lds\n"
    "buffer_load_dwordx4 v2, s[52:55], s89 offen offset:3072 sc1 lds\n"
    "v_lshlrev_b32 v10, 4, v5\n"
    "v_add_u32 v10, 0x10000, v10\n"
    "ds_read_b128 v[176:179], v10 offset:36864\n"
    "ds_read_b128 v[180:183], v10 offset:37376\n"
    "ds_read_b128 v[184:187], v10 offset:37888\n"
    "ds_read_b128 v[188:191], v10 offset:38400\n"
    "ds_read_b128 v[192:195], v10 offset:38912\n"
    "ds_read_b128 v[196:199], v10 offset:39424\n"
    "ds_read_b128 v[200:203], v10 offset:39936\n"
    "v_min_u32 v12, 27, v1\n"
    "v_lshlrev_b32 v12, 6, v12\n"
    "s_lshl_b32 s97, s64, 11\n"
    "s_add_u32 s97, s97, 0x483000\n"
    "s_lshl_b32 s14, s70, 13\n"
    "s_add_u32 s14, s14, 0x400000\n"
    "s_add_u32 s13, s14, 0x1000\n"
    "v_lshlrev_b32 v10, 4, v5\n"
    "s_mul_i32 s74, s69, 0xe00\n"
    "s_add_u32 s74, s74, 0x4b0000\n"
    "s_lshl_b32 s91, s70, 15\n"
    "s_lshl_b32 s73, s71, 6\n"
    "s_add_u32 s91, s91, s73\n"
    "s_sub_u32 s48, s48, 0x800\n"
    "s_subb_u32 s49, s49, 0\n"
    "s_mov_b32 s50, 0x1c0800\n"
    "s_mov_b32 s51, 0x20000\n"
    "s_lshr_b32 s20, s71, 1\n"
    "s_lshl_b32 s20, s20, 7\n"
    "v_lshlrev_b32 v8, 2, v1\n"
    "s_lshl_b32 s84, s63, 6\n"
    "v_xor_b32 v8, s84, v8\n"
    "v_and_b32 v8, 64, v8\n"
    "v_and_b32 v9, 0x70, v2\n"
    "v_xor_b32 v8, v8, v9\n"
    "v_lshrrev_b32 v9, 3, v1\n"
    "v_lshl_or_b32 v8, v9, 10, v8\n"
    "s_and_b32 s90, s61, 2\n"
    "s_and_b32 s84, s61, 1\n"
    "s_lshl_b32 s90, s90, 2\n"
    "s_lshl_b32 s73, s84, 1\n"
    "s_add_u32 s90, s90, s73\n"
    "s_xor_b32 s90, s90, s69\n"
    "s_xor_b32 s84, s90, 0\n"
    "s_mul_i32 s84, s84, 0x1c000\n"
    "s_add_u32 s84, s84, 0x800\n"
    "v_add_u32 v16, s84, v8\n"
    "s_xor_b32 s84, s90, 0\n"
    "s_mul_i32 s84, s84, 0x1c000\n"
    "s_add_u32 s84, s84, 0x2400\n"
    "v_add_u32 v17, s84, v8\n"
    "s_xor_b32 s84, s90, 1\n"
    "s_mul_i32 s84, s84, 0x1c000\n"
    "v_add_u32 v18, s84, v8\n"
    "s_xor_b32 s84, s90, 1\n"
    "s_mul_i32 s84, s84, 0x1c000\n"
    "s_add_u32 s84, s84, 0x1c00\n"
    "v_add_u32 v19, s84, v8\n"
    "v_lshrrev_b32 v9, 2, v1\n"
    "v_and_b32 v157, 48, v2\n"
    "v_lshl_or_b32 v157, v9, 10, v157\n"
    "s_and_b32 s90, s61, 1\n"
    "s_lshr_b32 s84, s61, 1\n"
    "s_lshl_b32 s90, s90, 3\n"
    "s_lshl_b32 s73, s84, 2\n"
    "s_add_u32 s90, s90, s73\n"
    "s_xor_b32 s90, s90, s69\n"
    "s_lshl_b32 s77, s71, 6\n"
    "s_add_u32 s77, s77, 0x18800\n"
    "s_xor_b32 s92, s90, 0\n"
    "s_mul_i32 s92, s92, 0x1c000\n"
    "s_add_u32 s92, s92, s77\n"
    "s_xor_b32 s93, s90, 1\n"
    "s_mul_i32 s93, s93, 0x1c000\n"
    "s_add_u32 s93, s93, s77\n"
    "s_add_u32 s93, s93, 0xfffffc00\n"
    "s_xor_b32 s94, s90, 2\n"
    "s_mul_i32 s94, s94, 0x1c000\n"
    "s_add_u32 s94, s94, s77\n"
    "s_add_u32 s94, s94, 0xfffff800\n"
    "s_xor_b32 s95, s90, 3\n"
    "s_mul_i32 s95, s95, 0x1c000\n"
    "s_add_u32 s95, s95, s77\n"
    "s_add_u32 s95, s95, 0xfffff400\n"
    "v_bfe_u32 v9, v1, 2, 2\n"
    "v_lshl_or_b32 v9, v6, 2, v9\n"
    "v_and_b32 v8, 3, v1\n"
    "v_lshlrev_b32 v8, 3, v8\n"
    "v_lshl_or_b32 v8, v9, 7, v8\n"
    "v_add_u32 v8, 0x10000, v8\n"
    "v_bfe_u32 v156, v1, 4, 1\n"
    "v_bfe_u32 v153, v1, 3, 1\n"
    "v_lshl_or_b32 v152, v153, 1, v156\n"
    "v_lshl_or_b32 v152, v152, 5, v8\n"
    "v_xor_b32 v154, 64, v152\n"
    "v_add_u32 v153, 0x10000, v152\n"
    "v_add_u32 v155, 0x10000, v154\n"
    "v_lshl_or_b32 v156, v9, 1, v156\n"
    "v_and_b32 v8, 24, v8\n"
    "v_lshl_or_b32 v156, v156, 5, v8\n"
    "s_xor_b32 s21, s63, 0\n"
    "s_lshl_b32 s21, s21, 10\n"
    "s_add_u32 s21, s21, s85\n"
    "s_xor_b32 s84, s64, 0\n"
    "s_lshl_b32 s84, s84, 12\n"
    "v_add_u32 v20, s84, v2\n"
    "s_xor_b32 s22, s63, 2\n"
    "s_lshl_b32 s22, s22, 10\n"
    "s_add_u32 s22, s22, s85\n"
    "s_xor_b32 s84, s64, 1\n"
    "s_lshl_b32 s84, s84, 12\n"
    "v_add_u32 v21, s84, v2\n"
    "s_xor_b32 s23, s63, 1\n"
    "s_lshl_b32 s23, s23, 10\n"
    "s_add_u32 s23, s23, s85\n"
    "s_xor_b32 s84, s64, 2\n"
    "s_lshl_b32 s84, s84, 12\n"
    "v_add_u32 v22, s84, v2\n"
    "s_xor_b32 s26, s63, 3\n"
    "s_lshl_b32 s26, s26, 10\n"
    "s_add_u32 s26, s26, s85\n"
    "s_xor_b32 s84, s64, 3\n"
    "s_lshl_b32 s84, s84, 12\n"
    "v_add_u32 v23, s84, v2\n"
    "s_add_u32 s17, s16, 0x24000\n"
    "s_xor_b32 s11, s17, 0x400\n"
    "s_add_u32 s18, s16, 0x28000\n"
    "s_xor_b32 s12, s18, 0x400\n"
    "s_add_u32 s27, s16, 0x2c000\n"
    "s_xor_b32 s15, s27, 0x400\n"
    "s_add_u32 s16, s16, 0x8000\n"
    "s_waitcnt lgkmcnt(0)\n"
    "s_waitcnt vmcnt(15)\n"
    "s_barrier\n"
    "ds_read_b128 v[120:123], v2\n"
    "ds_read_b128 v[124:127], v2 offset:32768\n"
    "ds_read_b128 v[128:131], v2 offset:2048\n"
    "ds_read_b128 v[132:135], v2 offset:34816\n"
    "ds_read_b128 v[136:139], v2 offset:4096\n"
    "s_waitcnt vmcnt(11)\n"
    "v_accvgpr_read_b32 v148, a131\n"
    "s_waitcnt vmcnt(10)\n"
    "v_accvgpr_read_b32 v149, a139\n"
    "s_waitcnt vmcnt(9)\n"
    "v_accvgpr_read_b32 v150, a147\n"
    "s_waitcnt vmcnt(8)\n"
    "v_accvgpr_read_b32 v151, a155\n"
    "v_cmp_eq_u32 vcc, s66, v148\n"
    "v_cmp_eq_u32 s[78:79], s66, v149\n"
    "v_cmp_eq_u32 s[80:81], s66, v150\n"
    "v_cmp_eq_u32 s[82:83], s66, v151\n"
    "s_or_b64 s[80:81], s[80:81], s[82:83]\n"
    "s_or_b64 s[78:79], s[78:79], s[80:81]\n"
    "s_or_b64 vcc, vcc, s[78:79]\n"
    "s_cbranch_vccnz Lslow43_%=\n"
    "Lback44_%=:\n"
    "s_waitcnt lgkmcnt(4)\n"
    "v_mfma_f32_32x32x16_bf16 v[40:55], v[120:123], a[128:131], 0\n"
    "ds_read_b128 v[140:143], v2 offset:36864\n"
    "buffer_load_dwordx4 a[192:195], v2, s[52:55], s10 offen sc1\n"
    "s_add_u32 s89, s19, 0x19000\n"
    "s_waitcnt lgkmcnt(4)\n"
    "v_mfma_f32_32x32x16_bf16 v[56:71], v[124:127], a[128:131], 0\n"
    "ds_read_b128 v[144:147], v2 offset:6144\n"
    "buffer_load_dwordx4 a[200:203], v2, s[52:55], s10 offen offset:1024 sc1\n"
    "s_waitcnt lgkmcnt(3)\n"
    "v_mfma_f32_32x32x16_bf16 v[40:55], v[128:131], a[136:139], v[40:55]\n"
    "ds_read_b128 v[120:123], v2 offset:38912\n"
    "buffer_load_dwordx4 a[208:211], v2, s[52:55], s10 offen offset:2048 sc1\n"
    "v_mfma_f32_32x32x16_bf16 v[56:71], v[132:135], a[136:139], v[56:71]\n"
    "ds_read_b128 v[124:127], v2 offset:1024\n"
    "buffer_load_dwordx4 a[216:219], v2, s[52:55], s10 offen offset:3072 sc1\n"
    "s_waitcnt lgkmcnt(3)\n"
    "v_mfma_f32_32x32x16_bf16 v[40:55], v[136:139], a[144:147], v[40:55]\n"
    "ds_read_b128 v[128:131], v2 offset:33792\n"
    "s_waitcnt vmcnt(11)\n"
    "v_accvgpr_read_b32 v148, a135\n"
    "s_waitcnt vmcnt(10)\n"
    "v_accvgpr_read_b32 v149, a143\n"
    "s_waitcnt vmcnt(9)\n"
    "v_accvgpr_read_b32 v150, a151\n"
    "v_mfma_f32_32x32x16_bf16 v[56:71], v[140:143], a[144:147], v[56:71]\n"
    "ds_read_b128 v[132:135], v2 offset:3072\n"
    "s_waitcnt vmcnt(8)\n"
    "v_accvgpr_read_b32 v151, a159\n"
    "buffer_load_dwordx4 a[196:199], v15, s[52:55], s10 offen sc1\n"
    "s_waitcnt lgkmcnt(3)\n"
    "v_mfma_f32_32x32x16_bf16 v[40:55], v[144:147], a[152:155], v[40:55]\n"
    "ds_read_b128 v[136:139], v2 offset:35840\n"
    "v_cmp_eq_u32 vcc, s66, v148\n"
    "v_cmp_eq_u32 s[78:79], s66, v149\n"
    "v_cmp_eq_u32 s[80:81], s66, v150\n"
    "v_cmp_eq_u32 s[82:83], s66, v151\n"
    "s_or_b64 s[80:81], s[80:81], s[82:83]\n"
    "s_or_b64 s[78:79], s[78:79], s[80:81]\n"
    "s_or_b64 vcc, vcc, s[78:79]\n"
    "s_cbranch_vccnz Lslow46_%=\n"
    "Lback47_%=:\n"
    "v_mfma_f32_32x32x16_bf16 v[56:71], v[120:123], a[152:155], v[56:71]\n"
    "ds_read_b128 v[140:143], v2 offset:5120\n"
    "buffer_load_dwordx4 a[204:207], v15, s[52:55], s10 offen offset:1024 sc1\n"
    "s_waitcnt lgkmcnt(3)\n"
    "v_mfma_f32_32x32x16_bf16 v[40:55], v[124:127], a[132:135], v[40:55]\n"
    "ds_read_b128 v[144:147], v2 offset:37888\n"
    "buffer_load_dwordx4 a[212:215], v15, s[52:55], s10 offen offset:2048 sc1\n"
    "v_mfma_f32_32x32x16_bf16 v[56:71], v[128:131], a[132:135], v[56:71]\n"
    "ds_read_b128 v[120:123], v2 offset:7168\n"
    "buffer_load_dwordx4 a[220:223], v15, s[52:55], s10 offen offset:3072 sc1\n"
    "s_waitcnt lgkmcnt(3)\n"
    "v_mfma_f32_32x32x16_bf16 v[40:55], v[132:135], a[140:143], v[40:55]\n"
    "ds_read_b128 v[124:127], v2 offset:39936\n"
    "buffer_load_dwordx4 a[160:163], v2, s[52:55], s8 offen sc1\n"
    "v_mfma_f32_32x32x16_bf16 v[56:71], v[136:139], a[140:143], v[56:71]\n"
    "ds_read_b128 v[128:131], v2 offset:16384\n"
    "buffer_load_dwordx4 a[168:171], v2, s[52:55], s8 offen offset:1024 sc1\n"
    "s_waitcnt lgkmcnt(3)\n"
    "v_mfma_f32_32x32x16_bf16 v[40:55], v[140:143], a[148:151], v[40:55]\n"
    "ds_read_b128 v[132:135], v2 offset:49152\n"
    "s_waitcnt vmcnt(9)\n"
    "v_accvgpr_read_b32 v148, a195\n"
    "s_waitcnt vmcnt(8)\n"
    "v_accvgpr_read_b32 v149, a203\n"
    "s_waitcnt vmcnt(7)\n"
    "v_accvgpr_read_b32 v150, a211\n"
    "v_mfma_f32_32x32x16_bf16 v[56:71], v[144:147], a[148:151], v[56:71]\n"
    "ds_read_b128 v[136:139], v2 offset:18432\n"
    "s_waitcnt vmcnt(6)\n"
    "v_accvgpr_read_b32 v151, a219\n"
    "buffer_load_dwordx4 a[176:179], v2, s[52:55], s8 offen offset:2048 sc1\n"
    "s_waitcnt lgkmcnt(3)\n"
    "v_mfma_f32_32x32x16_bf16 v[40:55], v[120:123], a[156:159], v[40:55]\n"
    "ds_read_b128 v[140:143], v2 offset:51200\n"
    "v_cmp_eq_u32 vcc, s66, v148\n"
    "v_cmp_eq_u32 s[78:79], s66, v149\n"
    "v_cmp_eq_u32 s[80:81], s66, v150\n"
    "v_cmp_eq_u32 s[82:83], s66, v151\n"
    "s_or_b64 s[80:81], s[80:81], s[82:83]\n"
    "s_or_b64 s[78:79], s[78:79], s[80:81]\n"
    "s_or_b64 vcc, vcc, s[78:79]\n"
    "s_cbranch_vccnz Lslow49_%=\n"
    "Lback50_%=:\n"
    "v_mfma_f32_32x32x16_bf16 v[56:71], v[124:127], a[156:159], v[56:71]\n"
    "ds_read_b128 v[144:147], v2 offset:20480\n"
    "buffer_load_dwordx4 a[184:187], v2, s[52:55], s8 offen offset:3072 sc1\n"
    "s_waitcnt lgkmcnt(3)\n"
    "v_mfma_f32_32x32x16_bf16 v[40:55], v[128:131], a[192:195], v[40:55]\n"
    "ds_read_b128 v[120:123], v2 offset:53248\n"
    "buffer_load_dwordx4 a[164:167], v15, s[52:55], s8 offen sc1\n"
    "v_mfma_f32_32x32x16_bf16 v[56:71], v[132:135], a[192:195], v[56:71]\n"
    "ds_read_b128 v[124:127], v2 offset:22528\n"
    "buffer_load_dwordx4 a[172:175], v15, s[52:55], s8 offen offset:1024 sc1\n"
    "s_waitcnt lgkmcnt(3)\n"
    "v_mfma_f32_32x32x16_bf16 v[40:55], v[136:139], a[200:203], v[40:55]\n"
    "ds_read_b128 v[128:131], v2 offset:55296\n"
    "buffer_load_dwordx4 a[180:183], v15, s[52:55], s8 offen offset:2048 sc1\n"
    "v_mfma_f32_32x32x16_bf16 v[56:71], v[140:143], a[200:203], v[56:71]\n"
    "ds_read_b128 v[132:135], v2 offset:17408\n"
    "buffer_load_dwordx4 a[188:191], v15, s[52:55], s8 offen offset:3072 sc1\n"
    "s_waitcnt lgkmcnt(3)\n"
    "v_mfma_f32_32x32x16_bf16 v[40:55], v[144:147], a[208:211], v[40:55]\n"
    "ds_read_b128 v[136:139], v2 offset:50176\n"
    "s_waitcnt vmcnt(11)\n"
    "v_accvgpr_read_b32 v148, a199\n"
    "s_waitcnt vmcnt(10)\n"
    "v_accvgpr_read_b32 v149, a207\n"
    "s_waitcnt vmcnt(9)\n"
    "v_accvgpr_read_b32 v150, a215\n"
    "v_mfma_f32_32x32x16_bf16 v[56:71], v[120:123], a[208:211], v[56:71]\n"
    "ds_read_b128 v[140:143], v2 offset:19456\n"
    "s_waitcnt vmcnt(8)\n"
    "v_accvgpr_read_b32 v151, a223\n"
    "s_add_u32 m0, s72, 0x19000\n"
    "s_nop 0\n"
    "buffer_load_dwordx4 v2, s[52:55], s89 offen sc1 lds\n"
    "s_waitcnt lgkmcnt(3)\n"
    "v_mfma_f32_32x32x16_bf16 v[40:55], v[124:127], a[216:219], v[40:55]\n"
    "ds_read_b128 v[144:147], v2 offset:52224\n"
    "v_cmp_eq_u32 vcc, s66, v148\n"
    "v_cmp_eq_u32 s[78:79], s66, v149\n"
    "v_cmp_eq_u32 s[80:81], s66, v150\n"
    "v_cmp_eq_u32 s[82:83], s66, v151\n"
    "s_or_b64 s[80:81], s[80:81], s[82:83]\n"
    "s_or_b64 s[78:79], s[78:79], s[80:81]\n"
    "s_or_b64 vcc, vcc, s[78:79]\n"
    "s_cbranch_vccnz Lslow52_%=\n"
    "Lback53_%=:\n"
    "v_mfma_f32_32x32x16_bf16 v[56:71], v[128:131], a[216:219], v[56:71]\n"
    "ds_read_b128 v[120:123], v2 offset:21504\n"
    "buffer_load_dwordx4 v2, s[52:55], s89 offen offset:1024 sc1 lds\n"
    "s_waitcnt lgkmcnt(3)\n"
    "v_mfma_f32_32x32x16_bf16 v[40:55], v[132:135], a[196:199], v[40:55]\n"
    "ds_read_b128 v[124:127], v2 offset:54272\n"
    "buffer_load_dwordx4 a[224:227], v2, s[52:55], s75 offen sc1\n"
    "v_mfma_f32_32x32x16_bf16 v[56:71], v[136:139], a[196:199], v[56:71]\n"
    "ds_read_b128 v[128:131], v2 offset:23552\n"
    "buffer_load_dwordx4 a[232:235], v2, s[52:55], s75 offen offset:1024 sc1\n"
    "s_waitcnt lgkmcnt(3)\n"
    "v_mfma_f32_32x32x16_bf16 v[40:55], v[140:143], a[204:207], v[40:55]\n"
    "ds_read_b128 v[132:135], v2 offset:56320\n"
    "buffer_load_dwordx4 a[240:243], v2, s[52:55], s75 offen offset:2048 sc1\n"
    "v_mfma_f32_32x32x16_bf16 v[56:71], v[144:147], a[204:207], v[56:71]\n"
    "ds_read_b128 v[136:139], v2 offset:8192\n"
    "buffer_load_dwordx4 a[248:251], v2, s[52:55], s75 offen offset:3072 sc1\n"
    "s_waitcnt lgkmcnt(3)\n"
    "v_mfma_f32_32x32x16_bf16 v[40:55], v[120:123], a[212:215], v[40:55]\n"
    "ds_read_b128 v[140:143], v2 offset:40960\n"
    "s_waitcnt vmcnt(13)\n"
    "v_accvgpr_read_b32 v148, a163\n"
    "s_waitcnt vmcnt(12)\n"
    "v_accvgpr_read_b32 v149, a171\n"
    "s_waitcnt vmcnt(11)\n"
    "v_accvgpr_read_b32 v150, a179\n"
    "v_mfma_f32_32x32x16_bf16 v[56:71], v[124:127], a[212:215], v[56:71]\n"
    "ds_read_b128 v[144:147], v2 offset:10240\n"
    "s_waitcnt vmcnt(10)\n"
    "v_accvgpr_read_b32 v151, a187\n"
    "buffer_load_dwordx4 v2, s[52:55], s89 offen offset:2048 sc1 lds\n"
    "s_waitcnt lgkmcnt(3)\n"
    "v_mfma_f32_32x32x16_bf16 v[40:55], v[128:131], a[220:223], v[40:55]\n"
    "ds_read_b128 v[120:123], v2 offset:43008\n"
    "v_cmp_eq_u32 vcc, s66, v148\n"
    "v_cmp_eq_u32 s[78:79], s66, v149\n"
    "v_cmp_eq_u32 s[80:81], s66, v150\n"
    "v_cmp_eq_u32 s[82:83], s66, v151\n"
    "s_or_b64 s[80:81], s[80:81], s[82:83]\n"
    "s_or_b64 s[78:79], s[78:79], s[80:81]\n"
    "s_or_b64 vcc, vcc, s[78:79]\n"
    "s_cbranch_vccnz Lslow55_%=\n"
    "Lback56_%=:\n"
    "v_mfma_f32_32x32x16_bf16 v[56:71], v[132:135], a[220:223], v[56:71]\n"
    "ds_read_b128 v[124:127], v2 offset:12288\n"
    "buffer_load_dwordx4 v2, s[52:55], s89 offen offset:3072 sc1 lds\n"
    "s_add_u32 s89, s19, 0x20000\n"
    "s_waitcnt lgkmcnt(3)\n"
    "v_mfma_f32_32x32x16_bf16 v[40:55], v[136:139], a[160:163], v[40:55]\n"
    "ds_read_b128 v[128:131], v2 offset:45056\n"
    "buffer_load_dwordx4 a[228:231], v15, s[52:55], s75 offen sc1\n"
    "v_mfma_f32_32x32x16_bf16 v[56:71], v[140:143], a[160:163], v[56:71]\n"
    "ds_read_b128 v[132:135], v2 offset:14336\n"
    "buffer_load_dwordx4 a[236:239], v15, s[52:55], s75 offen offset:1024 sc1\n"
    "s_waitcnt lgkmcnt(3)\n"
    "v_mfma_f32_32x32x16_bf16 v[40:55], v[144:147], a[168:171], v[40:55]\n"
    "ds_read_b128 v[136:139], v2 offset:47104\n"
    "buffer_load_dwordx4 a[244:247], v15, s[52:55], s75 offen offset:2048 sc1\n"
    "v_mfma_f32_32x32x16_bf16 v[56:71], v[120:123], a[168:171], v[56:71]\n"
    "ds_read_b128 v[140:143], v2 offset:9216\n"
    "buffer_load_dwordx4 a[252:255], v15, s[52:55], s75 offen offset:3072 sc1\n"
    "s_waitcnt lgkmcnt(3)\n"
    "v_mfma_f32_32x32x16_bf16 v[40:55], v[124:127], a[176:179], v[40:55]\n"
    "ds_read_b128 v[144:147], v2 offset:41984\n"
    "s_waitcnt vmcnt(15)\n"
    "v_accvgpr_read_b32 v148, a167\n"
    "s_waitcnt vmcnt(14)\n"
    "v_accvgpr_read_b32 v149, a175\n"
    "s_waitcnt vmcnt(13)\n"
    "v_accvgpr_read_b32 v150, a183\n"
    "v_mfma_f32_32x32x16_bf16 v[56:71], v[128:131], a[176:179], v[56:71]\n"
    "ds_read_b128 v[120:123], v2 offset:11264\n"
    "s_waitcnt vmcnt(12)\n"
    "v_accvgpr_read_b32 v151, a191\n"
    "buffer_load_dwordx4 v[104:107], v2, s[52:55], s14 offen sc1\n"
    "s_waitcnt lgkmcnt(3)\n"
    "v_mfma_f32_32x32x16_bf16 v[40:55], v[132:135], a[184:187], v[40:55]\n"
    "ds_read_b128 v[124:127], v2 offset:44032\n"
    "v_cmp_eq_u32 vcc, s66, v148\n"
    "v_cmp_eq_u32 s[78:79], s66, v149\n"
    "v_cmp_eq_u32 s[80:81], s66, v150\n"
    "v_cmp_eq_u32 s[82:83], s66, v151\n"
    "s_or_b64 s[80:81], s[80:81], s[82:83]\n"
    "s_or_b64 s[78:79], s[78:79], s[80:81]\n"
    "s_or_b64 vcc, vcc, s[78:79]\n"
    "s_cbranch_vccnz Lslow58_%=\n"
    "Lback59_%=:\n"
    "v_mfma_f32_32x32x16_bf16 v[56:71], v[136:139], a[184:187], v[56:71]\n"
    "ds_read_b128 v[128:131], v2 offset:13312\n"
    "buffer_load_dwordx4 v[108:111], v2, s[52:55], s14 offen offset:1024 sc1\n"
    "s_waitcnt lgkmcnt(3)\n"
    "v_mfma_f32_32x32x16_bf16 v[40:55], v[140:143], a[164:167], v[40:55]\n"
    "ds_read_b128 v[132:135], v2 offset:46080\n"
    "buffer_load_dwordx4 v[112:115], v2, s[52:55], s14 offen offset:2048 sc1\n"
    "v_mfma_f32_32x32x16_bf16 v[56:71], v[144:147], a[164:167], v[56:71]\n"
    "ds_read_b128 v[136:139], v2 offset:15360\n"
    "buffer_load_dwordx4 v[116:119], v2, s[52:55], s14 offen offset:3072 sc1\n"
    "s_waitcnt lgkmcnt(3)\n"
    "v_mfma_f32_32x32x16_bf16 v[40:55], v[120:123], a[172:175], v[40:55]\n"
    "ds_read_b128 v[140:143], v2 offset:48128\n"
    "buffer_load_dwordx4 v[210:213], v2, s[52:55], s13 offen sc1\n"
    "v_mfma_f32_32x32x16_bf16 v[56:71], v[124:127], a[172:175], v[56:71]\n"
    "ds_read_b128 v[144:147], v2 offset:24576\n"
    "buffer_load_dwordx4 v[214:217], v2, s[52:55], s13 offen offset:1024 sc1\n"
    "s_waitcnt lgkmcnt(3)\n"
    "v_mfma_f32_32x32x16_bf16 v[40:55], v[128:131], a[180:183], v[40:55]\n"
    "ds_read_b128 v[120:123], v2 offset:57344\n"
    "s_waitcnt vmcnt(15)\n"
    "v_accvgpr_read_b32 v148, a227\n"
    "s_waitcnt vmcnt(14)\n"
    "v_accvgpr_read_b32 v149, a235\n"
    "s_waitcnt vmcnt(13)\n"
    "v_accvgpr_read_b32 v150, a243\n"
    "v_mfma_f32_32x32x16_bf16 v[56:71], v[132:135], a[180:183], v[56:71]\n"
    "ds_read_b128 v[124:127], v2 offset:26624\n"
    "s_waitcnt vmcnt(12)\n"
    "v_accvgpr_read_b32 v151, a251\n"
    "buffer_load_dwordx4 v[218:221], v2, s[52:55], s13 offen offset:2048 sc1\n"
    "s_waitcnt lgkmcnt(3)\n"
    "v_mfma_f32_32x32x16_bf16 v[40:55], v[136:139], a[188:191], v[40:55]\n"
    "ds_read_b128 v[128:131], v2 offset:59392\n"
    "v_cmp_eq_u32 vcc, s66, v148\n"
    "v_cmp_eq_u32 s[78:79], s66, v149\n"
    "v_cmp_eq_u32 s[80:81], s66, v150\n"
    "v_cmp_eq_u32 s[82:83], s66, v151\n"
    "s_or_b64 s[80:81], s[80:81], s[82:83]\n"
    "s_or_b64 s[78:79], s[78:79], s[80:81]\n"
    "s_or_b64 vcc, vcc, s[78:79]\n"
    "s_cbranch_vccnz Lslow61_%=\n"
    "Lback62_%=:\n"
    "v_mfma_f32_32x32x16_bf16 v[56:71], v[140:143], a[188:191], v[56:71]\n"
    "ds_read_b128 v[132:135], v2 offset:28672\n"
    "buffer_load_dwordx4 v[222:225], v2, s[52:55], s13 offen offset:3072 sc1\n"
    "s_waitcnt lgkmcnt(3)\n"
    "v_mfma_f32_32x32x16_bf16 v[40:55], v[144:147], a[224:227], v[40:55]\n"
    "ds_read_b128 v[136:139], v2 offset:61440\n"
    "s_add_u32 m0, s72, 0x20000\n"
    "s_nop 0\n"
    "buffer_load_dwordx4 v2, s[52:55], s89 offen sc1 lds\n"
    "v_mfma_f32_32x32x16_bf16 v[56:71], v[120:123], a[224:227], v[56:71]\n"
    "ds_read_b128 v[140:143], v2 offset:30720\n"
    "s_waitcnt lgkmcnt(3)\n"
    "v_mfma_f32_32x32x16_bf16 v[40:55], v[124:127], a[232:235], v[40:55]\n"
    "ds_read_b128 v[144:147], v2 offset:63488\n"
    "buffer_load_dwordx4 v2, s[52:55], s89 offen offset:1024 sc1 lds\n"
    "v_mfma_f32_32x32x16_bf16 v[56:71], v[128:131], a[232:235], v[56:71]\n"
    "ds_read_b128 v[120:123], v2 offset:25600\n"
    "s_waitcnt lgkmcnt(3)\n"
    "v_mfma_f32_32x32x16_bf16 v[40:55], v[132:135], a[240:243], v[40:55]\n"
    "ds_read_b128 v[124:127], v2 offset:58368\n"
    "s_waitcnt vmcnt(13)\n"
    "v_accvgpr_read_b32 v148, a231\n"
    "s_waitcnt vmcnt(12)\n"
    "v_accvgpr_read_b32 v149, a239\n"
    "s_waitcnt vmcnt(11)\n"
    "v_accvgpr_read_b32 v150, a247\n"
    "v_mfma_f32_32x32x16_bf16 v[56:71], v[136:139], a[240:243], v[56:71]\n"
    "ds_read_b128 v[128:131], v2 offset:27648\n"
    "s_waitcnt vmcnt(10)\n"
    "v_accvgpr_read_b32 v151, a255\n"
    "buffer_load_dwordx4 v2, s[52:55], s89 offen offset:2048 sc1 lds\n"
    "s_waitcnt lgkmcnt(3)\n"
    "v_mfma_f32_32x32x16_bf16 v[40:55], v[140:143], a[248:251], v[40:55]\n"
    "ds_read_b128 v[132:135], v2 offset:60416\n"
    "v_cmp_eq_u32 vcc, s66, v148\n"
    "v_cmp_eq_u32 s[78:79], s66, v149\n"
    "v_cmp_eq_u32 s[80:81], s66, v150\n"
    "v_cmp_eq_u32 s[82:83], s66, v151\n"
    "s_or_b64 s[80:81], s[80:81], s[82:83]\n"
    "s_or_b64 s[78:79], s[78:79], s[80:81]\n"
    "s_or_b64 vcc, vcc, s[78:79]\n"
    "s_cbranch_vccnz Lslow64_%=\n"
    "Lback65_%=:\n"
    "v_mfma_f32_32x32x16_bf16 v[56:71], v[144:147], a[248:251], v[56:71]\n"
    "ds_read_b128 v[136:139], v2 offset:29696\n"
    "buffer_load_dwordx4 v2, s[52:55], s89 offen offset:3072 sc1 lds\n"
    "s_add_u32 s89, s19, 0x21000\n"
    "s_waitcnt lgkmcnt(3)\n"
    "v_mfma_f32_32x32x16_bf16 v[40:55], v[120:123], a[228:231], v[40:55]\n"
    "ds_read_b128 v[140:143], v2 offset:62464\n"
    "v_mfma_f32_32x32x16_bf16 v[56:71], v[124:127], a[228:231], v[56:71]\n"
    "ds_read_b128 v[144:147], v2 offset:31744\n"
    "s_add_u32 m0, s72, 0x21000\n"
    "s_nop 0\n"
    "buffer_load_dwordx4 v2, s[52:55], s89 offen sc1 lds\n"
    "s_waitcnt lgkmcnt(3)\n"
    "v_mfma_f32_32x32x16_bf16 v[40:55], v[128:131], a[236:239], v[40:55]\n"
    "ds_read_b128 v[120:123], v2 offset:64512\n"
    "v_mfma_f32_32x32x16_bf16 v[56:71], v[132:135], a[236:239], v[56:71]\n"
    "s_barrier\n"
    "ds_read_b128 v[124:127], v13\n"
    "buffer_load_dwordx4 v2, s[52:55], s89 offen offset:1024 sc1 lds\n"
    "s_waitcnt lgkmcnt(3)\n"
    "v_mfma_f32_32x32x16_bf16 v[40:55], v[136:139], a[244:247], v[40:55]\n"
    "ds_read_b128 v[128:131], v13 offset:1024\n"
    "v_mfma_f32_32x32x16_bf16 v[56:71], v[140:143], a[244:247], v[56:71]\n"
    "ds_read_b128 v[132:135], v13 offset:2048\n"
    "buffer_load_dwordx4 v2, s[52:55], s89 offen offset:2048 sc1 lds\n"
    "s_waitcnt lgkmcnt(3)\n"
    "v_mfma_f32_32x32x16_bf16 v[40:55], v[144:147], a[252:255], v[40:55]\n"
    "ds_read_b128 v[136:139], v13 offset:3072\n"
    "v_mfma_f32_32x32x16_bf16 v[56:71], v[120:123], a[252:255], v[56:71]\n"
    "s_barrier\n"
    "ds_read_b128 v[140:143], v13 offset:4096\n"
    "buffer_load_dwordx4 v2, s[52:55], s89 offen offset:3072 sc1 lds\n"
    "s_add_u32 s89, s19, 0x28000\n"
    "s_waitcnt lgkmcnt(3)\n"
    "v_mfma_f32_32x32x16_bf16 v[72:87], v[124:127], a[128:131], 0\n"
    "ds_read_b128 v[144:147], v13 offset:5120\n"
    "v_mfma_f32_32x32x16_bf16 v[72:87], v[128:131], a[132:135], v[72:87]\n"
    "ds_read_b128 v[120:123], v13 offset:6144\n"
    "s_add_u32 m0, s72, 0\n"
    "s_nop 0\n"
    "buffer_load_dwordx4 v2, s[52:55], s89 offen sc1 lds\n"
    "s_waitcnt lgkmcnt(3)\n"
    "v_mfma_f32_32x32x16_bf16 v[72:87], v[132:135], a[136:139], v[72:87]\n"
    "ds_read_b128 v[124:127], v13 offset:7168\n"
    "v_mfma_f32_32x32x16_bf16 v[72:87], v[136:139], a[140:143], v[72:87]\n"
    "ds_read_b128 v[128:131], v13 offset:8192\n"
    "buffer_load_dwordx4 v2, s[52:55], s89 offen offset:1024 sc1 lds\n"
    "s_waitcnt vmcnt(17)\n"
    "v_xor_b32 v9, s66, v105\n"
    "v_bitop3_b32 v9, v107, s66, v9 bitop3:0xbe\n"
    "s_waitcnt vmcnt(16)\n"
    "v_bitop3_b32 v9, v109, s66, v9 bitop3:0xbe\n"
    "s_waitcnt lgkmcnt(3)\n"
    "v_mfma_f32_32x32x16_bf16 v[72:87], v[140:143], a[144:147], v[72:87]\n"
    "ds_read_b128 v[132:135], v13 offset:9216\n"
    "v_bitop3_b32 v9, v111, s66, v9 bitop3:0xbe\n"
    "s_waitcnt vmcnt(15)\n"
    "v_bitop3_b32 v9, v113, s66, v9 bitop3:0xbe\n"
    "v_bitop3_b32 v9, v115, s66, v9 bitop3:0xbe\n"
    "s_waitcnt vmcnt(14)\n"
    "v_bitop3_b32 v9, v117, s66, v9 bitop3:0xbe\n"
    "v_mfma_f32_32x32x16_bf16 v[72:87], v[144:147], a[148:151], v[72:87]\n"
    "ds_read_b128 v[136:139], v13 offset:10240\n"
    "buffer_load_dwordx4 v2, s[52:55], s89 offen offset:2048 sc1 lds\n"
    "v_bitop3_b32 v9, v119, s66, v9 bitop3:0xbe\n"
    "s_waitcnt vmcnt(14)\n"
    "v_bitop3_b32 v9, v211, s66, v9 bitop3:0xbe\n"
    "v_bitop3_b32 v9, v213, s66, v9 bitop3:0xbe\n"
    "s_waitcnt lgkmcnt(3)\n"
    "v_mfma_f32_32x32x16_bf16 v[72:87], v[120:123], a[152:155], v[72:87]\n"
    "ds_read_b128 v[140:143], v13 offset:11264\n"
    "s_waitcnt vmcnt(13)\n"
    "v_bitop3_b32 v9, v215, s66, v9 bitop3:0xbe\n"
    "v_bitop3_b32 v9, v217, s66, v9 bitop3:0xbe\n"
    "s_waitcnt vmcnt(12)\n"
    "v_bitop3_b32 v9, v219, s66, v9 bitop3:0xbe\n"
    "v_bitop3_b32 v9, v221, s66, v9 bitop3:0xbe\n"
    "v_mfma_f32_32x32x16_bf16 v[72:87], v[124:127], a[156:159], v[72:87]\n"
    "ds_read_b128 v[144:147], v13 offset:12288\n"
    "buffer_load_dwordx4 v2, s[52:55], s89 offen offset:3072 sc1 lds\n"
    "s_add_u32 s89, s19, 0x29000\n"
    "s_waitcnt vmcnt(12)\n"
    "v_bitop3_b32 v9, v223, s66, v9 bitop3:0xbe\n"
    "v_bitop3_b32 v9, v225, s66, v9 bitop3:0xbe\n"
    "s_waitcnt lgkmcnt(3)\n"
    "v_mfma_f32_32x32x16_bf16 v[72:87], v[128:131], a[160:163], v[72:87]\n"
    "ds_read_b128 v[120:123], v13 offset:13312\n"
    "v_cmp_ne_u32 vcc, 0, v9\n"
    "s_cbranch_vccnz Lslow67_%=\n"
    "Lback68_%=:\n"
    "v_add_f32 v104, v104, v210\n"
    "v_add_f32 v106, v106, v212\n"
    "v_add_f32 v108, v108, v214\n"
    "v_add_f32 v110, v110, v216\n"
    "v_mfma_f32_32x32x16_bf16 v[72:87], v[132:135], a[164:167], v[72:87]\n"
    "ds_read_b128 v[124:127], v13 offset:14336\n"
    "s_add_u32 m0, s72, 0x1000\n"
    "s_nop 0\n"
    "buffer_load_dwordx4 v2, s[52:55], s89 offen sc1 lds\n"
    "v_add_f32 v112, v112, v218\n"
    "v_add_f32 v114, v114, v220\n"
    "s_waitcnt lgkmcnt(3)\n"
    "v_mfma_f32_32x32x16_bf16 v[72:87], v[136:139], a[168:171], v[72:87]\n"
    "ds_read_b128 v[128:131], v13 offset:15360\n"
    "v_add_f32 v116, v116, v222\n"
    "v_add_f32 v118, v118, v224\n"
    "v_add_f32 v104, v104, v112\n"
    "v_add_f32 v106, v106, v114\n"
    "v_add_f32 v108, v108, v116\n"
    "v_add_f32 v110, v110, v118\n"
    "v_mfma_f32_32x32x16_bf16 v[72:87], v[140:143], a[172:175], v[72:87]\n"
    "ds_read_b128 v[132:135], v13 offset:16384\n"
    "buffer_load_dwordx4 v2, s[52:55], s89 offen offset:1024 sc1 lds\n"
    "v_add_f32 v104, v104, v108\n"
    "v_add_f32 v106, v106, v110\n"
    "s_nop 1\n"
    "v_permlane32_swap_b32 v104, v106\n"
    "s_waitcnt lgkmcnt(3)\n"
    "v_mfma_f32_32x32x16_bf16 v[72:87], v[144:147], a[176:179], v[72:87]\n"
    "ds_read_b128 v[136:139], v13 offset:17408\n"
    "v_add_f32 v104, v104, v106\n"
    "v_mov_b32 v105, v104\n"
    "s_nop 1\n"
    "v_permlane32_swap_b32 v104, v105\n"
    "v_mfma_f32_32x32x16_bf16 v[72:87], v[120:123], a[180:183], v[72:87]\n"
    "ds_read_b128 v[140:143], v13 offset:18432\n"
    "buffer_load_dwordx4 v2, s[52:55], s89 offen offset:2048 sc1 lds\n"
    "s_waitcnt lgkmcnt(3)\n"
    "v_mfma_f32_32x32x16_bf16 v[72:87], v[124:127], a[184:187], v[72:87]\n"
    "ds_read_b128 v[144:147], v13 offset:19456\n"
    "v_mul_f32 v104, 0x3b000000, v104\n"
    "v_mul_f32 v105, 0x3b000000, v105\n"
    "v_fma_f32 v105, -v104, v104, v105\n"
    "v_max_f32 v105, 0, v105\n"
    "v_add_f32 v105, 0x3727c5ac, v105\n"
    "v_mfma_f32_32x32x16_bf16 v[72:87], v[128:131], a[188:191], v[72:87]\n"
    "ds_read_b128 v[120:123], v13 offset:20480\n"
    "buffer_load_dwordx4 v2, s[52:55], s89 offen offset:3072 sc1 lds\n"
    "s_add_u32 s89, s19, 0x30000\n"
    "s_waitcnt lgkmcnt(3)\n"
    "v_mfma_f32_32x32x16_bf16 v[72:87], v[132:135], a[192:195], v[72:87]\n"
    "ds_read_b128 v[124:127], v13 offset:21504\n"
    "v_rsq_f32 v11, v105\n"
    "s_nop 0\n"
    "v_mul_f32 v12, v105, v11\n"
    "v_mul_f32 v11, 0xbfb8aa3b, v11\n"
    "v_mfma_f32_32x32x16_bf16 v[72:87], v[136:139], a[196:199], v[72:87]\n"
    "ds_read_b128 v[128:131], v13 offset:22528\n"
    "s_add_u32 m0, s72, 0x8000\n"
    "s_nop 0\n"
    "buffer_load_dwordx4 v2, s[52:55], s89 offen sc1 lds\n"
    "s_waitcnt lgkmcnt(3)\n"
    "v_mfma_f32_32x32x16_bf16 v[72:87], v[140:143], a[200:203], v[72:87]\n"
    "ds_read_b128 v[132:135], v13 offset:23552\n"
    "v_cvt_pk_bf16_f32 v204, -v104, -v104\n"
    "v_and_b32 v8, 0xffff0000, v204\n"
    "v_sub_f32 v8, -v104, v8\n"
    "v_cvt_pk_bf16_f32 v205, v8, v8\n"
    "v_mfma_f32_32x32x16_bf16 v[72:87], v[144:147], a[204:207], v[72:87]\n"
    "ds_read_b128 v[136:139], v13 offset:24576\n"
    "buffer_load_dwordx4 v2, s[52:55], s89 offen offset:1024 sc1 lds\n"
    "v_cmp_gt_u32 vcc, 32, v1\n"
    "s_nop 1\n"
    "v_cndmask_b32 v204, v205, v204, vcc\n"
    "v_mov_b32 v205, v204\n"
    "s_waitcnt lgkmcnt(3)\n"
    "v_mfma_f32_32x32x16_bf16 v[72:87], v[120:123], a[208:211], v[72:87]\n"
    "ds_read_b128 v[140:143], v13 offset:25600\n"
    "v_mov_b32 v206, v204\n"
    "v_mov_b32 v207, v204\n"
    "v_mfma_f32_32x32x16_bf16 v[72:87], v[124:127], a[212:215], v[72:87]\n"
    "ds_read_b128 v[144:147], v13 offset:26624\n"
    "buffer_load_dwordx4 v2, s[52:55], s89 offen offset:2048 sc1 lds\n"
    "s_waitcnt lgkmcnt(3)\n"
    "v_mfma_f32_32x32x16_bf16 v[72:87], v[128:131], a[216:219], v[72:87]\n"
    "ds_read_b128 v[120:123], v13 offset:27648\n"
    "v_mfma_f32_32x32x16_bf16 v[72:87], v[132:135], a[220:223], v[72:87]\n"
    "ds_read_b128 v[124:127], v13 offset:28672\n"
    "buffer_load_dwordx4 v2, s[52:55], s89 offen offset:3072 sc1 lds\n"
    "s_add_u32 s89, s19, 0x31000\n"
    "s_waitcnt lgkmcnt(3)\n"
    "v_mfma_f32_32x32x16_bf16 v[72:87], v[136:139], a[224:227], v[72:87]\n"
    "ds_read_b128 v[128:131], v13 offset:29696\n"
    "v_mfma_f32_32x32x16_bf16 v[72:87], v[140:143], a[228:231], v[72:87]\n"
    "ds_read_b128 v[132:135], v13 offset:30720\n"
    "s_add_u32 m0, s72, 0x9000\n"
    "s_nop 0\n"
    "buffer_load_dwordx4 v2, s[52:55], s89 offen sc1 lds\n"
    "s_waitcnt lgkmcnt(3)\n"
    "v_mfma_f32_32x32x16_bf16 v[72:87], v[144:147], a[232:235], v[72:87]\n"
    "ds_read_b128 v[136:139], v13 offset:31744\n"
    "v_mfma_f32_32x32x16_bf16 v[72:87], v[120:123], a[236:239], v[72:87]\n"
    "ds_read_b128 v[140:143], v13 offset:32768\n"
    "buffer_load_dwordx4 v2, s[52:55], s89 offen offset:1024 sc1 lds\n"
    "s_waitcnt lgkmcnt(3)\n"
    "v_mfma_f32_32x32x16_bf16 v[72:87], v[124:127], a[240:243], v[72:87]\n"
    "ds_read_b128 v[144:147], v13 offset:33792\n"
    "v_mfma_f32_32x32x16_bf16 v[72:87], v[128:131], a[244:247], v[72:87]\n"
    "ds_read_b128 v[120:123], v13 offset:34816\n"
    "buffer_load_dwordx4 v2, s[52:55], s89 offen offset:2048 sc1 lds\n"
    "v_mfma_f32_32x32x16_bf16 v[40:55], v[176:179], v[204:207], v[40:55]\n"
    "v_mfma_f32_32x32x16_bf16 v[56:71], v[180:183], v[204:207], v[56:71]\n"
    "buffer_load_dwordx4 v2, s[52:55], s89 offen offset:3072 sc1 lds\n"
    "s_waitcnt lgkmcnt(3)\n"
    "v_mfma_f32_32x32x16_bf16 v[72:87], v[132:135], a[248:251], v[72:87]\n"
    "ds_read_b128 v[124:127], v13 offset:35840\n"
    "s_nop 6\n"
    "v_mul_f32 v104, v11, v40\n"
    "v_mul_f32 v105, v11, v41\n"
    "v_mul_f32 v106, v11, v42\n"
    "v_mul_f32 v107, v11, v43\n"
    "v_mfma_f32_32x32x16_bf16 v[72:87], v[136:139], a[252:255], v[72:87]\n"
    "s_waitcnt vmcnt(16)\n"
    "s_barrier\n"
    "ds_read_b128 v[128:131], v13 offset:36864\n"
    "s_add_u32 s88, s20, 0\n"
    "s_add_u32 m0, s9, 0x10000\n"
    "s_nop 0\n"
    "buffer_load_dwordx4 v16, s[48:51], s88 offen lds\n"
    "v_mfma_f32_32x32x16_bf16 v[72:87], v[184:187], v[204:207], v[72:87]\n"
    "v_exp_f32 v104, v104\n"
    "v_mul_f32 v108, v11, v44\n"
    "v_exp_f32 v105, v105\n"
    "v_mul_f32 v109, v11, v45\n"
    "s_waitcnt lgkmcnt(3)\n"
    "v_mfma_f32_32x32x16_bf16 v[88:103], v[140:143], a[128:131], 0\n"
    "ds_read_b128 v[132:135], v13 offset:37888\n"
    "buffer_load_dwordx4 v17, s[48:51], s88 offen offset:1024 lds\n"
    "v_exp_f32 v106, v106\n"
    "v_mul_f32 v110, v11, v46\n"
    "v_mfma_f32_32x32x16_bf16 v[88:103], v[144:147], a[132:135], v[88:103]\n"
    "ds_read_b128 v[136:139], v13 offset:38912\n"
    "v_exp_f32 v107, v107\n"
    "v_mul_f32 v111, v11, v47\n"
    "v_fma_f32 v104, v104, v12, v12\n"
    "v_exp_f32 v108, v108\n"
    "s_waitcnt lgkmcnt(3)\n"
    "v_mfma_f32_32x32x16_bf16 v[88:103], v[120:123], a[136:139], v[88:103]\n"
    "ds_read_b128 v[140:143], v13 offset:39936\n"
    "buffer_load_dwordx4 v18, s[48:51], s88 offen offset:2048 lds\n"
    "v_fma_f32 v105, v105, v12, v12\n"
    "v_exp_f32 v109, v109\n"
    "v_fma_f32 v106, v106, v12, v12\n"
    "v_mfma_f32_32x32x16_bf16 v[88:103], v[124:127], a[140:143], v[88:103]\n"
    "ds_read_b128 v[144:147], v13 offset:40960\n"
    "v_exp_f32 v110, v110\n"
    "v_fma_f32 v107, v107, v12, v12\n"
    "v_exp_f32 v111, v111\n"
    "s_waitcnt lgkmcnt(3)\n"
    "v_mfma_f32_32x32x16_bf16 v[88:103], v[128:131], a[144:147], v[88:103]\n"
    "ds_read_b128 v[120:123], v13 offset:41984\n"
    "buffer_load_dwordx4 v19, s[48:51], s88 offen offset:3072 lds\n"
    "s_add_u32 s88, s20, 0x4000\n"
    "v_rcp_f32 v104, v104\n"
    "v_fma_f32 v108, v108, v12, v12\n"
    "v_mfma_f32_32x32x16_bf16 v[88:103], v[132:135], a[148:151], v[88:103]\n"
    "ds_read_b128 v[124:127], v13 offset:43008\n"
    "v_rcp_f32 v105, v105\n"
    "v_fma_f32 v109, v109, v12, v12\n"
    "v_rcp_f32 v106, v106\n"
    "v_fma_f32 v110, v110, v12, v12\n"
    "s_waitcnt lgkmcnt(3)\n"
    "v_mfma_f32_32x32x16_bf16 v[88:103], v[136:139], a[152:155], v[88:103]\n"
    "ds_read_b128 v[128:131], v13 offset:44032\n"
    "s_add_u32 m0, s9, 0x14000\n"
    "s_nop 0\n"
    "buffer_load_dwordx4 v16, s[48:51], s88 offen lds\n"
    "v_rcp_f32 v107, v107\n"
    "v_mfma_f32_32x32x16_bf16 v[88:103], v[140:143], a[156:159], v[88:103]\n"
    "ds_read_b128 v[132:135], v13 offset:45056\n"
    "v_fma_f32 v111, v111, v12, v12\n"
    "v_mul_f32 v104, v104, v48\n"
    "v_rcp_f32 v108, v108\n"
    "v_mul_f32 v105, v105, v49\n"
    "s_waitcnt lgkmcnt(3)\n"
    "v_mfma_f32_32x32x16_bf16 v[88:103], v[144:147], a[160:163], v[88:103]\n"
    "ds_read_b128 v[136:139], v13 offset:46080\n"
    "buffer_load_dwordx4 v17, s[48:51], s88 offen offset:1024 lds\n"
    "v_rcp_f32 v109, v109\n"
    "v_mul_f32 v106, v106, v50\n"
    "v_mfma_f32_32x32x16_bf16 v[88:103], v[120:123], a[164:167], v[88:103]\n"
    "ds_read_b128 v[140:143], v13 offset:47104\n"
    "v_rcp_f32 v110, v110\n"
    "v_mul_f32 v107, v107, v51\n"
    "v_rcp_f32 v111, v111\n"
    "v_mul_f32 v108, v108, v52\n"
    "s_waitcnt lgkmcnt(3)\n"
    "v_mfma_f32_32x32x16_bf16 v[88:103], v[124:127], a[168:171], v[88:103]\n"
    "ds_read_b128 v[144:147], v13 offset:48128\n"
    "buffer_load_dwordx4 v18, s[48:51], s88 offen offset:2048 lds\n"
    "v_mul_f32 v109, v109, v53\n"
    "v_mul_f32 v110, v110, v54\n"
    "v_mul_f32 v111, v111, v55\n"
    "v_cvt_pk_bf16_f32 v179, v110, v111\n"
    "v_mfma_f32_32x32x16_bf16 v[88:103], v[128:131], a[172:175], v[88:103]\n"
    "ds_read_b128 v[120:123], v13 offset:49152\n"
    "v_cvt_pk_bf16_f32 v176, v104, v105\n"
    "v_cvt_pk_bf16_f32 v177, v106, v107\n"
    "v_cvt_pk_bf16_f32 v178, v108, v109\n"
    "buffer_store_dwordx4 v[176:179], v2, s[52:55], s16 offen sc1\n"
    "s_add_u32 s16, s16, 0x4000\n"
    "s_waitcnt lgkmcnt(3)\n"
    "v_mfma_f32_32x32x16_bf16 v[88:103], v[132:135], a[176:179], v[88:103]\n"
    "ds_read_b128 v[124:127], v13 offset:50176\n"
    "v_mul_f32 v104, v11, v56\n"
    "v_mul_f32 v105, v11, v57\n"
    "v_mul_f32 v106, v11, v58\n"
    "v_mul_f32 v107, v11, v59\n"
    "v_exp_f32 v104, v104\n"
    "v_mfma_f32_32x32x16_bf16 v[88:103], v[136:139], a[180:183], v[88:103]\n"
    "ds_read_b128 v[128:131], v13 offset:51200\n"
    "v_mul_f32 v108, v11, v60\n"
    "v_exp_f32 v105, v105\n"
    "v_mul_f32 v109, v11, v61\n"
    "v_exp_f32 v106, v106\n"
    "s_waitcnt lgkmcnt(3)\n"
    "v_mfma_f32_32x32x16_bf16 v[88:103], v[140:143], a[184:187], v[88:103]\n"
    "ds_read_b128 v[132:135], v13 offset:52224\n"
    "buffer_load_dwordx4 v19, s[48:51], s88 offen offset:3072 lds\n"
    "v_mul_f32 v110, v11, v62\n"
    "v_exp_f32 v107, v107\n"
    "v_mul_f32 v111, v11, v63\n"
    "v_mfma_f32_32x32x16_bf16 v[88:103], v[144:147], a[188:191], v[88:103]\n"
    "ds_read_b128 v[136:139], v13 offset:53248\n"
    "v_fma_f32 v104, v104, v12, v12\n"
    "v_exp_f32 v108, v108\n"
    "v_fma_f32 v105, v105, v12, v12\n"
    "v_exp_f32 v109, v109\n"
    "s_waitcnt lgkmcnt(3)\n"
    "v_mfma_f32_32x32x16_bf16 v[88:103], v[120:123], a[192:195], v[88:103]\n"
    "ds_read_b128 v[140:143], v13 offset:54272\n"
    "v_fma_f32 v106, v106, v12, v12\n"
    "v_exp_f32 v110, v110\n"
    "v_fma_f32 v107, v107, v12, v12\n"
    "v_exp_f32 v111, v111\n"
    "v_mfma_f32_32x32x16_bf16 v[88:103], v[124:127], a[196:199], v[88:103]\n"
    "ds_read_b128 v[144:147], v13 offset:55296\n"
    "v_rcp_f32 v104, v104\n"
    "v_fma_f32 v108, v108, v12, v12\n"
    "v_rcp_f32 v105, v105\n"
    "v_fma_f32 v109, v109, v12, v12\n"
    "s_waitcnt lgkmcnt(3)\n"
    "v_mfma_f32_32x32x16_bf16 v[88:103], v[128:131], a[200:203], v[88:103]\n"
    "ds_read_b128 v[120:123], v13 offset:56320\n"
    "v_rcp_f32 v106, v106\n"
    "v_fma_f32 v110, v110, v12, v12\n"
    "v_rcp_f32 v107, v107\n"
    "v_fma_f32 v111, v111, v12, v12\n"
    "v_mfma_f32_32x32x16_bf16 v[88:103], v[132:135], a[204:207], v[88:103]\n"
    "ds_read_b128 v[124:127], v13 offset:57344\n"
    "v_mul_f32 v104, v104, v64\n"
    "v_rcp_f32 v108, v108\n"
    "v_mul_f32 v105, v105, v65\n"
    "v_rcp_f32 v109, v109\n"
    "s_waitcnt lgkmcnt(3)\n"
    "v_mfma_f32_32x32x16_bf16 v[88:103], v[136:139], a[208:211], v[88:103]\n"
    "ds_read_b128 v[128:131], v13 offset:58368\n"
    "v_mul_f32 v106, v106, v66\n"
    "v_rcp_f32 v110, v110\n"
    "v_mul_f32 v107, v107, v67\n"
    "v_rcp_f32 v111, v111\n"
    "v_mfma_f32_32x32x16_bf16 v[88:103], v[140:143], a[212:215], v[88:103]\n"
    "ds_read_b128 v[132:135], v13 offset:59392\n"
    "v_mul_f32 v108, v108, v68\n"
    "v_mul_f32 v109, v109, v69\n"
    "v_mul_f32 v110, v110, v70\n"
    "v_mul_f32 v111, v111, v71\n"
    "v_cvt_pk_bf16_f32 v183, v110, v111\n"
    "v_cvt_pk_bf16_f32 v180, v104, v105\n"
    "s_waitcnt lgkmcnt(3)\n"
    "v_mfma_f32_32x32x16_bf16 v[88:103], v[144:147], a[216:219], v[88:103]\n"
    "ds_read_b128 v[136:139], v13 offset:60416\n"
    "v_cvt_pk_bf16_f32 v181, v106, v107\n"
    "v_cvt_pk_bf16_f32 v182, v108, v109\n"
    "buffer_store_dwordx4 v[180:183], v2, s[52:55], s16 offen sc1\n"
    "s_add_u32 s16, s16, 0x4000\n"
    "v_mul_f32 v104, v11, v72\n"
    "v_mfma_f32_32x32x16_bf16 v[88:103], v[120:123], a[220:223], v[88:103]\n"
    "ds_read_b128 v[140:143], v13 offset:61440\n"
    "v_mul_f32 v105, v11, v73\n"
    "v_mul_f32 v106, v11, v74\n"
    "v_mul_f32 v107, v11, v75\n"
    "v_exp_f32 v104, v104\n"
    "v_mul_f32 v108, v11, v76\n"
    "s_waitcnt lgkmcnt(3)\n"
    "v_mfma_f32_32x32x16_bf16 v[88:103], v[124:127], a[224:227], v[88:103]\n"
    "ds_read_b128 v[144:147], v13 offset:62464\n"
    "v_exp_f32 v105, v105\n"
    "v_mul_f32 v109, v11, v77\n"
    "v_exp_f32 v106, v106\n"
    "v_mul_f32 v110, v11, v78\n"
    "v_mfma_f32_32x32x16_bf16 v[88:103], v[128:131], a[228:231], v[88:103]\n"
    "ds_read_b128 v[120:123], v13 offset:63488\n"
    "v_exp_f32 v107, v107\n"
    "v_mul_f32 v111, v11, v79\n"
    "v_fma_f32 v104, v104, v12, v12\n"
    "v_exp_f32 v108, v108\n"
    "s_waitcnt lgkmcnt(3)\n"
    "v_mfma_f32_32x32x16_bf16 v[88:103], v[132:135], a[232:235], v[88:103]\n"
    "ds_read_b128 v[124:127], v13 offset:64512\n"
    "v_fma_f32 v105, v105, v12, v12\n"
    "v_exp_f32 v109, v109\n"
    "v_fma_f32 v106, v106, v12, v12\n"
    "v_exp_f32 v110, v110\n"
    "v_mfma_f32_32x32x16_bf16 v[88:103], v[136:139], a[236:239], v[88:103]\n"
    "ds_read_b128 v[128:131], v14\n"
    "v_fma_f32 v107, v107, v12, v12\n"
    "v_exp_f32 v111, v111\n"
    "v_rcp_f32 v104, v104\n"
    "v_fma_f32 v108, v108, v12, v12\n"
    "s_waitcnt lgkmcnt(3)\n"
    "v_mfma_f32_32x32x16_bf16 v[88:103], v[140:143], a[240:243], v[88:103]\n"
    "ds_read_b128 v[132:135], v14 offset:1024\n"
    "v_rcp_f32 v105, v105\n"
    "v_fma_f32 v109, v109, v12, v12\n"
    "v_rcp_f32 v106, v106\n"
    "v_fma_f32 v110, v110, v12, v12\n"
    "v_mfma_f32_32x32x16_bf16 v[88:103], v[144:147], a[244:247], v[88:103]\n"
    "ds_read_b128 v[136:139], v14 offset:2048\n"
    "v_rcp_f32 v107, v107\n"
    "v_fma_f32 v111, v111, v12, v12\n"
    "v_mul_f32 v104, v104, v80\n"
    "v_rcp_f32 v108, v108\n"
    "s_waitcnt lgkmcnt(3)\n"
    "v_mfma_f32_32x32x16_bf16 v[88:103], v[120:123], a[248:251], v[88:103]\n"
    "ds_read_b128 v[140:143], v14 offset:3072\n"
    "v_mul_f32 v105, v105, v81\n"
    "v_rcp_f32 v109, v109\n"
    "v_mul_f32 v106, v106, v82\n"
    "v_rcp_f32 v110, v110\n"
    "v_mfma_f32_32x32x16_bf16 v[88:103], v[124:127], a[252:255], v[88:103]\n"
    "s_waitcnt vmcnt(6)\n"
    "s_barrier\n"
    "ds_read_b128 v[144:147], v14 offset:4096\n"
    "s_add_u32 s88, s20, 0x8000\n"
    "s_add_u32 m0, s9, 0x18000\n"
    "s_nop 0\n"
    "buffer_load_dwordx4 v16, s[48:51], s88 offen lds\n"
    "v_mfma_f32_32x32x16_bf16 v[88:103], v[188:191], v[204:207], v[88:103]\n"
    "v_mul_f32 v107, v107, v83\n"
    "v_rcp_f32 v111, v111\n"
    "v_mul_f32 v108, v108, v84\n"
    "v_mul_f32 v109, v109, v85\n"
    "v_mul_f32 v110, v110, v86\n"
    "v_mul_f32 v111, v111, v87\n"
    "s_waitcnt lgkmcnt(3)\n"
    "v_mfma_f32_32x32x16_bf16 v[40:55], v[128:131], a[128:131], 0\n"
    "ds_read_b128 v[120:123], v14 offset:5120\n"
    "buffer_load_dwordx4 v17, s[48:51], s88 offen offset:1024 lds\n"
    "v_cvt_pk_bf16_f32 v187, v110, v111\n"
    "v_cvt_pk_bf16_f32 v184, v104, v105\n"
    "v_cvt_pk_bf16_f32 v185, v106, v107\n"
    "v_cvt_pk_bf16_f32 v186, v108, v109\n"
    "v_mfma_f32_32x32x16_bf16 v[40:55], v[132:135], a[132:135], v[40:55]\n"
    "ds_read_b128 v[124:127], v14 offset:6144\n"
    "buffer_store_dwordx4 v[184:187], v2, s[52:55], s16 offen sc1\n"
    "s_add_u32 s16, s16, 0x4000\n"
    "v_mul_f32 v104, v11, v88\n"
    "v_mul_f32 v105, v11, v89\n"
    "v_mul_f32 v106, v11, v90\n"
    "s_waitcnt lgkmcnt(3)\n"
    "v_mfma_f32_32x32x16_bf16 v[40:55], v[136:139], a[136:139], v[40:55]\n"
    "ds_read_b128 v[128:131], v14 offset:7168\n"
    "v_mul_f32 v107, v11, v91\n"
    "v_exp_f32 v104, v104\n"
    "v_mul_f32 v108, v11, v92\n"
    "v_exp_f32 v105, v105\n"
    "v_mfma_f32_32x32x16_bf16 v[40:55], v[140:143], a[140:143], v[40:55]\n"
    "ds_read_b128 v[132:135], v14 offset:8192\n"
    "v_mul_f32 v109, v11, v93\n"
    "v_exp_f32 v106, v106\n"
    "v_mul_f32 v110, v11, v94\n"
    "v_exp_f32 v107, v107\n"
    "s_waitcnt lgkmcnt(3)\n"
    "v_mfma_f32_32x32x16_bf16 v[40:55], v[144:147], a[144:147], v[40:55]\n"
    "ds_read_b128 v[136:139], v14 offset:9216\n"
    "buffer_load_dwordx4 v18, s[48:51], s88 offen offset:2048 lds\n"
    "v_mul_f32 v111, v11, v95\n"
    "v_fma_f32 v104, v104, v12, v12\n"
    "v_exp_f32 v108, v108\n"
    "v_mfma_f32_32x32x16_bf16 v[40:55], v[120:123], a[148:151], v[40:55]\n"
    "ds_read_b128 v[140:143], v14 offset:10240\n"
    "v_fma_f32 v105, v105, v12, v12\n"
    "v_exp_f32 v109, v109\n"
    "v_fma_f32 v106, v106, v12, v12\n"
    "v_exp_f32 v110, v110\n"
    "s_waitcnt lgkmcnt(3)\n"
    "v_mfma_f32_32x32x16_bf16 v[40:55], v[124:127], a[152:155], v[40:55]\n"
    "ds_read_b128 v[144:147], v14 offset:11264\n"
    "buffer_load_dwordx4 v19, s[48:51], s88 offen offset:3072 lds\n"
    "s_add_u32 s88, s20, 0xc000\n"
    "v_fma_f32 v107, v107, v12, v12\n"
    "v_exp_f32 v111, v111\n"
    "v_mfma_f32_32x32x16_bf16 v[40:55], v[128:131], a[156:159], v[40:55]\n"
    "ds_read_b128 v[120:123], v14 offset:12288\n"
    "v_rcp_f32 v104, v104\n"
    "v_fma_f32 v108, v108, v12, v12\n"
    "v_rcp_f32 v105, v105\n"
    "v_fma_f32 v109, v109, v12, v12\n"
    "s_waitcnt lgkmcnt(3)\n"
    "v_mfma_f32_32x32x16_bf16 v[40:55], v[132:135], a[160:163], v[40:55]\n"
    "ds_read_b128 v[124:127], v14 offset:13312\n"
    "s_add_u32 m0, s9, 0x1c000\n"
    "s_nop 0\n"
    "buffer_load_dwordx4 v16, s[48:51], s88 offen lds\n"
    "v_rcp_f32 v106, v106\n"
    "v_mfma_f32_32x32x16_bf16 v[40:55], v[136:139], a[164:167], v[40:55]\n"
    "ds_read_b128 v[128:131], v14 offset:14336\n"
    "v_fma_f32 v110, v110, v12, v12\n"
    "v_rcp_f32 v107, v107\n"
    "v_fma_f32 v111, v111, v12, v12\n"
    "v_mul_f32 v104, v104, v96\n"
    "s_waitcnt lgkmcnt(3)\n"
    "v_mfma_f32_32x32x16_bf16 v[40:55], v[140:143], a[168:171], v[40:55]\n"
    "ds_read_b128 v[132:135], v14 offset:15360\n"
    "buffer_load_dwordx4 v17, s[48:51], s88 offen offset:1024 lds\n"
    "v_rcp_f32 v108, v108\n"
    "v_mul_f32 v105, v105, v97\n"
    "v_mfma_f32_32x32x16_bf16 v[40:55], v[144:147], a[172:175], v[40:55]\n"
    "ds_read_b128 v[136:139], v14 offset:16384\n"
    "v_rcp_f32 v109, v109\n"
    "v_mul_f32 v106, v106, v98\n"
    "v_rcp_f32 v110, v110\n"
    "v_mul_f32 v107, v107, v99\n"
    "s_waitcnt lgkmcnt(3)\n"
    "v_mfma_f32_32x32x16_bf16 v[40:55], v[120:123], a[176:179], v[40:55]\n"
    "ds_read_b128 v[140:143], v14 offset:17408\n"
    "buffer_load_dwordx4 v18, s[48:51], s88 offen offset:2048 lds\n"
    "v_rcp_f32 v111, v111\n"
    "v_mul_f32 v108, v108, v100\n"
    "v_mul_f32 v109, v109, v101\n"
    "v_mfma_f32_32x32x16_bf16 v[40:55], v[124:127], a[180:183], v[40:55]\n"
    "ds_read_b128 v[144:147], v14 offset:18432\n"
    "v_mul_f32 v110, v110, v102\n"
    "v_mul_f32 v111, v111, v103\n"
    "v_cvt_pk_bf16_f32 v191, v110, v111\n"
    "v_cvt_pk_bf16_f32 v188, v104, v105\n"
    "v_cvt_pk_bf16_f32 v189, v106, v107\n"
    "v_cvt_pk_bf16_f32 v190, v108, v109\n"
    "s_waitcnt lgkmcnt(3)\n"
    "v_mfma_f32_32x32x16_bf16 v[40:55], v[128:131], a[184:187], v[40:55]\n"
    "ds_read_b128 v[120:123], v14 offset:19456\n"
    "buffer_load_dwordx4 v19, s[48:51], s88 offen offset:3072 lds\n"
    "s_add_u32 s86, s21, 0x8000\n"
    "buffer_store_dwordx4 v[188:191], v2, s[52:55], s16 offen sc1\n"
    "s_add_u32 s16, s16, 0x4000\n"
    "v_mfma_f32_32x32x16_bf16 v[40:55], v[132:135], a[188:191], v[40:55]\n"
    "ds_read_b128 v[124:127], v14 offset:20480\n"
    "s_waitcnt lgkmcnt(3)\n"
    "v_mfma_f32_32x32x16_bf16 v[40:55], v[136:139], a[192:195], v[40:55]\n"
    "ds_read_b128 v[128:131], v14 offset:21504\n"
    "v_mfma_f32_32x32x16_bf16 v[40:55], v[140:143], a[196:199], v[40:55]\n"
    "ds_read_b128 v[132:135], v14 offset:22528\n"
    "s_waitcnt lgkmcnt(3)\n"
    "v_mfma_f32_32x32x16_bf16 v[40:55], v[144:147], a[200:203], v[40:55]\n"
    "ds_read_b128 v[136:139], v14 offset:23552\n"
    "buffer_load_dwordx4 a[0:3], v21, s[52:55], s86 offen sc1\n"
    "v_mfma_f32_32x32x16_bf16 v[40:55], v[120:123], a[204:207], v[40:55]\n"
    "ds_read_b128 v[140:143], v14 offset:24576\n"
    "s_waitcnt lgkmcnt(3)\n"
    "v_mfma_f32_32x32x16_bf16 v[40:55], v[124:127], a[208:211], v[40:55]\n"
    "ds_read_b128 v[144:147], v14 offset:25600\n"
    "buffer_load_dwordx4 a[4:7], v22, s[52:55], s86 offen sc1\n"
    "v_mfma_f32_32x32x16_bf16 v[40:55], v[128:131], a[212:215], v[40:55]\n"
    "ds_read_b128 v[120:123], v14 offset:26624\n"
    "s_waitcnt lgkmcnt(3)\n"
    "v_mfma_f32_32x32x16_bf16 v[40:55], v[132:135], a[216:219], v[40:55]\n"
    "ds_read_b128 v[124:127], v14 offset:27648\n"
    "buffer_load_dwordx4 a[8:11], v23, s[52:55], s86 offen sc1\n"
    "s_add_u32 s86, s22, 0x8000\n"
    "v_mfma_f32_32x32x16_bf16 v[40:55], v[136:139], a[220:223], v[40:55]\n"
    "ds_read_b128 v[128:131], v14 offset:28672\n"
    "s_waitcnt lgkmcnt(3)\n"
    "v_mfma_f32_32x32x16_bf16 v[40:55], v[140:143], a[224:227], v[40:55]\n"
    "ds_read_b128 v[132:135], v14 offset:29696\n"
    "buffer_load_dwordx4 a[12:15], v20, s[52:55], s86 offen sc1\n"
    "v_mfma_f32_32x32x16_bf16 v[40:55], v[144:147], a[228:231], v[40:55]\n"
    "ds_read_b128 v[136:139], v14 offset:30720\n"
    "s_waitcnt lgkmcnt(3)\n"
    "v_mfma_f32_32x32x16_bf16 v[40:55], v[120:123], a[232:235], v[40:55]\n"
    "ds_read_b128 v[140:143], v14 offset:31744\n"
    "buffer_load_dwordx4 a[16:19], v21, s[52:55], s86 offen sc1\n"
    "v_mfma_f32_32x32x16_bf16 v[40:55], v[124:127], a[236:239], v[40:55]\n"
    "ds_read_b128 v[144:147], v2\n"
    "s_waitcnt lgkmcnt(3)\n"
    "v_mfma_f32_32x32x16_bf16 v[40:55], v[128:131], a[240:243], v[40:55]\n"
    "ds_read_b128 v[120:123], v2 offset:1024\n"
    "buffer_load_dwordx4 a[20:23], v22, s[52:55], s86 offen sc1\n"
    "v_mfma_f32_32x32x16_bf16 v[40:55], v[132:135], a[244:247], v[40:55]\n"
    "ds_read_b128 v[124:127], v2 offset:2048\n"
    "s_waitcnt lgkmcnt(3)\n"
    "v_mfma_f32_32x32x16_bf16 v[40:55], v[136:139], a[248:251], v[40:55]\n"
    "ds_read_b128 v[128:131], v2 offset:3072\n"
    "buffer_load_dwordx4 a[24:27], v23, s[52:55], s86 offen sc1\n"
    "s_add_u32 s86, s21, 0xc000\n"
    "v_mfma_f32_32x32x16_bf16 v[40:55], v[140:143], a[252:255], v[40:55]\n"
    "s_waitcnt vmcnt(12)\n"
    "s_barrier\n"
    "ds_read_b128 v[132:135], v2 offset:4096\n"
    "s_add_u32 s88, s20, 0x10000\n"
    "v_mfma_f32_32x32x16_bf16 v[40:55], v[192:195], v[204:207], v[40:55]\n"
    "s_add_u32 m0, s9, 0x20000\n"
    "s_nop 0\n"
    "buffer_load_dwordx4 v16, s[48:51], s88 offen lds\n"
    "s_waitcnt lgkmcnt(3)\n"
    "v_mfma_f32_32x32x16_bf16 v[56:71], v[144:147], a[128:131], 0\n"
    "ds_read_b128 v[136:139], v2 offset:5120\n"
    "v_mfma_f32_32x32x16_bf16 v[56:71], v[120:123], a[132:135], v[56:71]\n"
    "ds_read_b128 v[140:143], v2 offset:6144\n"
    "buffer_load_dwordx4 v17, s[48:51], s88 offen offset:1024 lds\n"
    "s_nop 2\n"
    "v_mul_f32 v104, v11, v40\n"
    "v_mul_f32 v105, v11, v41\n"
    "v_mul_f32 v106, v11, v42\n"
    "s_waitcnt lgkmcnt(3)\n"
    "v_mfma_f32_32x32x16_bf16 v[56:71], v[124:127], a[136:139], v[56:71]\n"
    "ds_read_b128 v[144:147], v2 offset:7168\n"
    "v_mul_f32 v107, v11, v43\n"
    "v_exp_f32 v104, v104\n"
    "v_mul_f32 v108, v11, v44\n"
    "v_exp_f32 v105, v105\n"
    "v_mfma_f32_32x32x16_bf16 v[56:71], v[128:131], a[140:143], v[56:71]\n"
    "ds_read_b128 v[120:123], v2 offset:8192\n"
    "buffer_load_dwordx4 v18, s[48:51], s88 offen offset:2048 lds\n"
    "v_mul_f32 v109, v11, v45\n"
    "v_exp_f32 v106, v106\n"
    "v_mul_f32 v110, v11, v46\n"
    "s_waitcnt lgkmcnt(3)\n"
    "v_mfma_f32_32x32x16_bf16 v[56:71], v[132:135], a[144:147], v[56:71]\n"
    "ds_read_b128 v[124:127], v2 offset:9216\n"
    "v_exp_f32 v107, v107\n"
    "v_mul_f32 v111, v11, v47\n"
    "v_fma_f32 v104, v104, v12, v12\n"
    "v_exp_f32 v108, v108\n"
    "v_mfma_f32_32x32x16_bf16 v[56:71], v[136:139], a[148:151], v[56:71]\n"
    "ds_read_b128 v[128:131], v2 offset:10240\n"
    "buffer_load_dwordx4 v19, s[48:51], s88 offen offset:3072 lds\n"
    "s_add_u32 s88, s20, 0x14000\n"
    "v_fma_f32 v105, v105, v12, v12\n"
    "v_exp_f32 v109, v109\n"
    "s_waitcnt lgkmcnt(3)\n"
    "v_mfma_f32_32x32x16_bf16 v[56:71], v[140:143], a[152:155], v[56:71]\n"
    "ds_read_b128 v[132:135], v2 offset:11264\n"
    "v_fma_f32 v106, v106, v12, v12\n"
    "v_exp_f32 v110, v110\n"
    "v_fma_f32 v107, v107, v12, v12\n"
    "v_exp_f32 v111, v111\n"
    "v_mfma_f32_32x32x16_bf16 v[56:71], v[144:147], a[156:159], v[56:71]\n"
    "ds_read_b128 v[136:139], v2 offset:12288\n"
    "s_add_u32 m0, s9, 0x24000\n"
    "s_nop 0\n"
    "buffer_load_dwordx4 v16, s[48:51], s88 offen lds\n"
    "v_rcp_f32 v104, v104\n"
    "s_waitcnt lgkmcnt(3)\n"
    "v_mfma_f32_32x32x16_bf16 v[56:71], v[120:123], a[160:163], v[56:71]\n"
    "ds_read_b128 v[140:143], v2 offset:13312\n"
    "v_fma_f32 v108, v108, v12, v12\n"
    "v_rcp_f32 v105, v105\n"
    "v_fma_f32 v109, v109, v12, v12\n"
    "v_rcp_f32 v106, v106\n"
    "v_mfma_f32_32x32x16_bf16 v[56:71], v[124:127], a[164:167], v[56:71]\n"
    "ds_read_b128 v[144:147], v2 offset:14336\n"
    "buffer_load_dwordx4 v17, s[48:51], s88 offen offset:1024 lds\n"
    "v_fma_f32 v110, v110, v12, v12\n"
    "v_rcp_f32 v107, v107\n"
    "v_fma_f32 v111, v111, v12, v12\n"
    "s_waitcnt lgkmcnt(3)\n"
    "v_mfma_f32_32x32x16_bf16 v[56:71], v[128:131], a[168:171], v[56:71]\n"
    "ds_read_b128 v[120:123], v2 offset:15360\n"
    "v_mul_f32 v104, v104, v48\n"
    "v_rcp_f32 v108, v108\n"
    "v_mul_f32 v105, v105, v49\n"
    "v_rcp_f32 v109, v109\n"
    "v_mfma_f32_32x32x16_bf16 v[56:71], v[132:135], a[172:175], v[56:71]\n"
    "ds_read_b128 v[124:127], v2 offset:16384\n"
    "buffer_load_dwordx4 v18, s[48:51], s88 offen offset:2048 lds\n"
    "v_mul_f32 v106, v106, v50\n"
    "v_rcp_f32 v110, v110\n"
    "v_mul_f32 v107, v107, v51\n"
    "s_waitcnt lgkmcnt(3)\n"
    "v_mfma_f32_32x32x16_bf16 v[56:71], v[136:139], a[176:179], v[56:71]\n"
    "ds_read_b128 v[128:131], v2 offset:17408\n"
    "v_rcp_f32 v111, v111\n"
    "v_mul_f32 v108, v108, v52\n"
    "v_mul_f32 v109, v109, v53\n"
    "v_mul_f32 v110, v110, v54\n"
    "v_mul_f32 v111, v111, v55\n"
    "v_mfma_f32_32x32x16_bf16 v[56:71], v[140:143], a[180:183], v[56:71]\n"
    "ds_read_b128 v[132:135], v2 offset:18432\n"
    "buffer_load_dwordx4 v19, s[48:51], s88 offen offset:3072 lds\n"
    "v_cvt_pk_bf16_f32 v195, v110, v111\n"
    "v_cvt_pk_bf16_f32 v192, v104, v105\n"
    "v_cvt_pk_bf16_f32 v193, v106, v107\n"
    "v_cvt_pk_bf16_f32 v194, v108, v109\n"
    "s_waitcnt lgkmcnt(3)\n"
    "v_mfma_f32_32x32x16_bf16 v[56:71], v[144:147], a[184:187], v[56:71]\n"
    "ds_read_b128 v[136:139], v2 offset:19456\n"
    "buffer_store_dwordx4 v[192:195], v2, s[52:55], s16 offen sc1\n"
    "s_add_u32 s16, s16, 0x4000\n"
    "v_mfma_f32_32x32x16_bf16 v[56:71], v[120:123], a[188:191], v[56:71]\n"
    "ds_read_b128 v[140:143], v2 offset:20480\n"
    "s_waitcnt lgkmcnt(3)\n"
    "v_mfma_f32_32x32x16_bf16 v[56:71], v[124:127], a[192:195], v[56:71]\n"
    "ds_read_b128 v[144:147], v2 offset:21504\n"
    "v_mfma_f32_32x32x16_bf16 v[56:71], v[128:131], a[196:199], v[56:71]\n"
    "ds_read_b128 v[120:123], v2 offset:22528\n"
    "buffer_load_dwordx4 a[28:31], v21, s[52:55], s86 offen sc1\n"
    "s_waitcnt lgkmcnt(3)\n"
    "v_mfma_f32_32x32x16_bf16 v[56:71], v[132:135], a[200:203], v[56:71]\n"
    "ds_read_b128 v[124:127], v2 offset:23552\n"
    "v_mfma_f32_32x32x16_bf16 v[56:71], v[136:139], a[204:207], v[56:71]\n"
    "ds_read_b128 v[128:131], v2 offset:24576\n"
    "buffer_load_dwordx4 a[32:35], v22, s[52:55], s86 offen sc1\n"
    "s_waitcnt lgkmcnt(3)\n"
    "v_mfma_f32_32x32x16_bf16 v[56:71], v[140:143], a[208:211], v[56:71]\n"
    "ds_read_b128 v[132:135], v2 offset:25600\n"
    "s_waitcnt vmcnt(17)\n"
    "v_accvgpr_read_b32 v148, a3\n"
    "s_waitcnt vmcnt(16)\n"
    "v_accvgpr_read_b32 v149, a7\n"
    "s_waitcnt vmcnt(15)\n"
    "v_accvgpr_read_b32 v150, a11\n"
    "v_mfma_f32_32x32x16_bf16 v[56:71], v[144:147], a[212:215], v[56:71]\n"
    "ds_read_b128 v[136:139], v2 offset:26624\n"
    "v_cmp_eq_u32 vcc, s66, v148\n"
    "v_cmp_eq_u32 s[78:79], s66, v149\n"
    "v_cmp_eq_u32 s[80:81], s66, v150\n"
    "s_or_b64 s[78:79], s[78:79], s[80:81]\n"
    "s_or_b64 vcc, vcc, s[78:79]\n"
    "s_cbranch_vccnz Lslow70_%=\n"
    "Lback71_%=:\n"
    "s_waitcnt lgkmcnt(3)\n"
    "v_mfma_f32_32x32x16_bf16 v[56:71], v[120:123], a[216:219], v[56:71]\n"
    "ds_read_b128 v[140:143], v2 offset:27648\n"
    "buffer_load_dwordx4 a[36:39], v23, s[52:55], s86 offen sc1\n"
    "s_add_u32 s86, s22, 0xc000\n"
    "v_mfma_f32_32x32x16_bf16 v[56:71], v[124:127], a[220:223], v[56:71]\n"
    "ds_read_b128 v[144:147], v2 offset:28672\n"
    "s_waitcnt vmcnt(15)\n"
    "v_accvgpr_read_b32 v148, a15\n"
    "s_waitcnt vmcnt(14)\n"
    "v_accvgpr_read_b32 v149, a19\n"
    "s_waitcnt vmcnt(13)\n"
    "v_accvgpr_read_b32 v150, a23\n"
    "s_waitcnt lgkmcnt(3)\n"
    "v_mfma_f32_32x32x16_bf16 v[56:71], v[128:131], a[224:227], v[56:71]\n"
    "ds_read_b128 v[120:123], v2 offset:29696\n"
    "s_waitcnt vmcnt(12)\n"
    "v_accvgpr_read_b32 v151, a27\n"
    "buffer_load_dwordx4 a[40:43], v20, s[52:55], s86 offen sc1\n"
    "v_mfma_f32_32x32x16_bf16 v[56:71], v[132:135], a[228:231], v[56:71]\n"
    "ds_read_b128 v[124:127], v2 offset:30720\n"
    "v_cmp_eq_u32 vcc, s66, v148\n"
    "v_cmp_eq_u32 s[78:79], s66, v149\n"
    "v_cmp_eq_u32 s[80:81], s66, v150\n"
    "v_cmp_eq_u32 s[82:83], s66, v151\n"
    "s_or_b64 s[80:81], s[80:81], s[82:83]\n"
    "s_or_b64 s[78:79], s[78:79], s[80:81]\n"
    "s_or_b64 vcc, vcc, s[78:79]\n"
    "s_cbranch_vccnz Lslow73_%=\n"
    "Lback74_%=:\n"
    "s_waitcnt lgkmcnt(3)\n"
    "v_mfma_f32_32x32x16_bf16 v[56:71], v[136:139], a[232:235], v[56:71]\n"
    "ds_read_b128 v[128:131], v2 offset:31744\n"
    "buffer_load_dwordx4 a[44:47], v21, s[52:55], s86 offen sc1\n"
    "v_mfma_f32_32x32x16_bf16 v[56:71], v[140:143], a[236:239], v[56:71]\n"
    "ds_read_b128 v[132:135], v2 offset:32768\n"
    "s_waitcnt lgkmcnt(3)\n"
    "v_mfma_f32_32x32x16_bf16 v[56:71], v[144:147], a[240:243], v[56:71]\n"
    "ds_read_b128 v[136:139], v2 offset:33792\n"
    "buffer_load_dwordx4 a[48:51], v22, s[52:55], s86 offen sc1\n"
    "v_mfma_f32_32x32x16_bf16 v[56:71], v[120:123], a[244:247], v[56:71]\n"
    "ds_read_b128 v[140:143], v2 offset:34816\n"
    "s_waitcnt lgkmcnt(3)\n"
    "v_mfma_f32_32x32x16_bf16 v[56:71], v[124:127], a[248:251], v[56:71]\n"
    "ds_read_b128 v[144:147], v2 offset:35840\n"
    "buffer_load_dwordx4 a[52:55], v23, s[52:55], s86 offen sc1\n"
    "s_add_u32 s86, s21, 0x10000\n"
    "v_mfma_f32_32x32x16_bf16 v[56:71], v[128:131], a[252:255], v[56:71]\n"
    "s_waitcnt vmcnt(12)\n"
    "s_barrier\n"
    "ds_read_b128 v[120:123], v2 offset:36864\n"
    "v_mfma_f32_32x32x16_bf16 v[56:71], v[196:199], v[204:207], v[56:71]\n"
    "s_add_u32 m0, s9, 0\n"
    "s_nop 0\n"
    "buffer_load_dwordx4 v157, s[48:51], s92 offen lds\n"
    "s_waitcnt lgkmcnt(3)\n"
    "v_mfma_f32_32x32x16_bf16 v[72:87], v[132:135], a[128:131], 0\n"
    "ds_read_b128 v[124:127], v2 offset:37888\n"
    "v_mfma_f32_32x32x16_bf16 v[72:87], v[136:139], a[132:135], v[72:87]\n"
    "ds_read_b128 v[128:131], v2 offset:38912\n"
    "buffer_load_dwordx4 v157, s[48:51], s93 offen offset:1024 lds\n"
    "s_nop 2\n"
    "v_mul_f32 v104, v11, v56\n"
    "v_mul_f32 v105, v11, v57\n"
    "v_mul_f32 v106, v11, v58\n"
    "s_waitcnt lgkmcnt(3)\n"
    "v_mfma_f32_32x32x16_bf16 v[72:87], v[140:143], a[136:139], v[72:87]\n"
    "ds_read_b128 v[132:135], v2 offset:39936\n"
    "v_mul_f32 v107, v11, v59\n"
    "v_exp_f32 v104, v104\n"
    "v_mul_f32 v108, v11, v60\n"
    "v_exp_f32 v105, v105\n"
    "v_mfma_f32_32x32x16_bf16 v[72:87], v[144:147], a[140:143], v[72:87]\n"
    "ds_read_b128 v[136:139], v2 offset:40960\n"
    "buffer_load_dwordx4 v157, s[48:51], s94 offen offset:2048 lds\n"
    "v_mul_f32 v109, v11, v61\n"
    "v_exp_f32 v106, v106\n"
    "v_mul_f32 v110, v11, v62\n"
    "s_waitcnt lgkmcnt(3)\n"
    "v_mfma_f32_32x32x16_bf16 v[72:87], v[120:123], a[144:147], v[72:87]\n"
    "ds_read_b128 v[140:143], v2 offset:41984\n"
    "v_exp_f32 v107, v107\n"
    "v_mul_f32 v111, v11, v63\n"
    "v_fma_f32 v104, v104, v12, v12\n"
    "v_exp_f32 v108, v108\n"
    "v_mfma_f32_32x32x16_bf16 v[72:87], v[124:127], a[148:151], v[72:87]\n"
    "ds_read_b128 v[144:147], v2 offset:43008\n"
    "buffer_load_dwordx4 v157, s[48:51], s95 offen offset:3072 lds\n"
    "v_fma_f32 v105, v105, v12, v12\n"
    "v_exp_f32 v109, v109\n"
    "v_fma_f32 v106, v106, v12, v12\n"
    "s_waitcnt lgkmcnt(3)\n"
    "v_mfma_f32_32x32x16_bf16 v[72:87], v[128:131], a[152:155], v[72:87]\n"
    "ds_read_b128 v[120:123], v2 offset:44032\n"
    "v_exp_f32 v110, v110\n"
    "v_fma_f32 v107, v107, v12, v12\n"
    "v_exp_f32 v111, v111\n"
    "v_mfma_f32_32x32x16_bf16 v[72:87], v[132:135], a[156:159], v[72:87]\n"
    "ds_read_b128 v[124:127], v2 offset:45056\n"
    "buffer_load_dwordx4 a[56:59], v21, s[52:55], s86 offen sc1\n"
    "v_rcp_f32 v104, v104\n"
    "v_fma_f32 v108, v108, v12, v12\n"
    "s_waitcnt lgkmcnt(3)\n"
    "v_mfma_f32_32x32x16_bf16 v[72:87], v[136:139], a[160:163], v[72:87]\n"
    "ds_read_b128 v[128:131], v2 offset:46080\n"
    "v_rcp_f32 v105, v105\n"
    "v_fma_f32 v109, v109, v12, v12\n"
    "v_rcp_f32 v106, v106\n"
    "v_fma_f32 v110, v110, v12, v12\n"
    "v_mfma_f32_32x32x16_bf16 v[72:87], v[140:143], a[164:167], v[72:87]\n"
    "ds_read_b128 v[132:135], v2 offset:47104\n"
    "s_waitcnt vmcnt(11)\n"
    "v_accvgpr_read_b32 v148, a31\n"
    "s_waitcnt vmcnt(10)\n"
    "v_accvgpr_read_b32 v149, a35\n"
    "s_waitcnt vmcnt(9)\n"
    "v_accvgpr_read_b32 v150, a39\n"
    "s_waitcnt lgkmcnt(3)\n"
    "v_mfma_f32_32x32x16_bf16 v[72:87], v[144:147], a[168:171], v[72:87]\n"
    "ds_read_b128 v[136:139], v2 offset:48128\n"
    "v_cmp_eq_u32 vcc, s66, v148\n"
    "v_cmp_eq_u32 s[78:79], s66, v149\n"
    "v_cmp_eq_u32 s[80:81], s66, v150\n"
    "s_or_b64 s[78:79], s[78:79], s[80:81]\n"
    "s_or_b64 vcc, vcc, s[78:79]\n"
    "s_cbranch_vccnz Lslow76_%=\n"
    "Lback77_%=:\n"
    "v_mfma_f32_32x32x16_bf16 v[72:87], v[120:123], a[172:175], v[72:87]\n"
    "ds_read_b128 v[140:143], v2 offset:49152\n"
    "buffer_load_dwordx4 a[60:63], v22, s[52:55], s86 offen sc1\n"
    "v_rcp_f32 v107, v107\n"
    "v_fma_f32 v111, v111, v12, v12\n"
    "v_mul_f32 v104, v104, v64\n"
    "s_waitcnt lgkmcnt(3)\n"
    "v_mfma_f32_32x32x16_bf16 v[72:87], v[124:127], a[176:179], v[72:87]\n"
    "ds_read_b128 v[144:147], v2 offset:50176\n"
    "v_rcp_f32 v108, v108\n"
    "v_mul_f32 v105, v105, v65\n"
    "v_rcp_f32 v109, v109\n"
    "v_mul_f32 v106, v106, v66\n"
    "v_mfma_f32_32x32x16_bf16 v[72:87], v[128:131], a[180:183], v[72:87]\n"
    "ds_read_b128 v[120:123], v2 offset:51200\n"
    "buffer_load_dwordx4 a[64:67], v23, s[52:55], s86 offen sc1\n"
    "s_add_u32 s86, s22, 0x10000\n"
    "v_rcp_f32 v110, v110\n"
    "v_mul_f32 v107, v107, v67\n"
    "s_waitcnt lgkmcnt(3)\n"
    "v_mfma_f32_32x32x16_bf16 v[72:87], v[132:135], a[184:187], v[72:87]\n"
    "ds_read_b128 v[124:127], v2 offset:52224\n"
    "v_rcp_f32 v111, v111\n"
    "v_mul_f32 v108, v108, v68\n"
    "v_mul_f32 v109, v109, v69\n"
    "v_mul_f32 v110, v110, v70\n"
    "v_mul_f32 v111, v111, v71\n"
    "v_mfma_f32_32x32x16_bf16 v[72:87], v[136:139], a[188:191], v[72:87]\n"
    "ds_read_b128 v[128:131], v2 offset:53248\n"
    "buffer_load_dwordx4 a[68:71], v20, s[52:55], s86 offen sc1\n"
    "v_cvt_pk_bf16_f32 v199, v110, v111\n"
    "v_cvt_pk_bf16_f32 v196, v104, v105\n"
    "v_cvt_pk_bf16_f32 v197, v106, v107\n"
    "v_cvt_pk_bf16_f32 v198, v108, v109\n"
    "s_waitcnt lgkmcnt(3)\n"
    "v_mfma_f32_32x32x16_bf16 v[72:87], v[140:143], a[192:195], v[72:87]\n"
    "ds_read_b128 v[132:135], v2 offset:54272\n"
    "buffer_store_dwordx4 v[196:199], v2, s[52:55], s16 offen sc1\n"
    "s_add_u32 s16, s16, 0x4000\n"
    "v_mfma_f32_32x32x16_bf16 v[72:87], v[144:147], a[196:199], v[72:87]\n"
    "ds_read_b128 v[136:139], v2 offset:55296\n"
    "s_waitcnt vmcnt(12)\n"
    "v_accvgpr_read_b32 v148, a43\n"
    "s_waitcnt vmcnt(11)\n"
    "v_accvgpr_read_b32 v149, a47\n"
    "s_waitcnt vmcnt(10)\n"
    "v_accvgpr_read_b32 v150, a51\n"
    "s_waitcnt lgkmcnt(3)\n"
    "v_mfma_f32_32x32x16_bf16 v[72:87], v[120:123], a[200:203], v[72:87]\n"
    "ds_read_b128 v[140:143], v2 offset:56320\n"
    "s_waitcnt vmcnt(9)\n"
    "v_accvgpr_read_b32 v151, a55\n"
    "v_mfma_f32_32x32x16_bf16 v[72:87], v[124:127], a[204:207], v[72:87]\n"
    "ds_read_b128 v[144:147], v2 offset:57344\n"
    "v_cmp_eq_u32 vcc, s66, v148\n"
    "v_cmp_eq_u32 s[78:79], s66, v149\n"
    "v_cmp_eq_u32 s[80:81], s66, v150\n"
    "v_cmp_eq_u32 s[82:83], s66, v151\n"
    "s_or_b64 s[80:81], s[80:81], s[82:83]\n"
    "s_or_b64 s[78:79], s[78:79], s[80:81]\n"
    "s_or_b64 vcc, vcc, s[78:79]\n"
    "s_cbranch_vccnz Lslow79_%=\n"
    "Lback80_%=:\n"
    "s_waitcnt lgkmcnt(3)\n"
    "v_mfma_f32_32x32x16_bf16 v[72:87], v[128:131], a[208:211], v[72:87]\n"
    "ds_read_b128 v[120:123], v2 offset:58368\n"
    "buffer_load_dwordx4 a[72:75], v21, s[52:55], s86 offen sc1\n"
    "v_mfma_f32_32x32x16_bf16 v[72:87], v[132:135], a[212:215], v[72:87]\n"
    "ds_read_b128 v[124:127], v2 offset:59392\n"
    "s_or_b32 s84, s60, s61\n"
    "s_cmp_eq_u32 s84, 0\n"
    "s_cselect_b32 s24, -1, 0\n"
    "s_mov_b32 s84, 0x480000\n"
    "v_lshlrev_b32 v8, 2, v5\n"
    "s_and_b32 exec_lo, s24, 0xffff\n"
    "s_mov_b32 exec_hi, 0\n"
    "buffer_store_dword v4, v8, s[52:55], s84 offen sc1\n"
    "s_mov_b64 exec, -1\n"
    "s_waitcnt lgkmcnt(3)\n"
    "v_mfma_f32_32x32x16_bf16 v[72:87], v[136:139], a[216:219], v[72:87]\n"
    "ds_read_b128 v[128:131], v2 offset:60416\n"
    "buffer_load_dwordx4 a[76:79], v22, s[52:55], s86 offen sc1\n"
    "v_mfma_f32_32x32x16_bf16 v[72:87], v[140:143], a[220:223], v[72:87]\n"
    "ds_read_b128 v[132:135], v2 offset:61440\n"
    "s_waitcnt lgkmcnt(3)\n"
    "v_mfma_f32_32x32x16_bf16 v[72:87], v[144:147], a[224:227], v[72:87]\n"
    "ds_read_b128 v[136:139], v2 offset:62464\n"
    "buffer_load_dwordx4 a[80:83], v23, s[52:55], s86 offen sc1\n"
    "s_add_u32 s86, s21, 0x14000\n"
    "v_mfma_f32_32x32x16_bf16 v[72:87], v[120:123], a[228:231], v[72:87]\n"
    "ds_read_b128 v[140:143], v2 offset:63488\n"
    "s_waitcnt lgkmcnt(3)\n"
    "v_mfma_f32_32x32x16_bf16 v[72:87], v[124:127], a[232:235], v[72:87]\n"
    "ds_read_b128 v[144:147], v2 offset:64512\n"
    "buffer_load_dwordx4 a[84:87], v21, s[52:55], s86 offen sc1\n"
    "v_mfma_f32_32x32x16_bf16 v[72:87], v[128:131], a[236:239], v[72:87]\n"
    "ds_read_b64_tr_b16 v[120:121], v154\n"
    "ds_read_b64_tr_b16 v[122:123], v154 offset:1024\n"
    "s_waitcnt lgkmcnt(4)\n"
    "v_mfma_f32_32x32x16_bf16 v[72:87], v[132:135], a[240:243], v[72:87]\n"
    "ds_read_b64_tr_b16 v[124:125], v154 offset:16384\n"
    "ds_read_b64_tr_b16 v[126:127], v154 offset:17408\n"
    "buffer_load_dwordx4 a[88:91], v22, s[52:55], s86 offen sc1\n"
    "v_mfma_f32_32x32x16_bf16 v[72:87], v[136:139], a[244:247], v[72:87]\n"
    "ds_read_b64_tr_b16 v[128:129], v154 offset:32768\n"
    "ds_read_b64_tr_b16 v[130:131], v154 offset:33792\n"
    "s_waitcnt lgkmcnt(6)\n"
    "v_mfma_f32_32x32x16_bf16 v[72:87], v[140:143], a[248:251], v[72:87]\n"
    "ds_read_b64_tr_b16 v[132:133], v154 offset:49152\n"
    "ds_read_b64_tr_b16 v[134:135], v154 offset:50176\n"
    "buffer_load_dwordx4 a[92:95], v23, s[52:55], s86 offen sc1\n"
    "s_add_u32 s86, s22, 0x14000\n"
    "v_mfma_f32_32x32x16_bf16 v[72:87], v[144:147], a[252:255], v[72:87]\n"
    "s_waitcnt vmcnt(11)\n"
    "s_barrier\n"
    "ds_read_b64_tr_b16 v[136:137], v154 offset:2048\n"
    "ds_read_b64_tr_b16 v[138:139], v154 offset:3072\n"
    "v_mfma_f32_32x32x16_bf16 v[72:87], v[200:203], v[204:207], v[72:87]\n"
    "buffer_load_dwordx4 a[96:99], v20, s[52:55], s86 offen sc1\n"
    "s_waitcnt lgkmcnt(6)\n"
    "v_mfma_f32_32x32x16_bf16 v[40:55], v[120:123], v[176:179], 0\n"
    "ds_read_b64_tr_b16 v[140:141], v154 offset:4096\n"
    "ds_read_b64_tr_b16 v[142:143], v154 offset:5120\n"
    "s_waitcnt vmcnt(11)\n"
    "v_accvgpr_read_b32 v148, a59\n"
    "s_waitcnt vmcnt(10)\n"
    "v_accvgpr_read_b32 v149, a63\n"
    "s_waitcnt vmcnt(9)\n"
    "v_accvgpr_read_b32 v150, a67\n"
    "v_mfma_f32_32x32x16_bf16 v[40:55], v[124:127], v[180:183], v[40:55]\n"
    "ds_read_b64_tr_b16 v[144:145], v154 offset:6144\n"
    "ds_read_b64_tr_b16 v[146:147], v154 offset:7168\n"
    "v_cmp_eq_u32 vcc, s66, v148\n"
    "v_cmp_eq_u32 s[78:79], s66, v149\n"
    "v_cmp_eq_u32 s[80:81], s66, v150\n"
    "s_or_b64 s[78:79], s[78:79], s[80:81]\n"
    "s_or_b64 vcc, vcc, s[78:79]\n"
    "s_cbranch_vccnz Lslow82_%=\n"
    "Lback83_%=:\n"
    "buffer_load_dwordx4 a[100:103], v21, s[52:55], s86 offen sc1\n"
    "s_waitcnt lgkmcnt(6)\n"
    "v_mfma_f32_32x32x16_bf16 v[40:55], v[128:131], v[184:187], v[40:55]\n"
    "ds_read_b64_tr_b16 v[120:121], v154 offset:8192\n"
    "ds_read_b64_tr_b16 v[122:123], v154 offset:9216\n"
    "buffer_load_dwordx4 a[104:107], v22, s[52:55], s86 offen sc1\n"
    "v_mul_f32 v104, v11, v72\n"
    "v_mul_f32 v105, v11, v73\n"
    "v_mul_f32 v106, v11, v74\n"
    "v_mul_f32 v107, v11, v75\n"
    "v_exp_f32 v104, v104\n"
    "v_mfma_f32_32x32x16_bf16 v[40:55], v[132:135], v[188:191], v[40:55]\n"
    "ds_read_b64_tr_b16 v[124:125], v154 offset:10240\n"
    "ds_read_b64_tr_b16 v[126:127], v154 offset:11264\n"
    "v_mul_f32 v108, v11, v76\n"
    "v_exp_f32 v105, v105\n"
    "v_mul_f32 v109, v11, v77\n"
    "v_exp_f32 v106, v106\n"
    "v_mul_f32 v110, v11, v78\n"
    "s_waitcnt lgkmcnt(6)\n"
    "v_mfma_f32_32x32x16_bf16 v[40:55], v[136:139], a[0:3], v[40:55]\n"
    "ds_read_b64_tr_b16 v[128:129], v154 offset:12288\n"
    "ds_read_b64_tr_b16 v[130:131], v154 offset:13312\n"
    "buffer_load_dwordx4 a[108:111], v23, s[52:55], s86 offen sc1\n"
    "s_add_u32 s86, s21, 0x18000\n"
    "v_exp_f32 v107, v107\n"
    "v_mul_f32 v111, v11, v79\n"
    "v_fma_f32 v104, v104, v12, v12\n"
    "v_mfma_f32_32x32x16_bf16 v[40:55], v[140:143], a[4:7], v[40:55]\n"
    "ds_read_b64_tr_b16 v[132:133], v154 offset:14336\n"
    "ds_read_b64_tr_b16 v[134:135], v154 offset:15360\n"
    "buffer_load_dwordx4 v[210:213], v21, s[52:55], s86 offen sc1\n"
    "v_exp_f32 v108, v108\n"
    "v_fma_f32 v105, v105, v12, v12\n"
    "v_exp_f32 v109, v109\n"
    "v_fma_f32 v106, v106, v12, v12\n"
    "s_waitcnt lgkmcnt(6)\n"
    "v_mfma_f32_32x32x16_bf16 v[40:55], v[144:147], a[8:11], v[40:55]\n"
    "ds_read_b64_tr_b16 v[136:137], v154 offset:18432\n"
    "ds_read_b64_tr_b16 v[138:139], v154 offset:19456\n"
    "v_exp_f32 v110, v110\n"
    "v_fma_f32 v107, v107, v12, v12\n"
    "v_exp_f32 v111, v111\n"
    "v_rcp_f32 v104, v104\n"
    "v_fma_f32 v108, v108, v12, v12\n"
    "v_mfma_f32_32x32x16_bf16 v[40:55], v[120:123], a[12:15], v[40:55]\n"
    "ds_read_b64_tr_b16 v[140:141], v154 offset:20480\n"
    "ds_read_b64_tr_b16 v[142:143], v154 offset:21504\n"
    "buffer_load_dwordx4 v[214:217], v22, s[52:55], s86 offen sc1\n"
    "v_rcp_f32 v105, v105\n"
    "v_fma_f32 v109, v109, v12, v12\n"
    "v_rcp_f32 v106, v106\n"
    "v_fma_f32 v110, v110, v12, v12\n"
    "s_waitcnt lgkmcnt(6)\n"
    "v_mfma_f32_32x32x16_bf16 v[40:55], v[124:127], a[16:19], v[40:55]\n"
    "ds_read_b64_tr_b16 v[144:145], v154 offset:22528\n"
    "ds_read_b64_tr_b16 v[146:147], v154 offset:23552\n"
    "v_rcp_f32 v107, v107\n"
    "v_fma_f32 v111, v111, v12, v12\n"
    "v_mul_f32 v104, v104, v80\n"
    "v_rcp_f32 v108, v108\n"
    "v_mul_f32 v105, v105, v81\n"
    "v_mfma_f32_32x32x16_bf16 v[40:55], v[128:131], a[20:23], v[40:55]\n"
    "ds_read_b64_tr_b16 v[120:121], v154 offset:24576\n"
    "ds_read_b64_tr_b16 v[122:123], v154 offset:25600\n"
    "buffer_load_dwordx4 v[218:221], v23, s[52:55], s86 offen sc1\n"
    "s_add_u32 s86, s22, 0x18000\n"
    "v_rcp_f32 v109, v109\n"
    "v_mul_f32 v106, v106, v82\n"
    "v_rcp_f32 v110, v110\n"
    "s_waitcnt lgkmcnt(6)\n"
    "v_mfma_f32_32x32x16_bf16 v[40:55], v[132:135], a[24:27], v[40:55]\n"
    "ds_read_b64_tr_b16 v[124:125], v154 offset:26624\n"
    "ds_read_b64_tr_b16 v[126:127], v154 offset:27648\n"
    "buffer_load_dwordx4 v[222:225], v20, s[52:55], s86 offen sc1\n"
    "v_mul_f32 v107, v107, v83\n"
    "v_rcp_f32 v111, v111\n"
    "v_mul_f32 v108, v108, v84\n"
    "v_mul_f32 v109, v109, v85\n"
    "v_mul_f32 v110, v110, v86\n"
    "v_mfma_f32_32x32x16_bf16 v[40:55], v[136:139], a[28:31], v[40:55]\n"
    "ds_read_b64_tr_b16 v[128:129], v154 offset:28672\n"
    "ds_read_b64_tr_b16 v[130:131], v154 offset:29696\n"
    "s_waitcnt vmcnt(15)\n"
    "v_accvgpr_read_b32 v148, a71\n"
    "s_waitcnt vmcnt(13)\n"
    "v_accvgpr_read_b32 v149, a75\n"
    "s_waitcnt vmcnt(12)\n"
    "v_accvgpr_read_b32 v150, a79\n"
    "s_waitcnt vmcnt(11)\n"
    "v_accvgpr_read_b32 v151, a83\n"
    "s_waitcnt lgkmcnt(6)\n"
    "v_mfma_f32_32x32x16_bf16 v[40:55], v[140:143], a[32:35], v[40:55]\n"
    "ds_read_b64_tr_b16 v[132:133], v154 offset:30720\n"
    "ds_read_b64_tr_b16 v[134:135], v154 offset:31744\n"
    "v_cmp_eq_u32 vcc, s66, v148\n"
    "v_cmp_eq_u32 s[78:79], s66, v149\n"
    "v_cmp_eq_u32 s[80:81], s66, v150\n"
    "v_cmp_eq_u32 s[82:83], s66, v151\n"
    "s_or_b64 s[80:81], s[80:81], s[82:83]\n"
    "s_or_b64 s[78:79], s[78:79], s[80:81]\n"
    "s_or_b64 vcc, vcc, s[78:79]\n"
    "s_cbranch_vccnz Lslow85_%=\n"
    "Lback86_%=:\n"
    "v_mfma_f32_32x32x16_bf16 v[40:55], v[144:147], a[36:39], v[40:55]\n"
    "ds_read_b64_tr_b16 v[136:137], v154 offset:34816\n"
    "ds_read_b64_tr_b16 v[138:139], v154 offset:35840\n"
    "buffer_load_dwordx4 v[226:229], v21, s[52:55], s86 offen sc1\n"
    "v_mul_f32 v111, v111, v87\n"
    "v_cvt_pk_bf16_f32 v203, v110, v111\n"
    "v_cvt_pk_bf16_f32 v200, v104, v105\n"
    "v_cvt_pk_bf16_f32 v201, v106, v107\n"
    "v_cvt_pk_bf16_f32 v202, v108, v109\n"
    "s_waitcnt lgkmcnt(6)\n"
    "v_mfma_f32_32x32x16_bf16 v[40:55], v[120:123], a[40:43], v[40:55]\n"
    "ds_read_b64_tr_b16 v[140:141], v154 offset:36864\n"
    "ds_read_b64_tr_b16 v[142:143], v154 offset:37888\n"
    "buffer_load_dwordx4 v[230:233], v22, s[52:55], s86 offen sc1\n"
    "buffer_store_dwordx4 v[200:203], v2, s[52:55], s16 offen sc1\n"
    "s_add_u32 s16, s16, 0x4000\n"
    "v_bfe_u32 v11, v1, 1, 1\n"
    "v_and_b32 v12, 60, v1\n"
    "v_lshl_or_b32 v12, v11, 6, v12\n"
    "v_mfma_f32_32x32x16_bf16 v[40:55], v[124:127], a[44:47], v[40:55]\n"
    "ds_read_b64_tr_b16 v[144:145], v154 offset:38912\n"
    "ds_read_b64_tr_b16 v[146:147], v154 offset:39936\n"
    "v_mov_b32 v107, s66\n"
    "v_mov_b32 v111, s66\n"
    "v_mov_b32 v115, s66\n"
    "v_and_b32 v11, 1, v1\n"
    "s_waitcnt lgkmcnt(6)\n"
    "v_mfma_f32_32x32x16_bf16 v[40:55], v[128:131], a[48:51], v[40:55]\n"
    "ds_read_b64_tr_b16 v[120:121], v154 offset:40960\n"
    "ds_read_b64_tr_b16 v[122:123], v154 offset:41984\n"
    "v_lshl_or_b32 v12, v11, 7, v12\n"
    "v_lshrrev_b32 v11, 2, v1\n"
    "v_lshlrev_b32 v11, 10, v11\n"
    "v_and_or_b32 v11, v2, 48, v11\n"
    "v_mfma_f32_32x32x16_bf16 v[40:55], v[132:135], a[52:55], v[40:55]\n"
    "ds_read_b64_tr_b16 v[124:125], v154 offset:43008\n"
    "ds_read_b64_tr_b16 v[126:127], v154 offset:44032\n"
    "s_waitcnt vmcnt(13)\n"
    "v_accvgpr_read_b32 v148, a87\n"
    "s_waitcnt vmcnt(12)\n"
    "v_accvgpr_read_b32 v149, a91\n"
    "s_waitcnt lgkmcnt(6)\n"
    "v_mfma_f32_32x32x16_bf16 v[40:55], v[136:139], a[56:59], v[40:55]\n"
    "ds_read_b64_tr_b16 v[128:129], v154 offset:45056\n"
    "ds_read_b64_tr_b16 v[130:131], v154 offset:46080\n"
    "s_waitcnt vmcnt(11)\n"
    "v_accvgpr_read_b32 v150, a95\n"
    "buffer_load_dwordx4 v[234:237], v23, s[52:55], s86 offen sc1\n"
    "v_mfma_f32_32x32x16_bf16 v[40:55], v[140:143], a[60:63], v[40:55]\n"
    "ds_read_b64_tr_b16 v[132:133], v154 offset:47104\n"
    "ds_read_b64_tr_b16 v[134:135], v154 offset:48128\n"
    "v_cmp_eq_u32 vcc, s66, v148\n"
    "v_cmp_eq_u32 s[78:79], s66, v149\n"
    "v_cmp_eq_u32 s[80:81], s66, v150\n"
    "s_or_b64 s[78:79], s[78:79], s[80:81]\n"
    "s_or_b64 vcc, vcc, s[78:79]\n"
    "s_cbranch_vccnz Lslow88_%=\n"
    "Lback89_%=:\n"
    "s_waitcnt lgkmcnt(6)\n"
    "v_mfma_f32_32x32x16_bf16 v[40:55], v[144:147], a[64:67], v[40:55]\n"
    "ds_read_b64_tr_b16 v[136:137], v154 offset:51200\n"
    "ds_read_b64_tr_b16 v[138:139], v154 offset:52224\n"
    "s_add_u32 s86, s21, 0x1c000\n"
    "buffer_load_dwordx4 a[128:131], v21, s[52:55], s86 offen sc1\n"
    "v_mfma_f32_32x32x16_bf16 v[40:55], v[120:123], a[68:71], v[40:55]\n"
    "ds_read_b64_tr_b16 v[140:141], v154 offset:53248\n"
    "ds_read_b64_tr_b16 v[142:143], v154 offset:54272\n"
    "buffer_load_dwordx4 a[132:135], v22, s[52:55], s86 offen sc1\n"
    "s_waitcnt lgkmcnt(6)\n"
    "v_mfma_f32_32x32x16_bf16 v[40:55], v[124:127], a[72:75], v[40:55]\n"
    "ds_read_b64_tr_b16 v[144:145], v154 offset:55296\n"
    "ds_read_b64_tr_b16 v[146:147], v154 offset:56320\n"
    "v_mfma_f32_32x32x16_bf16 v[40:55], v[128:131], a[76:79], v[40:55]\n"
    "ds_read_b64_tr_b16 v[120:121], v154 offset:57344\n"
    "ds_read_b64_tr_b16 v[122:123], v154 offset:58368\n"
    "buffer_load_dwordx4 a[136:139], v23, s[52:55], s86 offen sc1\n"
    "s_add_u32 s86, s22, 0x1c000\n"
    "s_waitcnt lgkmcnt(6)\n"
    "v_mfma_f32_32x32x16_bf16 v[40:55], v[132:135], a[80:83], v[40:55]\n"
    "ds_read_b64_tr_b16 v[124:125], v154 offset:59392\n"
    "ds_read_b64_tr_b16 v[126:127], v154 offset:60416\n"
    "s_waitcnt vmcnt(14)\n"
    "v_accvgpr_read_b32 v148, a99\n"
    "s_waitcnt vmcnt(13)\n"
    "v_accvgpr_read_b32 v149, a103\n"
    "v_mfma_f32_32x32x16_bf16 v[40:55], v[136:139], a[84:87], v[40:55]\n"
    "ds_read_b64_tr_b16 v[128:129], v154 offset:61440\n"
    "ds_read_b64_tr_b16 v[130:131], v154 offset:62464\n"
    "s_waitcnt vmcnt(12)\n"
    "v_accvgpr_read_b32 v150, a107\n"
    "s_waitcnt vmcnt(11)\n"
    "v_accvgpr_read_b32 v151, a111\n"
    "s_waitcnt lgkmcnt(6)\n"
    "v_mfma_f32_32x32x16_bf16 v[40:55], v[140:143], a[88:91], v[40:55]\n"
    "ds_read_b64_tr_b16 v[132:133], v154 offset:63488\n"
    "ds_read_b64_tr_b16 v[134:135], v154 offset:64512\n"
    "v_cmp_eq_u32 vcc, s66, v148\n"
    "v_cmp_eq_u32 s[78:79], s66, v149\n"
    "v_cmp_eq_u32 s[80:81], s66, v150\n"
    "v_cmp_eq_u32 s[82:83], s66, v151\n"
    "s_or_b64 s[80:81], s[80:81], s[82:83]\n"
    "s_or_b64 s[78:79], s[78:79], s[80:81]\n"
    "s_or_b64 vcc, vcc, s[78:79]\n"
    "s_cbranch_vccnz Lslow91_%=\n"
    "Lback92_%=:\n"
    "v_mfma_f32_32x32x16_bf16 v[40:55], v[144:147], a[92:95], v[40:55]\n"
    "ds_read_b64_tr_b16 v[136:137], v152\n"
    "ds_read_b64_tr_b16 v[138:139], v152 offset:1024\n"
    "buffer_load_dwordx4 a[140:143], v20, s[52:55], s86 offen sc1\n"
    "s_waitcnt lgkmcnt(6)\n"
    "v_mfma_f32_32x32x16_bf16 v[40:55], v[120:123], a[96:99], v[40:55]\n"
    "ds_read_b64_tr_b16 v[140:141], v152 offset:16384\n"
    "ds_read_b64_tr_b16 v[142:143], v152 offset:17408\n"
    "buffer_load_dwordx4 a[144:147], v21, s[52:55], s86 offen sc1\n"
    "v_mfma_f32_32x32x16_bf16 v[40:55], v[124:127], a[100:103], v[40:55]\n"
    "ds_read_b64_tr_b16 v[144:145], v152 offset:32768\n"
    "ds_read_b64_tr_b16 v[146:147], v152 offset:33792\n"
    "s_waitcnt vmcnt(12)\n"
    "v_cmp_eq_u32 vcc, s66, v213\n"
    "s_waitcnt vmcnt(11)\n"
    "v_cmp_eq_u32 s[78:79], s66, v217\n"
    "s_waitcnt vmcnt(10)\n"
    "v_cmp_eq_u32 s[80:81], s66, v221\n"
    "s_or_b64 s[78:79], s[78:79], s[80:81]\n"
    "s_or_b64 vcc, vcc, s[78:79]\n"
    "s_cbranch_vccnz Lslow94_%=\n"
    "Lback95_%=:\n"
    "s_waitcnt lgkmcnt(6)\n"
    "v_mfma_f32_32x32x16_bf16 v[40:55], v[128:131], a[104:107], v[40:55]\n"
    "ds_read_b64_tr_b16 v[120:121], v152 offset:49152\n"
    "ds_read_b64_tr_b16 v[122:123], v152 offset:50176\n"
    "buffer_load_dwordx4 a[148:151], v22, s[52:55], s86 offen sc1\n"
    "v_mfma_f32_32x32x16_bf16 v[40:55], v[132:135], a[108:111], v[40:55]\n"
    "ds_read_b64_tr_b16 v[124:125], v155\n"
    "ds_read_b64_tr_b16 v[126:127], v155 offset:1024\n"
    "buffer_load_dwordx4 a[152:155], v23, s[52:55], s86 offen sc1\n"
    "s_waitcnt lgkmcnt(6)\n"
    "v_mfma_f32_32x32x16_bf16 v[24:39], v[136:139], v[176:179], v[24:39]\n"
    "ds_read_b64_tr_b16 v[128:129], v155 offset:2048\n"
    "ds_read_b64_tr_b16 v[130:131], v155 offset:3072\n"
    "v_mfma_f32_32x32x16_bf16 v[24:39], v[140:143], v[180:183], v[24:39]\n"
    "ds_read_b64_tr_b16 v[132:133], v155 offset:4096\n"
    "ds_read_b64_tr_b16 v[134:135], v155 offset:5120\n"
    "s_waitcnt lgkmcnt(6)\n"
    "v_mfma_f32_32x32x16_bf16 v[24:39], v[144:147], v[184:187], v[24:39]\n"
    "ds_read_b64_tr_b16 v[136:137], v155 offset:6144\n"
    "ds_read_b64_tr_b16 v[138:139], v155 offset:7168\n"
    "v_mfma_f32_32x32x16_bf16 v[24:39], v[120:123], v[188:191], v[24:39]\n"
    "ds_read_b64_tr_b16 v[140:141], v155 offset:8192\n"
    "ds_read_b64_tr_b16 v[142:143], v155 offset:9216\n"
    "s_waitcnt lgkmcnt(6)\n"
    "v_mfma_f32_32x32x16_bf16 v[40:55], v[124:127], v[192:195], v[40:55]\n"
    "ds_read_b64_tr_b16 v[144:145], v155 offset:10240\n"
    "ds_read_b64_tr_b16 v[146:147], v155 offset:11264\n"
    "v_mfma_f32_32x32x16_bf16 v[40:55], v[128:131], v[210:213], v[40:55]\n"
    "ds_read_b64_tr_b16 v[120:121], v155 offset:12288\n"
    "ds_read_b64_tr_b16 v[122:123], v155 offset:13312\n"
    "s_waitcnt lgkmcnt(6)\n"
    "v_mfma_f32_32x32x16_bf16 v[40:55], v[132:135], v[214:217], v[40:55]\n"
    "ds_read_b64_tr_b16 v[124:125], v155 offset:14336\n"
    "ds_read_b64_tr_b16 v[126:127], v155 offset:15360\n"
    "s_waitcnt vmcnt(11)\n"
    "v_cmp_eq_u32 vcc, s66, v225\n"
    "s_waitcnt vmcnt(10)\n"
    "v_cmp_eq_u32 s[78:79], s66, v229\n"
    "s_waitcnt vmcnt(9)\n"
    "v_cmp_eq_u32 s[80:81], s66, v233\n"
    "s_waitcnt vmcnt(7)\n"
    "v_cmp_eq_u32 s[82:83], s66, v237\n"
    "s_or_b64 s[80:81], s[80:81], s[82:83]\n"
    "s_or_b64 s[78:79], s[78:79], s[80:81]\n"
    "s_or_b64 vcc, vcc, s[78:79]\n"
    "s_cbranch_vccnz Lslow97_%=\n"
    "Lback98_%=:\n"
    "v_mfma_f32_32x32x16_bf16 v[40:55], v[136:139], v[218:221], v[40:55]\n"
    "ds_read_b64_tr_b16 v[128:129], v155 offset:16384\n"
    "ds_read_b64_tr_b16 v[130:131], v155 offset:17408\n"
    "s_waitcnt lgkmcnt(6)\n"
    "v_mfma_f32_32x32x16_bf16 v[40:55], v[140:143], v[222:225], v[40:55]\n"
    "ds_read_b64_tr_b16 v[132:133], v155 offset:18432\n"
    "ds_read_b64_tr_b16 v[134:135], v155 offset:19456\n"
    "v_mfma_f32_32x32x16_bf16 v[40:55], v[144:147], v[226:229], v[40:55]\n"
    "ds_read_b64_tr_b16 v[136:137], v155 offset:20480\n"
    "ds_read_b64_tr_b16 v[138:139], v155 offset:21504\n"
    "s_waitcnt lgkmcnt(6)\n"
    "v_mfma_f32_32x32x16_bf16 v[40:55], v[120:123], v[230:233], v[40:55]\n"
    "ds_read_b64_tr_b16 v[140:141], v155 offset:22528\n"
    "ds_read_b64_tr_b16 v[142:143], v155 offset:23552\n"
    "v_mfma_f32_32x32x16_bf16 v[40:55], v[124:127], v[234:237], v[40:55]\n"
    "ds_read_b64_tr_b16 v[144:145], v155 offset:24576\n"
    "ds_read_b64_tr_b16 v[146:147], v155 offset:25600\n"
    "s_waitcnt vmcnt(6)\n"
    "v_accvgpr_read_b32 v148, a131\n"
    "s_waitcnt vmcnt(5)\n"
    "v_accvgpr_read_b32 v149, a135\n"
    "s_waitcnt lgkmcnt(6)\n"
    "v_mfma_f32_32x32x16_bf16 v[40:55], v[128:131], v[196:199], v[40:55]\n"
    "ds_read_b64_tr_b16 v[120:121], v155 offset:26624\n"
    "ds_read_b64_tr_b16 v[122:123], v155 offset:27648\n"
    "s_waitcnt vmcnt(4)\n"
    "v_accvgpr_read_b32 v150, a139\n"
    "v_cmp_eq_u32 vcc, s66, v148\n"
    "v_cmp_eq_u32 s[78:79], s66, v149\n"
    "v_cmp_eq_u32 s[80:81], s66, v150\n"
    "s_or_b64 s[78:79], s[78:79], s[80:81]\n"
    "s_or_b64 vcc, vcc, s[78:79]\n"
    "s_cbranch_vccnz Lslow100_%=\n"
    "Lback101_%=:\n"
    "v_mfma_f32_32x32x16_bf16 v[40:55], v[132:135], a[128:131], v[40:55]\n"
    "ds_read_b64_tr_b16 v[124:125], v155 offset:28672\n"
    "ds_read_b64_tr_b16 v[126:127], v155 offset:29696\n"
    "s_waitcnt lgkmcnt(6)\n"
    "v_mfma_f32_32x32x16_bf16 v[40:55], v[136:139], a[132:135], v[40:55]\n"
    "ds_read_b64_tr_b16 v[128:129], v155 offset:30720\n"
    "ds_read_b64_tr_b16 v[130:131], v155 offset:31744\n"
    "v_mfma_f32_32x32x16_bf16 v[40:55], v[140:143], a[136:139], v[40:55]\n"
    "ds_read_b64_tr_b16 v[132:133], v152 offset:2048\n"
    "ds_read_b64_tr_b16 v[134:135], v152 offset:3072\n"
    "s_waitcnt vmcnt(3)\n"
    "v_accvgpr_read_b32 v148, a143\n"
    "s_waitcnt vmcnt(2)\n"
    "v_accvgpr_read_b32 v149, a147\n"
    "s_waitcnt vmcnt(1)\n"
    "v_accvgpr_read_b32 v150, a151\n"
    "s_waitcnt vmcnt(0)\n"
    "v_accvgpr_read_b32 v151, a155\n"
    "v_cmp_eq_u32 vcc, s66, v148\n"
    "v_cmp_eq_u32 s[78:79], s66, v149\n"
    "v_cmp_eq_u32 s[80:81], s66, v150\n"
    "v_cmp_eq_u32 s[82:83], s66, v151\n"
    "s_or_b64 s[80:81], s[80:81], s[82:83]\n"
    "s_or_b64 s[78:79], s[78:79], s[80:81]\n"
    "s_or_b64 vcc, vcc, s[78:79]\n"
    "s_cbranch_vccnz Lslow103_%=\n"
    "Lback104_%=:\n"
    "s_waitcnt lgkmcnt(6)\n"
    "v_mfma_f32_32x32x16_bf16 v[40:55], v[144:147], a[140:143], v[40:55]\n"
    "ds_read_b64_tr_b16 v[136:137], v152 offset:4096\n"
    "ds_read_b64_tr_b16 v[138:139], v152 offset:5120\n"
    "s_add_u32 s86, s21, 0x20000\n"
    "buffer_load_dwordx4 a[192:195], v21, s[52:55], s86 offen sc1\n"
    "v_mfma_f32_32x32x16_bf16 v[40:55], v[120:123], a[144:147], v[40:55]\n"
    "ds_read_b64_tr_b16 v[140:141], v152 offset:6144\n"
    "ds_read_b64_tr_b16 v[142:143], v152 offset:7168\n"
    "s_waitcnt lgkmcnt(6)\n"
    "v_mfma_f32_32x32x16_bf16 v[40:55], v[124:127], a[148:151], v[40:55]\n"
    "ds_read_b64_tr_b16 v[144:145], v152 offset:8192\n"
    "ds_read_b64_tr_b16 v[146:147], v152 offset:9216\n"
    "buffer_load_dwordx4 a[196:199], v22, s[52:55], s86 offen sc1\n"
    "v_mfma_f32_32x32x16_bf16 v[40:55], v[128:131], a[152:155], v[40:55]\n"
    "ds_read_b64_tr_b16 v[120:121], v152 offset:10240\n"
    "ds_read_b64_tr_b16 v[122:123], v152 offset:11264\n"
    "s_waitcnt lgkmcnt(6)\n"
    "v_mfma_f32_32x32x16_bf16 v[24:39], v[132:135], a[0:3], v[24:39]\n"
    "ds_read_b64_tr_b16 v[124:125], v152 offset:12288\n"
    "ds_read_b64_tr_b16 v[126:127], v152 offset:13312\n"
    "buffer_load_dwordx4 a[200:203], v23, s[52:55], s86 offen sc1\n"
    "s_add_u32 s86, s22, 0x20000\n"
    "v_mfma_f32_32x32x16_bf16 v[24:39], v[136:139], a[4:7], v[24:39]\n"
    "ds_read_b64_tr_b16 v[128:129], v152 offset:14336\n"
    "ds_read_b64_tr_b16 v[130:131], v152 offset:15360\n"
    "s_nop 0\n"
    "v_cvt_pk_bf16_f32 v104, v40, v41\n"
    "v_cvt_pk_bf16_f32 v105, v42, v43\n"
    "v_cvt_pk_bf16_f32 v106, v44, v45\n"
    "s_waitcnt lgkmcnt(6)\n"
    "v_mfma_f32_32x32x16_bf16 v[24:39], v[140:143], a[8:11], v[24:39]\n"
    "ds_read_b64_tr_b16 v[132:133], v152 offset:18432\n"
    "ds_read_b64_tr_b16 v[134:135], v152 offset:19456\n"
    "buffer_store_dwordx4 v[104:107], v2, s[52:55], s11 offen sc1\n"
    "v_cvt_pk_bf16_f32 v108, v46, v47\n"
    "v_cvt_pk_bf16_f32 v109, v48, v49\n"
    "v_mfma_f32_32x32x16_bf16 v[24:39], v[144:147], a[12:15], v[24:39]\n"
    "ds_read_b64_tr_b16 v[136:137], v152 offset:20480\n"
    "ds_read_b64_tr_b16 v[138:139], v152 offset:21504\n"
    "v_cvt_pk_bf16_f32 v110, v50, v51\n"
    "buffer_store_dwordx4 v[108:111], v2, s[52:55], s12 offen sc1\n"
    "v_cvt_pk_bf16_f32 v112, v52, v53\n"
    "s_waitcnt lgkmcnt(6)\n"
    "v_mfma_f32_32x32x16_bf16 v[24:39], v[120:123], a[16:19], v[24:39]\n"
    "ds_read_b64_tr_b16 v[140:141], v152 offset:22528\n"
    "ds_read_b64_tr_b16 v[142:143], v152 offset:23552\n"
    "v_cvt_pk_bf16_f32 v113, v54, v55\n"
    "buffer_store_dwordx4 v[112:115], v2, s[52:55], s15 offen sc1\n"
    "v_mfma_f32_32x32x16_bf16 v[24:39], v[124:127], a[20:23], v[24:39]\n"
    "ds_read_b64_tr_b16 v[144:145], v152 offset:24576\n"
    "ds_read_b64_tr_b16 v[146:147], v152 offset:25600\n"
    "s_waitcnt lgkmcnt(6)\n"
    "v_mfma_f32_32x32x16_bf16 v[24:39], v[128:131], a[24:27], v[24:39]\n"
    "ds_read_b64_tr_b16 v[120:121], v152 offset:26624\n"
    "ds_read_b64_tr_b16 v[122:123], v152 offset:27648\n"
    "v_mfma_f32_32x32x16_bf16 v[24:39], v[132:135], a[28:31], v[24:39]\n"
    "ds_read_b64_tr_b16 v[124:125], v152 offset:28672\n"
    "ds_read_b64_tr_b16 v[126:127], v152 offset:29696\n"
    "s_waitcnt lgkmcnt(6)\n"
    "v_mfma_f32_32x32x16_bf16 v[24:39], v[136:139], a[32:35], v[24:39]\n"
    "ds_read_b64_tr_b16 v[128:129], v152 offset:30720\n"
    "ds_read_b64_tr_b16 v[130:131], v152 offset:31744\n"
    "v_mfma_f32_32x32x16_bf16 v[24:39], v[140:143], a[36:39], v[24:39]\n"
    "ds_read_b64_tr_b16 v[132:133], v152 offset:34816\n"
    "ds_read_b64_tr_b16 v[134:135], v152 offset:35840\n"
    "buffer_load_dwordx4 a[204:207], v20, s[52:55], s86 offen sc1\n"
    "s_waitcnt lgkmcnt(6)\n"
    "v_mfma_f32_32x32x16_bf16 v[24:39], v[144:147], a[40:43], v[24:39]\n"
    "ds_read_b64_tr_b16 v[136:137], v152 offset:36864\n"
    "ds_read_b64_tr_b16 v[138:139], v152 offset:37888\n"
    "v_mfma_f32_32x32x16_bf16 v[24:39], v[120:123], a[44:47], v[24:39]\n"
    "ds_read_b64_tr_b16 v[140:141], v152 offset:38912\n"
    "ds_read_b64_tr_b16 v[142:143], v152 offset:39936\n"
    "buffer_load_dwordx4 a[208:211], v21, s[52:55], s86 offen sc1\n"
    "s_waitcnt lgkmcnt(6)\n"
    "v_mfma_f32_32x32x16_bf16 v[24:39], v[124:127], a[48:51], v[24:39]\n"
    "ds_read_b64_tr_b16 v[144:145], v152 offset:40960\n"
    "ds_read_b64_tr_b16 v[146:147], v152 offset:41984\n"
    "v_mfma_f32_32x32x16_bf16 v[24:39], v[128:131], a[52:55], v[24:39]\n"
    "ds_read_b64_tr_b16 v[120:121], v152 offset:43008\n"
    "ds_read_b64_tr_b16 v[122:123], v152 offset:44032\n"
    "buffer_load_dwordx4 a[212:215], v22, s[52:55], s86 offen sc1\n"
    "s_waitcnt lgkmcnt(6)\n"
    "v_mfma_f32_32x32x16_bf16 v[24:39], v[132:135], a[56:59], v[24:39]\n"
    "ds_read_b64_tr_b16 v[124:125], v152 offset:45056\n"
    "ds_read_b64_tr_b16 v[126:127], v152 offset:46080\n"
    "v_mfma_f32_32x32x16_bf16 v[24:39], v[136:139], a[60:63], v[24:39]\n"
    "ds_read_b64_tr_b16 v[128:129], v152 offset:47104\n"
    "ds_read_b64_tr_b16 v[130:131], v152 offset:48128\n"
    "buffer_load_dwordx4 a[216:219], v23, s[52:55], s86 offen sc1\n"
    "s_add_u32 s86, s23, 0x20000\n"
    "s_waitcnt lgkmcnt(6)\n"
    "v_mfma_f32_32x32x16_bf16 v[24:39], v[140:143], a[64:67], v[24:39]\n"
    "ds_read_b64_tr_b16 v[132:133], v152 offset:51200\n"
    "ds_read_b64_tr_b16 v[134:135], v152 offset:52224\n"
    "v_mfma_f32_32x32x16_bf16 v[24:39], v[144:147], a[68:71], v[24:39]\n"
    "ds_read_b64_tr_b16 v[136:137], v152 offset:53248\n"
    "ds_read_b64_tr_b16 v[138:139], v152 offset:54272\n"
    "s_waitcnt lgkmcnt(6)\n"
    "v_mfma_f32_32x32x16_bf16 v[24:39], v[120:123], a[72:75], v[24:39]\n"
    "ds_read_b64_tr_b16 v[140:141], v152 offset:55296\n"
    "ds_read_b64_tr_b16 v[142:143], v152 offset:56320\n"
    "buffer_load_dwordx4 a[220:223], v20, s[52:55], s86 offen sc1\n"
    "v_mfma_f32_32x32x16_bf16 v[24:39], v[124:127], a[76:79], v[24:39]\n"
    "ds_read_b64_tr_b16 v[144:145], v152 offset:57344\n"
    "ds_read_b64_tr_b16 v[146:147], v152 offset:58368\n"
    "s_waitcnt lgkmcnt(6)\n"
    "v_mfma_f32_32x32x16_bf16 v[24:39], v[128:131], a[80:83], v[24:39]\n"
    "ds_read_b64_tr_b16 v[120:121], v152 offset:59392\n"
    "ds_read_b64_tr_b16 v[122:123], v152 offset:60416\n"
    "s_waitcnt vmcnt(10)\n"
    "v_accvgpr_read_b32 v148, a195\n"
    "s_waitcnt vmcnt(9)\n"
    "v_accvgpr_read_b32 v149, a199\n"
    "v_mfma_f32_32x32x16_bf16 v[24:39], v[132:135], a[84:87], v[24:39]\n"
    "ds_read_b64_tr_b16 v[124:125], v152 offset:61440\n"
    "ds_read_b64_tr_b16 v[126:127], v152 offset:62464\n"
    "s_waitcnt vmcnt(8)\n"
    "v_accvgpr_read_b32 v150, a203\n"
    "buffer_load_dwordx4 a[224:227], v21, s[52:55], s86 offen sc1\n"
    "s_waitcnt lgkmcnt(6)\n"
    "v_mfma_f32_32x32x16_bf16 v[24:39], v[136:139], a[88:91], v[24:39]\n"
    "ds_read_b64_tr_b16 v[128:129], v152 offset:63488\n"
    "ds_read_b64_tr_b16 v[130:131], v152 offset:64512\n"
    "v_cmp_eq_u32 vcc, s66, v148\n"
    "v_cmp_eq_u32 s[78:79], s66, v149\n"
    "v_cmp_eq_u32 s[80:81], s66, v150\n"
    "s_or_b64 s[78:79], s[78:79], s[80:81]\n"
    "s_or_b64 vcc, vcc, s[78:79]\n"
    "s_cbranch_vccnz Lslow106_%=\n"
    "Lback107_%=:\n"
    "v_mfma_f32_32x32x16_bf16 v[24:39], v[140:143], a[92:95], v[24:39]\n"
    "ds_read_b64_tr_b16 v[132:133], v153\n"
    "ds_read_b64_tr_b16 v[134:135], v153 offset:1024\n"
    "buffer_load_dwordx4 a[228:231], v22, s[52:55], s86 offen sc1\n"
    "s_waitcnt lgkmcnt(6)\n"
    "v_mfma_f32_32x32x16_bf16 v[24:39], v[144:147], a[96:99], v[24:39]\n"
    "ds_read_b64_tr_b16 v[136:137], v153 offset:2048\n"
    "ds_read_b64_tr_b16 v[138:139], v153 offset:3072\n"
    "v_mfma_f32_32x32x16_bf16 v[24:39], v[120:123], a[100:103], v[24:39]\n"
    "ds_read_b64_tr_b16 v[140:141], v153 offset:4096\n"
    "ds_read_b64_tr_b16 v[142:143], v153 offset:5120\n"
    "buffer_load_dwordx4 a[232:235], v23, s[52:55], s86 offen sc1\n"
    "s_add_u32 s86, s26, 0x20000\n"
    "s_waitcnt lgkmcnt(6)\n"
    "v_mfma_f32_32x32x16_bf16 v[24:39], v[124:127], a[104:107], v[24:39]\n"
    "ds_read_b64_tr_b16 v[144:145], v153 offset:6144\n"
    "ds_read_b64_tr_b16 v[146:147], v153 offset:7168\n"
    "v_mfma_f32_32x32x16_bf16 v[24:39], v[128:131], a[108:111], v[24:39]\n"
    "ds_read_b64_tr_b16 v[120:121], v153 offset:8192\n"
    "ds_read_b64_tr_b16 v[122:123], v153 offset:9216\n"
    "buffer_load_dwordx4 a[236:239], v20, s[52:55], s86 offen sc1\n"
    "s_waitcnt lgkmcnt(6)\n"
    "v_mfma_f32_32x32x16_bf16 v[24:39], v[132:135], v[192:195], v[24:39]\n"
    "ds_read_b64_tr_b16 v[124:125], v153 offset:10240\n"
    "ds_read_b64_tr_b16 v[126:127], v153 offset:11264\n"
    "v_mfma_f32_32x32x16_bf16 v[24:39], v[136:139], v[210:213], v[24:39]\n"
    "ds_read_b64_tr_b16 v[128:129], v153 offset:12288\n"
    "ds_read_b64_tr_b16 v[130:131], v153 offset:13312\n"
    "buffer_load_dwordx4 a[240:243], v21, s[52:55], s86 offen sc1\n"
    "s_waitcnt lgkmcnt(6)\n"
    "v_mfma_f32_32x32x16_bf16 v[24:39], v[140:143], v[214:217], v[24:39]\n"
    "ds_read_b64_tr_b16 v[132:133], v153 offset:14336\n"
    "ds_read_b64_tr_b16 v[134:135], v153 offset:15360\n"
    "v_mfma_f32_32x32x16_bf16 v[24:39], v[144:147], v[218:221], v[24:39]\n"
    "ds_read_b64_tr_b16 v[136:137], v153 offset:16384\n"
    "ds_read_b64_tr_b16 v[138:139], v153 offset:17408\n"
    "s_waitcnt lgkmcnt(6)\n"
    "v_mfma_f32_32x32x16_bf16 v[24:39], v[120:123], v[222:225], v[24:39]\n"
    "ds_read_b64_tr_b16 v[140:141], v153 offset:18432\n"
    "ds_read_b64_tr_b16 v[142:143], v153 offset:19456\n"
    "buffer_load_dwordx4 a[244:247], v22, s[52:55], s86 offen sc1\n"
    "v_mfma_f32_32x32x16_bf16 v[24:39], v[124:127], v[226:229], v[24:39]\n"
    "ds_read_b64_tr_b16 v[144:145], v153 offset:20480\n"
    "ds_read_b64_tr_b16 v[146:147], v153 offset:21504\n"
    "s_waitcnt lgkmcnt(6)\n"
    "v_mfma_f32_32x32x16_bf16 v[24:39], v[128:131], v[230:233], v[24:39]\n"
    "ds_read_b64_tr_b16 v[120:121], v153 offset:22528\n"
    "ds_read_b64_tr_b16 v[122:123], v153 offset:23552\n"
    "buffer_load_dwordx4 a[248:251], v23, s[52:55], s86 offen sc1\n"
    "v_mfma_f32_32x32x16_bf16 v[24:39], v[132:135], v[234:237], v[24:39]\n"
    "ds_read_b64_tr_b16 v[124:125], v153 offset:24576\n"
    "ds_read_b64_tr_b16 v[126:127], v153 offset:25600\n"
    "s_waitcnt vmcnt(11)\n"
    "v_accvgpr_read_b32 v148, a207\n"
    "s_waitcnt vmcnt(10)\n"
    "v_accvgpr_read_b32 v149, a211\n"
    "s_waitcnt lgkmcnt(6)\n"
    "v_mfma_f32_32x32x16_bf16 v[24:39], v[136:139], v[196:199], v[24:39]\n"
    "ds_read_b64_tr_b16 v[128:129], v153 offset:26624\n"
    "ds_read_b64_tr_b16 v[130:131], v153 offset:27648\n"
    "s_waitcnt vmcnt(9)\n"
    "v_accvgpr_read_b32 v150, a215\n"
    "s_waitcnt vmcnt(8)\n"
    "v_accvgpr_read_b32 v151, a219\n"
    "v_mfma_f32_32x32x16_bf16 v[24:39], v[140:143], a[128:131], v[24:39]\n"
    "ds_read_b64_tr_b16 v[132:133], v153 offset:28672\n"
    "ds_read_b64_tr_b16 v[134:135], v153 offset:29696\n"
    "v_cmp_eq_u32 vcc, s66, v148\n"
    "v_cmp_eq_u32 s[78:79], s66, v149\n"
    "v_cmp_eq_u32 s[80:81], s66, v150\n"
    "v_cmp_eq_u32 s[82:83], s66, v151\n"
    "s_or_b64 s[80:81], s[80:81], s[82:83]\n"
    "s_or_b64 s[78:79], s[78:79], s[80:81]\n"
    "s_or_b64 vcc, vcc, s[78:79]\n"
    "s_cbranch_vccnz Lslow109_%=\n"
    "Lback110_%=:\n"
    "s_waitcnt lgkmcnt(6)\n"
    "v_mfma_f32_32x32x16_bf16 v[24:39], v[144:147], a[132:135], v[24:39]\n"
    "ds_read_b64_tr_b16 v[136:137], v153 offset:30720\n"
    "ds_read_b64_tr_b16 v[138:139], v153 offset:31744\n"
    "buffer_load_dwordx4 v[104:107], v2, s[52:55], s17 offen sc1\n"
    "buffer_load_dwordx4 v[108:111], v2, s[52:55], s18 offen sc1\n"
    "v_mfma_f32_32x32x16_bf16 v[24:39], v[120:123], a[136:139], v[24:39]\n"
    "ds_read_b64_tr_b16 v[140:141], v156\n"
    "ds_read_b64_tr_b16 v[142:143], v156 offset:512\n"
    "buffer_load_dwordx4 v[112:115], v2, s[52:55], s27 offen sc1\n"
    "s_waitcnt lgkmcnt(6)\n"
    "v_mfma_f32_32x32x16_bf16 v[24:39], v[124:127], a[140:143], v[24:39]\n"
    "ds_read_b64_tr_b16 v[144:145], v156 offset:1024\n"
    "ds_read_b64_tr_b16 v[146:147], v156 offset:1536\n"
    "v_mfma_f32_32x32x16_bf16 v[24:39], v[128:131], a[144:147], v[24:39]\n"
    "ds_read_b64_tr_b16 v[120:121], v156 offset:2048\n"
    "ds_read_b64_tr_b16 v[122:123], v156 offset:2560\n"
    "s_waitcnt lgkmcnt(6)\n"
    "v_mfma_f32_32x32x16_bf16 v[24:39], v[132:135], a[148:151], v[24:39]\n"
    "ds_read_b64_tr_b16 v[124:125], v156 offset:3072\n"
    "ds_read_b64_tr_b16 v[126:127], v156 offset:3584\n"
    "v_mfma_f32_32x32x16_bf16 v[24:39], v[136:139], a[152:155], v[24:39]\n"
    "ds_read_b64_tr_b16 v[128:129], v156 offset:4096\n"
    "ds_read_b64_tr_b16 v[130:131], v156 offset:4608\n"
    "s_waitcnt lgkmcnt(6)\n"
    "v_mfma_f32_32x32x16_bf16 v[24:39], v[140:143], v[200:203], v[24:39]\n"
    "ds_read_b64_tr_b16 v[132:133], v156 offset:5120\n"
    "ds_read_b64_tr_b16 v[134:135], v156 offset:5632\n"
    "v_mfma_f32_32x32x16_bf16 v[24:39], v[144:147], a[192:195], v[24:39]\n"
    "ds_read_b64_tr_b16 v[136:137], v156 offset:6144\n"
    "ds_read_b64_tr_b16 v[138:139], v156 offset:6656\n"
    "s_waitcnt vmcnt(10)\n"
    "v_accvgpr_read_b32 v148, a223\n"
    "s_waitcnt vmcnt(9)\n"
    "v_accvgpr_read_b32 v149, a227\n"
    "s_waitcnt lgkmcnt(6)\n"
    "v_mfma_f32_32x32x16_bf16 v[24:39], v[120:123], a[196:199], v[24:39]\n"
    "ds_read_b64_tr_b16 v[140:141], v156 offset:7168\n"
    "ds_read_b64_tr_b16 v[142:143], v156 offset:7680\n"
    "s_waitcnt vmcnt(8)\n"
    "v_accvgpr_read_b32 v150, a231\n"
    "s_waitcnt vmcnt(7)\n"
    "v_accvgpr_read_b32 v151, a235\n"
    "v_mfma_f32_32x32x16_bf16 v[24:39], v[124:127], a[200:203], v[24:39]\n"
    "ds_read_b64_tr_b16 v[144:145], v156 offset:8192\n"
    "ds_read_b64_tr_b16 v[146:147], v156 offset:8704\n"
    "v_cmp_eq_u32 vcc, s66, v148\n"
    "v_cmp_eq_u32 s[78:79], s66, v149\n"
    "v_cmp_eq_u32 s[80:81], s66, v150\n"
    "v_cmp_eq_u32 s[82:83], s66, v151\n"
    "s_or_b64 s[80:81], s[80:81], s[82:83]\n"
    "s_or_b64 s[78:79], s[78:79], s[80:81]\n"
    "s_or_b64 vcc, vcc, s[78:79]\n"
    "s_cbranch_vccnz Lslow112_%=\n"
    "Lback113_%=:\n"
    "s_waitcnt lgkmcnt(6)\n"
    "v_mfma_f32_32x32x16_bf16 v[24:39], v[128:131], a[204:207], v[24:39]\n"
    "ds_read_b64_tr_b16 v[120:121], v156 offset:9216\n"
    "ds_read_b64_tr_b16 v[122:123], v156 offset:9728\n"
    "v_mfma_f32_32x32x16_bf16 v[24:39], v[132:135], a[208:211], v[24:39]\n"
    "ds_read_b64_tr_b16 v[124:125], v156 offset:10240\n"
    "ds_read_b64_tr_b16 v[126:127], v156 offset:10752\n"
    "s_waitcnt lgkmcnt(6)\n"
    "v_mfma_f32_32x32x16_bf16 v[24:39], v[136:139], a[212:215], v[24:39]\n"
    "ds_read_b64_tr_b16 v[128:129], v156 offset:11264\n"
    "ds_read_b64_tr_b16 v[130:131], v156 offset:11776\n"
    "v_mfma_f32_32x32x16_bf16 v[24:39], v[140:143], a[216:219], v[24:39]\n"
    "ds_read_b64_tr_b16 v[132:133], v156 offset:12288\n"
    "ds_read_b64_tr_b16 v[134:135], v156 offset:12800\n"
    "s_waitcnt lgkmcnt(6)\n"
    "v_mfma_f32_32x32x16_bf16 v[24:39], v[144:147], a[220:223], v[24:39]\n"
    "ds_read_b64_tr_b16 v[136:137], v156 offset:13312\n"
    "ds_read_b64_tr_b16 v[138:139], v156 offset:13824\n"
    "v_mfma_f32_32x32x16_bf16 v[24:39], v[120:123], a[224:227], v[24:39]\n"
    "ds_read_b64_tr_b16 v[140:141], v156 offset:14336\n"
    "ds_read_b64_tr_b16 v[142:143], v156 offset:14848\n"
    "s_waitcnt lgkmcnt(6)\n"
    "v_mfma_f32_32x32x16_bf16 v[24:39], v[124:127], a[228:231], v[24:39]\n"
    "ds_read_b64_tr_b16 v[144:145], v156 offset:15360\n"
    "ds_read_b64_tr_b16 v[146:147], v156 offset:15872\n"
    "s_waitcnt vmcnt(6)\n"
    "v_accvgpr_read_b32 v148, a239\n"
    "s_waitcnt vmcnt(5)\n"
    "v_accvgpr_read_b32 v149, a243\n"
    "v_mfma_f32_32x32x16_bf16 v[24:39], v[128:131], a[232:235], v[24:39]\n"
    "s_waitcnt vmcnt(4)\n"
    "v_accvgpr_read_b32 v150, a247\n"
    "s_waitcnt vmcnt(3)\n"
    "v_accvgpr_read_b32 v151, a251\n"
    "v_cmp_eq_u32 vcc, s66, v148\n"
    "v_cmp_eq_u32 s[78:79], s66, v149\n"
    "v_cmp_eq_u32 s[80:81], s66, v150\n"
    "v_cmp_eq_u32 s[82:83], s66, v151\n"
    "s_or_b64 s[80:81], s[80:81], s[82:83]\n"
    "s_or_b64 s[78:79], s[78:79], s[80:81]\n"
    "s_or_b64 vcc, vcc, s[78:79]\n"
    "s_cbranch_vccnz Lslow115_%=\n"
    "Lback116_%=:\n"
    "s_waitcnt lgkmcnt(4)\n"
    "v_mfma_f32_32x32x16_bf16 v[24:39], v[132:135], a[236:239], v[24:39]\n"
    "v_mfma_f32_32x32x16_bf16 v[24:39], v[136:139], a[240:243], v[24:39]\n"
    "s_waitcnt lgkmcnt(0)\n"
    "v_mfma_f32_32x32x16_bf16 v[24:39], v[140:143], a[244:247], v[24:39]\n"
    "v_mfma_f32_32x32x16_bf16 v[24:39], v[144:147], a[248:251], v[24:39]\n"
    "s_waitcnt vmcnt(2)\n"
    "v_xor_b32 v8, s66, v107\n"
    "s_waitcnt vmcnt(1)\n"
    "v_bitop3_b32 v8, v111, s66, v8 bitop3:0xbe\n"
    "s_waitcnt vmcnt(0)\n"
    "v_bitop3_b32 v8, v115, s66, v8 bitop3:0xbe\n"
    "v_cmp_ne_u32 vcc, 0, v8\n"
    "s_cbranch_vccnz Lslow118_%=\n"
    "Lback119_%=:\n"
    "v_lshlrev_b32 v72, 16, v104\n"
    "v_and_b32 v73, 0xffff0000, v104\n"
    "v_lshlrev_b32 v74, 16, v105\n"
    "v_and_b32 v75, 0xffff0000, v105\n"
    "v_lshlrev_b32 v76, 16, v106\n"
    "v_and_b32 v77, 0xffff0000, v106\n"
    "v_lshlrev_b32 v78, 16, v108\n"
    "v_and_b32 v79, 0xffff0000, v108\n"
    "v_lshlrev_b32 v80, 16, v109\n"
    "v_and_b32 v81, 0xffff0000, v109\n"
    "v_lshlrev_b32 v82, 16, v110\n"
    "v_and_b32 v83, 0xffff0000, v110\n"
    "v_lshlrev_b32 v84, 16, v112\n"
    "v_and_b32 v85, 0xffff0000, v112\n"
    "v_lshlrev_b32 v86, 16, v113\n"
    "v_and_b32 v87, 0xffff0000, v113\n"
    "v_pk_add_f32 v[24:25], v[24:25], v[72:73]\n"
    "v_pk_add_f32 v[26:27], v[26:27], v[74:75]\n"
    "v_pk_add_f32 v[28:29], v[28:29], v[76:77]\n"
    "v_pk_add_f32 v[30:31], v[30:31], v[78:79]\n"
    "v_pk_add_f32 v[32:33], v[32:33], v[80:81]\n"
    "v_pk_add_f32 v[34:35], v[34:35], v[82:83]\n"
    "v_pk_add_f32 v[36:37], v[36:37], v[84:85]\n"
    "v_pk_add_f32 v[38:39], v[38:39], v[86:87]\n"
    "v_cvt_pk_bf16_f32 v104, v24, v25\n"
    "v_cvt_pk_bf16_f32 v105, v26, v27\n"
    "v_cvt_pk_bf16_f32 v106, v28, v29\n"
    "v_cvt_pk_bf16_f32 v107, v30, v31\n"
    "v_cvt_pk_bf16_f32 v108, v32, v33\n"
    "v_cvt_pk_bf16_f32 v109, v34, v35\n"
    "v_cvt_pk_bf16_f32 v110, v36, v37\n"
    "v_cvt_pk_bf16_f32 v111, v38, v39\n"
    "v_permlane32_swap_b32 v104, v106\n"
    "v_permlane32_swap_b32 v105, v107\n"
    "v_permlane32_swap_b32 v108, v110\n"
    "v_permlane32_swap_b32 v109, v111\n"
    "s_nop 0\n"
    "v_permlane16_swap_b32 v104, v108\n"
    "v_permlane16_swap_b32 v105, v109\n"
    "v_permlane16_swap_b32 v106, v110\n"
    "v_permlane16_swap_b32 v107, v111\n"
    "ds_bpermute_b32 v72, v12, v104\n"
    "ds_bpermute_b32 v73, v12, v105\n"
    "ds_bpermute_b32 v74, v12, v106\n"
    "ds_bpermute_b32 v75, v12, v107\n"
    "ds_bpermute_b32 v76, v12, v108\n"
    "ds_bpermute_b32 v77, v12, v109\n"
    "ds_bpermute_b32 v78, v12, v110\n"
    "ds_bpermute_b32 v79, v12, v111\n"
    "s_add_u32 s84, s91, 0x4000\n"
    "s_waitcnt lgkmcnt(4)\n"
    "buffer_store_dwordx4 v[72:75], v11, s[56:59], s91 offen nt\n"
    "s_waitcnt lgkmcnt(0)\n"
    "buffer_store_dwordx4 v[76:79], v11, s[56:59], s84 offen nt\n"
    "s_branch Lfin_%=\n"
    "Lslow3_%=:\n"
    "s_nop 0\n"
    "s_nop 3\n"
    "s_mov_b32 s76, 0x30d40\n"
    "Lpoll5_%=:\n"
    "s_nop 0\n"
    "buffer_load_dwordx4 v[180:183], v220, s[52:55], s87 offen sc1\n"
    "buffer_load_dwordx2 v[184:185], v221, s[52:55], s88 offen sc1\n"
    "buffer_load_dwordx2 v[188:189], v221, s[52:55], s88 offen offset:256 sc1\n"
    "buffer_load_dwordx2 v[192:193], v221, s[52:55], s88 offen offset:512 sc1\n"
    "buffer_load_dwordx2 v[196:197], v221, s[52:55], s88 offen offset:768 sc1\n"
    "s_waitcnt vmcnt(0)\n"
    "v_xor_b32 v24, s66, v181\n"
    "v_bitop3_b32 v24, v183, s66, v24 bitop3:0xbe\n"
    "v_bitop3_b32 v24, v185, s66, v24 bitop3:0xbe\n"
    "v_bitop3_b32 v24, v189, s66, v24 bitop3:0xbe\n"
    "v_bitop3_b32 v24, v193, s66, v24 bitop3:0xbe\n"
    "v_bitop3_b32 v24, v197, s66, v24 bitop3:0xbe\n"
    "v_cmp_ne_u32 vcc, 0, v24\n"
    "s_cbranch_vccz Lback4_%=\n"
    "s_sleep 1\n"
    "s_sub_u32 s76, s76, 1\n"
    "s_cbranch_scc0 Lpoll5_%=\n"
    "s_branch Lback4_%=\n"
    "Lslow7_%=:\n"
    "s_nop 0\n"
    "s_nop 3\n"
    "s_mov_b32 s76, 0x30d40\n"
    "Lpoll9_%=:\n"
    "s_nop 0\n"
    "buffer_load_dword v169, v168, s[52:55], s77 offen sc1\n"
    "s_waitcnt vmcnt(0)\n"
    "v_cmp_ne_u32 vcc, s66, v169\n"
    "s_cbranch_vccz Lback8_%=\n"
    "s_sleep 1\n"
    "s_sub_u32 s76, s76, 1\n"
    "s_cbranch_scc0 Lpoll9_%=\n"
    "s_branch Lback8_%=\n"
    "Lslow10_%=:\n"
    "s_nop 0\n"
    "s_nop 3\n"
    "s_mov_b32 s76, 0x30d40\n"
    "Lpoll12_%=:\n"
    "s_nop 0\n"
    "buffer_load_dword v160, v161, s[52:55], s97 offen sc1\n"
    "s_waitcnt vmcnt(0)\n"
    "v_cmp_ne_u32 vcc, s66, v160\n"
    "s_cbranch_vccz Lback11_%=\n"
    "s_sleep 4\n"
    "s_sub_u32 s76, s76, 1\n"
    "s_cbranch_scc0 Lpoll12_%=\n"
    "s_branch Lback11_%=\n"
    "Lslow13_%=:\n"
    "s_nop 0\n"
    "s_nop 3\n"
    "s_mov_b32 s76, 0x30d40\n"
    "Lpoll15_%=:\n"
    "s_nop 0\n"
    "buffer_load_dwordx4 v[64:67], v2, s[52:55], s16 offen sc1\n"
    "buffer_load_dwordx4 v[68:71], v174, s[52:55], s16 offen sc1\n"
    "buffer_load_dwordx4 v[72:75], v175, s[52:55], s16 offen sc1\n"
    "s_waitcnt vmcnt(0)\n"
    "v_xor_b32 v168, s66, v67\n"
    "v_bitop3_b32 v168, v71, s66, v168 bitop3:0xbe\n"
    "v_bitop3_b32 v168, v75, s66, v168 bitop3:0xbe\n"
    "v_cmp_ne_u32 vcc, 0, v168\n"
    "s_cbranch_vccz Lback14_%=\n"
    "s_sleep 1\n"
    "s_sub_u32 s76, s76, 1\n"
    "s_cbranch_scc0 Lpoll15_%=\n"
    "s_branch Lback14_%=\n"
    "Lslow16_%=:\n"
    "s_nop 0\n"
    "s_nop 3\n"
    "s_mov_b32 s76, 0x30d40\n"
    "Lpoll18_%=:\n"
    "s_nop 0\n"
    "buffer_load_dwordx4 v[80:83], v2, s[52:55], s17 offen sc1\n"
    "buffer_load_dwordx4 v[84:87], v174, s[52:55], s17 offen sc1\n"
    "buffer_load_dwordx4 v[88:91], v175, s[52:55], s17 offen sc1\n"
    "s_waitcnt vmcnt(0)\n"
    "v_xor_b32 v168, s66, v83\n"
    "v_bitop3_b32 v168, v87, s66, v168 bitop3:0xbe\n"
    "v_bitop3_b32 v168, v91, s66, v168 bitop3:0xbe\n"
    "v_cmp_ne_u32 vcc, 0, v168\n"
    "s_cbranch_vccz Lback17_%=\n"
    "s_sleep 1\n"
    "s_sub_u32 s76, s76, 1\n"
    "s_cbranch_scc0 Lpoll18_%=\n"
    "s_branch Lback17_%=\n"
    "Lslow19_%=:\n"
    "s_nop 0\n"
    "s_nop 3\n"
    "s_mov_b32 s76, 0x30d40\n"
    "Lpoll21_%=:\n"
    "s_nop 0\n"
    "buffer_load_dwordx4 v[96:99], v2, s[52:55], s18 offen sc1\n"
    "buffer_load_dwordx4 v[100:103], v174, s[52:55], s18 offen sc1\n"
    "buffer_load_dwordx4 v[104:107], v175, s[52:55], s18 offen sc1\n"
    "s_waitcnt vmcnt(0)\n"
    "v_xor_b32 v168, s66, v99\n"
    "v_bitop3_b32 v168, v103, s66, v168 bitop3:0xbe\n"
    "v_bitop3_b32 v168, v107, s66, v168 bitop3:0xbe\n"
    "v_cmp_ne_u32 vcc, 0, v168\n"
    "s_cbranch_vccz Lback20_%=\n"
    "s_sleep 1\n"
    "s_sub_u32 s76, s76, 1\n"
    "s_cbranch_scc0 Lpoll21_%=\n"
    "s_branch Lback20_%=\n"
    "Lslow22_%=:\n"
    "s_nop 0\n"
    "s_nop 3\n"
    "s_mov_b32 s76, 0x30d40\n"
    "Lpoll24_%=:\n"
    "s_nop 0\n"
    "buffer_load_dword v171, v170, s[52:55], s82 offen sc1\n"
    "s_waitcnt vmcnt(0)\n"
    "v_cmp_ne_u32 vcc, s66, v171\n"
    "s_cbranch_vccz Lback23_%=\n"
    "s_sleep 1\n"
    "s_sub_u32 s76, s76, 1\n"
    "s_cbranch_scc0 Lpoll24_%=\n"
    "s_branch Lback23_%=\n"
    "Lslow28_%=:\n"
    "s_nop 0\n"
    "s_nop 3\n"
    "s_mov_b32 s76, 0x30d40\n"
    "Lpoll30_%=:\n"
    "s_nop 0\n"
    "buffer_load_dword v160, v161, s[52:55], s97 offen sc1\n"
    "s_waitcnt vmcnt(0)\n"
    "v_cmp_ne_u32 vcc, s66, v160\n"
    "s_cbranch_vccz Lback29_%=\n"
    "s_sleep 4\n"
    "s_sub_u32 s76, s76, 1\n"
    "s_cbranch_scc0 Lpoll30_%=\n"
    "s_branch Lback29_%=\n"
    "Lslow31_%=:\n"
    "s_nop 0\n"
    "s_nop 3\n"
    "s_mov_b32 s76, 0x30d40\n"
    "Lpoll33_%=:\n"
    "s_nop 0\n"
    "buffer_load_dwordx4 v[64:67], v2, s[52:55], s16 offen sc1\n"
    "buffer_load_dwordx4 v[68:71], v174, s[52:55], s16 offen sc1\n"
    "buffer_load_dwordx4 v[72:75], v175, s[52:55], s16 offen sc1\n"
    "s_waitcnt vmcnt(0)\n"
    "v_xor_b32 v168, s66, v67\n"
    "v_bitop3_b32 v168, v71, s66, v168 bitop3:0xbe\n"
    "v_bitop3_b32 v168, v75, s66, v168 bitop3:0xbe\n"
    "v_cmp_ne_u32 vcc, 0, v168\n"
    "s_cbranch_vccz Lback32_%=\n"
    "s_sleep 1\n"
    "s_sub_u32 s76, s76, 1\n"
    "s_cbranch_scc0 Lpoll33_%=\n"
    "s_branch Lback32_%=\n"
    "Lslow34_%=:\n"
    "s_nop 0\n"
    "s_nop 3\n"
    "s_mov_b32 s76, 0x30d40\n"
    "Lpoll36_%=:\n"
    "s_nop 0\n"
    "buffer_load_dwordx4 v[80:83], v2, s[52:55], s17 offen sc1\n"
    "buffer_load_dwordx4 v[84:87], v174, s[52:55], s17 offen sc1\n"
    "buffer_load_dwordx4 v[88:91], v175, s[52:55], s17 offen sc1\n"
    "s_waitcnt vmcnt(0)\n"
    "v_xor_b32 v168, s66, v83\n"
    "v_bitop3_b32 v168, v87, s66, v168 bitop3:0xbe\n"
    "v_bitop3_b32 v168, v91, s66, v168 bitop3:0xbe\n"
    "v_cmp_ne_u32 vcc, 0, v168\n"
    "s_cbranch_vccz Lback35_%=\n"
    "s_sleep 1\n"
    "s_sub_u32 s76, s76, 1\n"
    "s_cbranch_scc0 Lpoll36_%=\n"
    "s_branch Lback35_%=\n"
    "Lslow37_%=:\n"
    "s_nop 0\n"
    "s_nop 3\n"
    "s_mov_b32 s76, 0x30d40\n"
    "Lpoll39_%=:\n"
    "s_nop 0\n"
    "buffer_load_dwordx4 v[96:99], v2, s[52:55], s18 offen sc1\n"
    "buffer_load_dwordx4 v[100:103], v174, s[52:55], s18 offen sc1\n"
    "buffer_load_dwordx4 v[104:107], v175, s[52:55], s18 offen sc1\n"
    "s_waitcnt vmcnt(0)\n"
    "v_xor_b32 v168, s66, v99\n"
    "v_bitop3_b32 v168, v103, s66, v168 bitop3:0xbe\n"
    "v_bitop3_b32 v168, v107, s66, v168 bitop3:0xbe\n"
    "v_cmp_ne_u32 vcc, 0, v168\n"
    "s_cbranch_vccz Lback38_%=\n"
    "s_sleep 1\n"
    "s_sub_u32 s76, s76, 1\n"
    "s_cbranch_scc0 Lpoll39_%=\n"
    "s_branch Lback38_%=\n"
    "Lslow40_%=:\n"
    "s_nop 0\n"
    "s_nop 3\n"
    "s_mov_b32 s76, 0x30d40\n"
    "Lpoll42_%=:\n"
    "s_nop 0\n"
    "buffer_load_dword v171, v170, s[52:55], s82 offen sc1\n"
    "s_waitcnt vmcnt(0)\n"
    "v_cmp_ne_u32 vcc, s66, v171\n"
    "s_cbranch_vccz Lback41_%=\n"
    "s_sleep 1\n"
    "s_sub_u32 s76, s76, 1\n"
    "s_cbranch_scc0 Lpoll42_%=\n"
    "s_branch Lback41_%=\n"
    "Lslow43_%=:\n"
    "s_nop 0\n"
    "s_nop 3\n"
    "s_mov_b32 s76, 0x30d40\n"
    "Lpoll45_%=:\n"
    "s_nop 0\n"
    "buffer_load_dwordx4 a[128:131], v2, s[52:55], s96 offen sc1\n"
    "buffer_load_dwordx4 a[136:139], v2, s[52:55], s96 offen offset:1024 sc1\n"
    "buffer_load_dwordx4 a[144:147], v2, s[52:55], s96 offen offset:2048 sc1\n"
    "buffer_load_dwordx4 a[152:155], v2, s[52:55], s96 offen offset:3072 sc1\n"
    "s_waitcnt vmcnt(11)\n"
    "buffer_load_dwordx4 a[132:135], v15, s[52:55], s96 offen sc1\n"
    "s_waitcnt vmcnt(11)\n"
    "buffer_load_dwordx4 a[140:143], v15, s[52:55], s96 offen offset:1024 sc1\n"
    "s_waitcnt vmcnt(11)\n"
    "buffer_load_dwordx4 a[148:151], v15, s[52:55], s96 offen offset:2048 sc1\n"
    "s_waitcnt vmcnt(11)\n"
    "buffer_load_dwordx4 a[156:159], v15, s[52:55], s96 offen offset:3072 sc1\n"
    "s_waitcnt vmcnt(0)\n"
    "v_accvgpr_read_b32 v148, a131\n"
    "v_accvgpr_read_b32 v149, a139\n"
    "v_accvgpr_read_b32 v150, a147\n"
    "v_accvgpr_read_b32 v151, a155\n"
    "v_cmp_eq_u32 vcc, s66, v148\n"
    "v_cmp_eq_u32 s[78:79], s66, v149\n"
    "v_cmp_eq_u32 s[80:81], s66, v150\n"
    "v_cmp_eq_u32 s[82:83], s66, v151\n"
    "s_or_b64 s[80:81], s[80:81], s[82:83]\n"
    "s_or_b64 s[78:79], s[78:79], s[80:81]\n"
    "s_or_b64 vcc, vcc, s[78:79]\n"
    "s_cbranch_vccz Lback44_%=\n"
    "s_sleep 2\n"
    "s_sub_u32 s76, s76, 1\n"
    "s_cbranch_scc0 Lpoll45_%=\n"
    "s_branch Lback44_%=\n"
    "Lslow46_%=:\n"
    "s_nop 0\n"
    "s_nop 3\n"
    "s_mov_b32 s76, 0x30d40\n"
    "Lpoll48_%=:\n"
    "s_nop 0\n"
    "buffer_load_dwordx4 a[132:135], v15, s[52:55], s96 offen sc1\n"
    "buffer_load_dwordx4 a[140:143], v15, s[52:55], s96 offen offset:1024 sc1\n"
    "buffer_load_dwordx4 a[148:151], v15, s[52:55], s96 offen offset:2048 sc1\n"
    "buffer_load_dwordx4 a[156:159], v15, s[52:55], s96 offen offset:3072 sc1\n"
    "s_waitcnt vmcnt(8)\n"
    "buffer_load_dwordx4 a[192:195], v2, s[52:55], s10 offen sc1\n"
    "s_waitcnt vmcnt(8)\n"
    "buffer_load_dwordx4 a[200:203], v2, s[52:55], s10 offen offset:1024 sc1\n"
    "s_waitcnt vmcnt(8)\n"
    "buffer_load_dwordx4 a[208:211], v2, s[52:55], s10 offen offset:2048 sc1\n"
    "s_waitcnt vmcnt(8)\n"
    "buffer_load_dwordx4 a[216:219], v2, s[52:55], s10 offen offset:3072 sc1\n"
    "s_waitcnt vmcnt(8)\n"
    "buffer_load_dwordx4 a[196:199], v15, s[52:55], s10 offen sc1\n"
    "s_waitcnt vmcnt(0)\n"
    "v_accvgpr_read_b32 v148, a135\n"
    "v_accvgpr_read_b32 v149, a143\n"
    "v_accvgpr_read_b32 v150, a151\n"
    "v_accvgpr_read_b32 v151, a159\n"
    "v_cmp_eq_u32 vcc, s66, v148\n"
    "v_cmp_eq_u32 s[78:79], s66, v149\n"
    "v_cmp_eq_u32 s[80:81], s66, v150\n"
    "v_cmp_eq_u32 s[82:83], s66, v151\n"
    "s_or_b64 s[80:81], s[80:81], s[82:83]\n"
    "s_or_b64 s[78:79], s[78:79], s[80:81]\n"
    "s_or_b64 vcc, vcc, s[78:79]\n"
    "s_cbranch_vccz Lback47_%=\n"
    "s_sleep 2\n"
    "s_sub_u32 s76, s76, 1\n"
    "s_cbranch_scc0 Lpoll48_%=\n"
    "s_branch Lback47_%=\n"
    "Lslow49_%=:\n"
    "s_nop 0\n"
    "s_nop 3\n"
    "s_mov_b32 s76, 0x30d40\n"
    "Lpoll51_%=:\n"
    "s_nop 0\n"
    "buffer_load_dwordx4 a[192:195], v2, s[52:55], s10 offen sc1\n"
    "buffer_load_dwordx4 a[200:203], v2, s[52:55], s10 offen offset:1024 sc1\n"
    "buffer_load_dwordx4 a[208:211], v2, s[52:55], s10 offen offset:2048 sc1\n"
    "buffer_load_dwordx4 a[216:219], v2, s[52:55], s10 offen offset:3072 sc1\n"
    "s_waitcnt vmcnt(10)\n"
    "buffer_load_dwordx4 a[196:199], v15, s[52:55], s10 offen sc1\n"
    "s_waitcnt vmcnt(10)\n"
    "buffer_load_dwordx4 a[204:207], v15, s[52:55], s10 offen offset:1024 sc1\n"
    "s_waitcnt vmcnt(10)\n"
    "buffer_load_dwordx4 a[212:215], v15, s[52:55], s10 offen offset:2048 sc1\n"
    "s_waitcnt vmcnt(10)\n"
    "buffer_load_dwordx4 a[220:223], v15, s[52:55], s10 offen offset:3072 sc1\n"
    "s_waitcnt vmcnt(10)\n"
    "buffer_load_dwordx4 a[160:163], v2, s[52:55], s8 offen sc1\n"
    "s_waitcnt vmcnt(10)\n"
    "buffer_load_dwordx4 a[168:171], v2, s[52:55], s8 offen offset:1024 sc1\n"
    "s_waitcnt vmcnt(10)\n"
    "buffer_load_dwordx4 a[176:179], v2, s[52:55], s8 offen offset:2048 sc1\n"
    "s_waitcnt vmcnt(0)\n"
    "v_accvgpr_read_b32 v148, a195\n"
    "v_accvgpr_read_b32 v149, a203\n"
    "v_accvgpr_read_b32 v150, a211\n"
    "v_accvgpr_read_b32 v151, a219\n"
    "v_cmp_eq_u32 vcc, s66, v148\n"
    "v_cmp_eq_u32 s[78:79], s66, v149\n"
    "v_cmp_eq_u32 s[80:81], s66, v150\n"
    "v_cmp_eq_u32 s[82:83], s66, v151\n"
    "s_or_b64 s[80:81], s[80:81], s[82:83]\n"
    "s_or_b64 s[78:79], s[78:79], s[80:81]\n"
    "s_or_b64 vcc, vcc, s[78:79]\n"
    "s_cbranch_vccz Lback50_%=\n"
    "s_sleep 2\n"
    "s_sub_u32 s76, s76, 1\n"
    "s_cbranch_scc0 Lpoll51_%=\n"
    "s_branch Lback50_%=\n"
    "Lslow52_%=:\n"
    "s_nop 0\n"
    "s_nop 3\n"
    "s_mov_b32 s76, 0x30d40\n"
    "Lpoll54_%=:\n"
    "s_nop 0\n"
    "buffer_load_dwordx4 a[196:199], v15, s[52:55], s10 offen sc1\n"
    "buffer_load_dwordx4 a[204:207], v15, s[52:55], s10 offen offset:1024 sc1\n"
    "buffer_load_dwordx4 a[212:215], v15, s[52:55], s10 offen offset:2048 sc1\n"
    "buffer_load_dwordx4 a[220:223], v15, s[52:55], s10 offen offset:3072 sc1\n"
    "s_waitcnt vmcnt(12)\n"
    "buffer_load_dwordx4 a[160:163], v2, s[52:55], s8 offen sc1\n"
    "s_waitcnt vmcnt(12)\n"
    "buffer_load_dwordx4 a[168:171], v2, s[52:55], s8 offen offset:1024 sc1\n"
    "s_waitcnt vmcnt(12)\n"
    "buffer_load_dwordx4 a[176:179], v2, s[52:55], s8 offen offset:2048 sc1\n"
    "s_waitcnt vmcnt(12)\n"
    "buffer_load_dwordx4 a[184:187], v2, s[52:55], s8 offen offset:3072 sc1\n"
    "s_waitcnt vmcnt(12)\n"
    "buffer_load_dwordx4 a[164:167], v15, s[52:55], s8 offen sc1\n"
    "s_waitcnt vmcnt(12)\n"
    "buffer_load_dwordx4 a[172:175], v15, s[52:55], s8 offen offset:1024 sc1\n"
    "s_waitcnt vmcnt(12)\n"
    "buffer_load_dwordx4 a[180:183], v15, s[52:55], s8 offen offset:2048 sc1\n"
    "s_waitcnt vmcnt(12)\n"
    "buffer_load_dwordx4 a[188:191], v15, s[52:55], s8 offen offset:3072 sc1\n"
    "s_waitcnt vmcnt(0)\n"
    "v_accvgpr_read_b32 v148, a199\n"
    "v_accvgpr_read_b32 v149, a207\n"
    "v_accvgpr_read_b32 v150, a215\n"
    "v_accvgpr_read_b32 v151, a223\n"
    "v_cmp_eq_u32 vcc, s66, v148\n"
    "v_cmp_eq_u32 s[78:79], s66, v149\n"
    "v_cmp_eq_u32 s[80:81], s66, v150\n"
    "v_cmp_eq_u32 s[82:83], s66, v151\n"
    "s_or_b64 s[80:81], s[80:81], s[82:83]\n"
    "s_or_b64 s[78:79], s[78:79], s[80:81]\n"
    "s_or_b64 vcc, vcc, s[78:79]\n"
    "s_cbranch_vccz Lback53_%=\n"
    "s_sleep 2\n"
    "s_sub_u32 s76, s76, 1\n"
    "s_cbranch_scc0 Lpoll54_%=\n"
    "s_branch Lback53_%=\n"
    "Lslow55_%=:\n"
    "s_nop 0\n"
    "s_nop 3\n"
    "s_mov_b32 s76, 0x30d40\n"
    "Lpoll57_%=:\n"
    "s_nop 0\n"
    "buffer_load_dwordx4 a[160:163], v2, s[52:55], s8 offen sc1\n"
    "buffer_load_dwordx4 a[168:171], v2, s[52:55], s8 offen offset:1024 sc1\n"
    "buffer_load_dwordx4 a[176:179], v2, s[52:55], s8 offen offset:2048 sc1\n"
    "buffer_load_dwordx4 a[184:187], v2, s[52:55], s8 offen offset:3072 sc1\n"
    "s_waitcnt vmcnt(14)\n"
    "buffer_load_dwordx4 a[164:167], v15, s[52:55], s8 offen sc1\n"
    "s_waitcnt vmcnt(14)\n"
    "buffer_load_dwordx4 a[172:175], v15, s[52:55], s8 offen offset:1024 sc1\n"
    "s_waitcnt vmcnt(14)\n"
    "buffer_load_dwordx4 a[180:183], v15, s[52:55], s8 offen offset:2048 sc1\n"
    "s_waitcnt vmcnt(14)\n"
    "buffer_load_dwordx4 a[188:191], v15, s[52:55], s8 offen offset:3072 sc1\n"
    "s_waitcnt vmcnt(12)\n"
    "buffer_load_dwordx4 a[224:227], v2, s[52:55], s75 offen sc1\n"
    "s_waitcnt vmcnt(12)\n"
    "buffer_load_dwordx4 a[232:235], v2, s[52:55], s75 offen offset:1024 sc1\n"
    "s_waitcnt vmcnt(12)\n"
    "buffer_load_dwordx4 a[240:243], v2, s[52:55], s75 offen offset:2048 sc1\n"
    "s_waitcnt vmcnt(12)\n"
    "buffer_load_dwordx4 a[248:251], v2, s[52:55], s75 offen offset:3072 sc1\n"
    "s_waitcnt vmcnt(0)\n"
    "v_accvgpr_read_b32 v148, a163\n"
    "v_accvgpr_read_b32 v149, a171\n"
    "v_accvgpr_read_b32 v150, a179\n"
    "v_accvgpr_read_b32 v151, a187\n"
    "v_cmp_eq_u32 vcc, s66, v148\n"
    "v_cmp_eq_u32 s[78:79], s66, v149\n"
    "v_cmp_eq_u32 s[80:81], s66, v150\n"
    "v_cmp_eq_u32 s[82:83], s66, v151\n"
    "s_or_b64 s[80:81], s[80:81], s[82:83]\n"
    "s_or_b64 s[78:79], s[78:79], s[80:81]\n"
    "s_or_b64 vcc, vcc, s[78:79]\n"
    "s_cbranch_vccz Lback56_%=\n"
    "s_sleep 2\n"
    "s_sub_u32 s76, s76, 1\n"
    "s_cbranch_scc0 Lpoll57_%=\n"
    "s_branch Lback56_%=\n"
    "Lslow58_%=:\n"
    "s_nop 0\n"
    "s_nop 3\n"
    "s_mov_b32 s76, 0x30d40\n"
    "Lpoll60_%=:\n"
    "s_nop 0\n"
    "buffer_load_dwordx4 a[164:167], v15, s[52:55], s8 offen sc1\n"
    "buffer_load_dwordx4 a[172:175], v15, s[52:55], s8 offen offset:1024 sc1\n"
    "buffer_load_dwordx4 a[180:183], v15, s[52:55], s8 offen offset:2048 sc1\n"
    "buffer_load_dwordx4 a[188:191], v15, s[52:55], s8 offen offset:3072 sc1\n"
    "s_waitcnt vmcnt(14)\n"
    "buffer_load_dwordx4 a[224:227], v2, s[52:55], s75 offen sc1\n"
    "s_waitcnt vmcnt(14)\n"
    "buffer_load_dwordx4 a[232:235], v2, s[52:55], s75 offen offset:1024 sc1\n"
    "s_waitcnt vmcnt(14)\n"
    "buffer_load_dwordx4 a[240:243], v2, s[52:55], s75 offen offset:2048 sc1\n"
    "s_waitcnt vmcnt(14)\n"
    "buffer_load_dwordx4 a[248:251], v2, s[52:55], s75 offen offset:3072 sc1\n"
    "s_waitcnt vmcnt(12)\n"
    "buffer_load_dwordx4 a[228:231], v15, s[52:55], s75 offen sc1\n"
    "s_waitcnt vmcnt(12)\n"
    "buffer_load_dwordx4 a[236:239], v15, s[52:55], s75 offen offset:1024 sc1\n"
    "s_waitcnt vmcnt(12)\n"
    "buffer_load_dwordx4 a[244:247], v15, s[52:55], s75 offen offset:2048 sc1\n"
    "s_waitcnt vmcnt(12)\n"
    "buffer_load_dwordx4 a[252:255], v15, s[52:55], s75 offen offset:3072 sc1\n"
    "s_waitcnt vmcnt(0)\n"
    "v_accvgpr_read_b32 v148, a167\n"
    "v_accvgpr_read_b32 v149, a175\n"
    "v_accvgpr_read_b32 v150, a183\n"
    "v_accvgpr_read_b32 v151, a191\n"
    "v_cmp_eq_u32 vcc, s66, v148\n"
    "v_cmp_eq_u32 s[78:79], s66, v149\n"
    "v_cmp_eq_u32 s[80:81], s66, v150\n"
    "v_cmp_eq_u32 s[82:83], s66, v151\n"
    "s_or_b64 s[80:81], s[80:81], s[82:83]\n"
    "s_or_b64 s[78:79], s[78:79], s[80:81]\n"
    "s_or_b64 vcc, vcc, s[78:79]\n"
    "s_cbranch_vccz Lback59_%=\n"
    "s_sleep 2\n"
    "s_sub_u32 s76, s76, 1\n"
    "s_cbranch_scc0 Lpoll60_%=\n"
    "s_branch Lback59_%=\n"
    "Lslow61_%=:\n"
    "s_nop 0\n"
    "s_nop 3\n"
    "s_mov_b32 s76, 0x30d40\n"
    "Lpoll63_%=:\n"
    "s_nop 0\n"
    "buffer_load_dwordx4 a[224:227], v2, s[52:55], s75 offen sc1\n"
    "buffer_load_dwordx4 a[232:235], v2, s[52:55], s75 offen offset:1024 sc1\n"
    "buffer_load_dwordx4 a[240:243], v2, s[52:55], s75 offen offset:2048 sc1\n"
    "buffer_load_dwordx4 a[248:251], v2, s[52:55], s75 offen offset:3072 sc1\n"
    "s_waitcnt vmcnt(14)\n"
    "buffer_load_dwordx4 a[228:231], v15, s[52:55], s75 offen sc1\n"
    "s_waitcnt vmcnt(14)\n"
    "buffer_load_dwordx4 a[236:239], v15, s[52:55], s75 offen offset:1024 sc1\n"
    "s_waitcnt vmcnt(14)\n"
    "buffer_load_dwordx4 a[244:247], v15, s[52:55], s75 offen offset:2048 sc1\n"
    "s_waitcnt vmcnt(14)\n"
    "buffer_load_dwordx4 a[252:255], v15, s[52:55], s75 offen offset:3072 sc1\n"
    "s_waitcnt vmcnt(0)\n"
    "v_accvgpr_read_b32 v148, a227\n"
    "v_accvgpr_read_b32 v149, a235\n"
    "v_accvgpr_read_b32 v150, a243\n"
    "v_accvgpr_read_b32 v151, a251\n"
    "v_cmp_eq_u32 vcc, s66, v148\n"
    "v_cmp_eq_u32 s[78:79], s66, v149\n"
    "v_cmp_eq_u32 s[80:81], s66, v150\n"
    "v_cmp_eq_u32 s[82:83], s66, v151\n"
    "s_or_b64 s[80:81], s[80:81], s[82:83]\n"
    "s_or_b64 s[78:79], s[78:79], s[80:81]\n"
    "s_or_b64 vcc, vcc, s[78:79]\n"
    "s_cbranch_vccz Lback62_%=\n"
    "s_sleep 2\n"
    "s_sub_u32 s76, s76, 1\n"
    "s_cbranch_scc0 Lpoll63_%=\n"
    "s_branch Lback62_%=\n"
    "Lslow64_%=:\n"
    "s_nop 0\n"
    "s_nop 3\n"
    "s_mov_b32 s76, 0x30d40\n"
    "Lpoll66_%=:\n"
    "s_nop 0\n"
    "buffer_load_dwordx4 a[228:231], v15, s[52:55], s75 offen sc1\n"
    "buffer_load_dwordx4 a[236:239], v15, s[52:55], s75 offen offset:1024 sc1\n"
    "buffer_load_dwordx4 a[244:247], v15, s[52:55], s75 offen offset:2048 sc1\n"
    "buffer_load_dwordx4 a[252:255], v15, s[52:55], s75 offen offset:3072 sc1\n"
    "s_waitcnt vmcnt(0)\n"
    "v_accvgpr_read_b32 v148, a231\n"
    "v_accvgpr_read_b32 v149, a239\n"
    "v_accvgpr_read_b32 v150, a247\n"
    "v_accvgpr_read_b32 v151, a255\n"
    "v_cmp_eq_u32 vcc, s66, v148\n"
    "v_cmp_eq_u32 s[78:79], s66, v149\n"
    "v_cmp_eq_u32 s[80:81], s66, v150\n"
    "v_cmp_eq_u32 s[82:83], s66, v151\n"
    "s_or_b64 s[80:81], s[80:81], s[82:83]\n"
    "s_or_b64 s[78:79], s[78:79], s[80:81]\n"
    "s_or_b64 vcc, vcc, s[78:79]\n"
    "s_cbranch_vccz Lback65_%=\n"
    "s_sleep 2\n"
    "s_sub_u32 s76, s76, 1\n"
    "s_cbranch_scc0 Lpoll66_%=\n"
    "s_branch Lback65_%=\n"
    "Lslow67_%=:\n"
    "s_nop 0\n"
    "s_nop 3\n"
    "s_mov_b32 s76, 0x30d40\n"
    "Lpoll69_%=:\n"
    "s_nop 0\n"
    "buffer_load_dwordx4 v[104:107], v2, s[52:55], s14 offen sc1\n"
    "buffer_load_dwordx4 v[108:111], v2, s[52:55], s14 offen offset:1024 sc1\n"
    "buffer_load_dwordx4 v[112:115], v2, s[52:55], s14 offen offset:2048 sc1\n"
    "buffer_load_dwordx4 v[116:119], v2, s[52:55], s14 offen offset:3072 sc1\n"
    "buffer_load_dwordx4 v[210:213], v2, s[52:55], s13 offen sc1\n"
    "buffer_load_dwordx4 v[214:217], v2, s[52:55], s13 offen offset:1024 sc1\n"
    "buffer_load_dwordx4 v[218:221], v2, s[52:55], s13 offen offset:2048 sc1\n"
    "buffer_load_dwordx4 v[222:225], v2, s[52:55], s13 offen offset:3072 sc1\n"
    "s_waitcnt vmcnt(0)\n"
    "v_xor_b32 v9, s66, v105\n"
    "v_bitop3_b32 v9, v107, s66, v9 bitop3:0xbe\n"
    "v_bitop3_b32 v9, v109, s66, v9 bitop3:0xbe\n"
    "v_bitop3_b32 v9, v111, s66, v9 bitop3:0xbe\n"
    "v_bitop3_b32 v9, v113, s66, v9 bitop3:0xbe\n"
    "v_bitop3_b32 v9, v115, s66, v9 bitop3:0xbe\n"
    "v_bitop3_b32 v9, v117, s66, v9 bitop3:0xbe\n"
    "v_bitop3_b32 v9, v119, s66, v9 bitop3:0xbe\n"
    "v_bitop3_b32 v9, v211, s66, v9 bitop3:0xbe\n"
    "v_bitop3_b32 v9, v213, s66, v9 bitop3:0xbe\n"
    "v_bitop3_b32 v9, v215, s66, v9 bitop3:0xbe\n"
    "v_bitop3_b32 v9, v217, s66, v9 bitop3:0xbe\n"
    "v_bitop3_b32 v9, v219, s66, v9 bitop3:0xbe\n"
    "v_bitop3_b32 v9, v221, s66, v9 bitop3:0xbe\n"
    "v_bitop3_b32 v9, v223, s66, v9 bitop3:0xbe\n"
    "v_bitop3_b32 v9, v225, s66, v9 bitop3:0xbe\n"
    "v_cmp_ne_u32 vcc, 0, v9\n"
    "s_cbranch_vccz Lback68_%=\n"
    "s_sleep 1\n"
    "s_sub_u32 s76, s76, 1\n"
    "s_cbranch_scc0 Lpoll69_%=\n"
    "s_branch Lback68_%=\n"
    "Lslow70_%=:\n"
    "s_nop 0\n"
    "s_nop 3\n"
    "s_mov_b32 s76, 0x30d40\n"
    "Lpoll72_%=:\n"
    "s_nop 0\n"
    "s_add_u32 s87, s21, 0x8000\n"
    "buffer_load_dwordx4 a[0:3], v21, s[52:55], s87 offen sc1\n"
    "buffer_load_dwordx4 a[4:7], v22, s[52:55], s87 offen sc1\n"
    "buffer_load_dwordx4 a[8:11], v23, s[52:55], s87 offen sc1\n"
    "s_waitcnt vmcnt(0)\n"
    "v_accvgpr_read_b32 v148, a3\n"
    "v_accvgpr_read_b32 v149, a7\n"
    "v_accvgpr_read_b32 v150, a11\n"
    "v_cmp_eq_u32 vcc, s66, v148\n"
    "v_cmp_eq_u32 s[78:79], s66, v149\n"
    "v_cmp_eq_u32 s[80:81], s66, v150\n"
    "s_or_b64 s[78:79], s[78:79], s[80:81]\n"
    "s_or_b64 vcc, vcc, s[78:79]\n"
    "s_cbranch_vccz Lback71_%=\n"
    "s_sleep 1\n"
    "s_sub_u32 s76, s76, 1\n"
    "s_cbranch_scc0 Lpoll72_%=\n"
    "s_branch Lback71_%=\n"
    "Lslow73_%=:\n"
    "s_nop 0\n"
    "s_nop 3\n"
    "s_mov_b32 s76, 0x30d40\n"
    "Lpoll75_%=:\n"
    "s_nop 0\n"
    "s_add_u32 s87, s22, 0x8000\n"
    "buffer_load_dwordx4 a[12:15], v20, s[52:55], s87 offen sc1\n"
    "buffer_load_dwordx4 a[16:19], v21, s[52:55], s87 offen sc1\n"
    "buffer_load_dwordx4 a[20:23], v22, s[52:55], s87 offen sc1\n"
    "buffer_load_dwordx4 a[24:27], v23, s[52:55], s87 offen sc1\n"
    "s_waitcnt vmcnt(0)\n"
    "v_accvgpr_read_b32 v148, a15\n"
    "v_accvgpr_read_b32 v149, a19\n"
    "v_accvgpr_read_b32 v150, a23\n"
    "v_accvgpr_read_b32 v151, a27\n"
    "v_cmp_eq_u32 vcc, s66, v148\n"
    "v_cmp_eq_u32 s[78:79], s66, v149\n"
    "v_cmp_eq_u32 s[80:81], s66, v150\n"
    "v_cmp_eq_u32 s[82:83], s66, v151\n"
    "s_or_b64 s[80:81], s[80:81], s[82:83]\n"
    "s_or_b64 s[78:79], s[78:79], s[80:81]\n"
    "s_or_b64 vcc, vcc, s[78:79]\n"
    "s_cbranch_vccz Lback74_%=\n"
    "s_sleep 1\n"
    "s_sub_u32 s76, s76, 1\n"
    "s_cbranch_scc0 Lpoll75_%=\n"
    "s_branch Lback74_%=\n"
    "Lslow76_%=:\n"
    "s_nop 0\n"
    "s_nop 3\n"
    "s_mov_b32 s76, 0x30d40\n"
    "Lpoll78_%=:\n"
    "s_nop 0\n"
    "s_add_u32 s87, s21, 0xc000\n"
    "buffer_load_dwordx4 a[28:31], v21, s[52:55], s87 offen sc1\n"
    "buffer_load_dwordx4 a[32:35], v22, s[52:55], s87 offen sc1\n"
    "buffer_load_dwordx4 a[36:39], v23, s[52:55], s87 offen sc1\n"
    "s_waitcnt vmcnt(0)\n"
    "v_accvgpr_read_b32 v148, a31\n"
    "v_accvgpr_read_b32 v149, a35\n"
    "v_accvgpr_read_b32 v150, a39\n"
    "v_cmp_eq_u32 vcc, s66, v148\n"
    "v_cmp_eq_u32 s[78:79], s66, v149\n"
    "v_cmp_eq_u32 s[80:81], s66, v150\n"
    "s_or_b64 s[78:79], s[78:79], s[80:81]\n"
    "s_or_b64 vcc, vcc, s[78:79]\n"
    "s_cbranch_vccz Lback77_%=\n"
    "s_sleep 1\n"
    "s_sub_u32 s76, s76, 1\n"
    "s_cbranch_scc0 Lpoll78_%=\n"
    "s_branch Lback77_%=\n"
    "Lslow79_%=:\n"
    "s_nop 0\n"
    "s_nop 3\n"
    "s_mov_b32 s76, 0x30d40\n"
    "Lpoll81_%=:\n"
    "s_nop 0\n"
    "s_add_u32 s87, s22, 0xc000\n"
    "buffer_load_dwordx4 a[40:43], v20, s[52:55], s87 offen sc1\n"
    "buffer_load_dwordx4 a[44:47], v21, s[52:55], s87 offen sc1\n"
    "buffer_load_dwordx4 a[48:51], v22, s[52:55], s87 offen sc1\n"
    "buffer_load_dwordx4 a[52:55], v23, s[52:55], s87 offen sc1\n"
    "s_waitcnt vmcnt(0)\n"
    "v_accvgpr_read_b32 v148, a43\n"
    "v_accvgpr_read_b32 v149, a47\n"
    "v_accvgpr_read_b32 v150, a51\n"
    "v_accvgpr_read_b32 v151, a55\n"
    "v_cmp_eq_u32 vcc, s66, v148\n"
    "v_cmp_eq_u32 s[78:79], s66, v149\n"
    "v_cmp_eq_u32 s[80:81], s66, v150\n"
    "v_cmp_eq_u32 s[82:83], s66, v151\n"
    "s_or_b64 s[80:81], s[80:81], s[82:83]\n"
    "s_or_b64 s[78:79], s[78:79], s[80:81]\n"
    "s_or_b64 vcc, vcc, s[78:79]\n"
    "s_cbranch_vccz Lback80_%=\n"
    "s_sleep 1\n"
    "s_sub_u32 s76, s76, 1\n"
    "s_cbranch_scc0 Lpoll81_%=\n"
    "s_branch Lback80_%=\n"
    "Lslow82_%=:\n"
    "s_nop 0\n"
    "s_nop 3\n"
    "s_mov_b32 s76, 0x30d40\n"
    "Lpoll84_%=:\n"
    "s_nop 0\n"
    "s_add_u32 s87, s21, 0x10000\n"
    "buffer_load_dwordx4 a[56:59], v21, s[52:55], s87 offen sc1\n"
    "buffer_load_dwordx4 a[60:63], v22, s[52:55], s87 offen sc1\n"
    "buffer_load_dwordx4 a[64:67], v23, s[52:55], s87 offen sc1\n"
    "s_waitcnt vmcnt(0)\n"
    "v_accvgpr_read_b32 v148, a59\n"
    "v_accvgpr_read_b32 v149, a63\n"
    "v_accvgpr_read_b32 v150, a67\n"
    "v_cmp_eq_u32 vcc, s66, v148\n"
    "v_cmp_eq_u32 s[78:79], s66, v149\n"
    "v_cmp_eq_u32 s[80:81], s66, v150\n"
    "s_or_b64 s[78:79], s[78:79], s[80:81]\n"
    "s_or_b64 vcc, vcc, s[78:79]\n"
    "s_cbranch_vccz Lback83_%=\n"
    "s_sleep 1\n"
    "s_sub_u32 s76, s76, 1\n"
    "s_cbranch_scc0 Lpoll84_%=\n"
    "s_branch Lback83_%=\n"
    "Lslow85_%=:\n"
    "s_nop 0\n"
    "s_nop 3\n"
    "s_mov_b32 s76, 0x30d40\n"
    "Lpoll87_%=:\n"
    "s_nop 0\n"
    "s_add_u32 s87, s22, 0x10000\n"
    "buffer_load_dwordx4 a[68:71], v20, s[52:55], s87 offen sc1\n"
    "buffer_load_dwordx4 a[72:75], v21, s[52:55], s87 offen sc1\n"
    "buffer_load_dwordx4 a[76:79], v22, s[52:55], s87 offen sc1\n"
    "buffer_load_dwordx4 a[80:83], v23, s[52:55], s87 offen sc1\n"
    "s_waitcnt vmcnt(0)\n"
    "v_accvgpr_read_b32 v148, a71\n"
    "v_accvgpr_read_b32 v149, a75\n"
    "v_accvgpr_read_b32 v150, a79\n"
    "v_accvgpr_read_b32 v151, a83\n"
    "v_cmp_eq_u32 vcc, s66, v148\n"
    "v_cmp_eq_u32 s[78:79], s66, v149\n"
    "v_cmp_eq_u32 s[80:81], s66, v150\n"
    "v_cmp_eq_u32 s[82:83], s66, v151\n"
    "s_or_b64 s[80:81], s[80:81], s[82:83]\n"
    "s_or_b64 s[78:79], s[78:79], s[80:81]\n"
    "s_or_b64 vcc, vcc, s[78:79]\n"
    "s_cbranch_vccz Lback86_%=\n"
    "s_sleep 1\n"
    "s_sub_u32 s76, s76, 1\n"
    "s_cbranch_scc0 Lpoll87_%=\n"
    "s_branch Lback86_%=\n"
    "Lslow88_%=:\n"
    "s_nop 0\n"
    "s_nop 3\n"
    "s_mov_b32 s76, 0x30d40\n"
    "Lpoll90_%=:\n"
    "s_nop 0\n"
    "s_add_u32 s87, s21, 0x14000\n"
    "buffer_load_dwordx4 a[84:87], v21, s[52:55], s87 offen sc1\n"
    "buffer_load_dwordx4 a[88:91], v22, s[52:55], s87 offen sc1\n"
    "buffer_load_dwordx4 a[92:95], v23, s[52:55], s87 offen sc1\n"
    "s_waitcnt vmcnt(0)\n"
    "v_accvgpr_read_b32 v148, a87\n"
    "v_accvgpr_read_b32 v149, a91\n"
    "v_accvgpr_read_b32 v150, a95\n"
    "v_cmp_eq_u32 vcc, s66, v148\n"
    "v_cmp_eq_u32 s[78:79], s66, v149\n"
    "v_cmp_eq_u32 s[80:81], s66, v150\n"
    "s_or_b64 s[78:79], s[78:79], s[80:81]\n"
    "s_or_b64 vcc, vcc, s[78:79]\n"
    "s_cbranch_vccz Lback89_%=\n"
    "s_sleep 1\n"
    "s_sub_u32 s76, s76, 1\n"
    "s_cbranch_scc0 Lpoll90_%=\n"
    "s_branch Lback89_%=\n"
    "Lslow91_%=:\n"
    "s_nop 0\n"
    "s_nop 3\n"
    "s_mov_b32 s76, 0x30d40\n"
    "Lpoll93_%=:\n"
    "s_nop 0\n"
    "s_add_u32 s87, s22, 0x14000\n"
    "buffer_load_dwordx4 a[96:99], v20, s[52:55], s87 offen sc1\n"
    "buffer_load_dwordx4 a[100:103], v21, s[52:55], s87 offen sc1\n"
    "buffer_load_dwordx4 a[104:107], v22, s[52:55], s87 offen sc1\n"
    "buffer_load_dwordx4 a[108:111], v23, s[52:55], s87 offen sc1\n"
    "s_waitcnt vmcnt(0)\n"
    "v_accvgpr_read_b32 v148, a99\n"
    "v_accvgpr_read_b32 v149, a103\n"
    "v_accvgpr_read_b32 v150, a107\n"
    "v_accvgpr_read_b32 v151, a111\n"
    "v_cmp_eq_u32 vcc, s66, v148\n"
    "v_cmp_eq_u32 s[78:79], s66, v149\n"
    "v_cmp_eq_u32 s[80:81], s66, v150\n"
    "v_cmp_eq_u32 s[82:83], s66, v151\n"
    "s_or_b64 s[80:81], s[80:81], s[82:83]\n"
    "s_or_b64 s[78:79], s[78:79], s[80:81]\n"
    "s_or_b64 vcc, vcc, s[78:79]\n"
    "s_cbranch_vccz Lback92_%=\n"
    "s_sleep 1\n"
    "s_sub_u32 s76, s76, 1\n"
    "s_cbranch_scc0 Lpoll93_%=\n"
    "s_branch Lback92_%=\n"
    "Lslow94_%=:\n"
    "s_nop 0\n"
    "s_nop 3\n"
    "s_mov_b32 s76, 0x30d40\n"
    "Lpoll96_%=:\n"
    "s_nop 0\n"
    "s_add_u32 s87, s21, 0x18000\n"
    "buffer_load_dwordx4 v[210:213], v21, s[52:55], s87 offen sc1\n"
    "buffer_load_dwordx4 v[214:217], v22, s[52:55], s87 offen sc1\n"
    "buffer_load_dwordx4 v[218:221], v23, s[52:55], s87 offen sc1\n"
    "s_waitcnt vmcnt(0)\n"
    "v_cmp_eq_u32 vcc, s66, v213\n"
    "v_cmp_eq_u32 s[78:79], s66, v217\n"
    "v_cmp_eq_u32 s[80:81], s66, v221\n"
    "s_or_b64 s[78:79], s[78:79], s[80:81]\n"
    "s_or_b64 vcc, vcc, s[78:79]\n"
    "s_cbranch_vccz Lback95_%=\n"
    "s_sleep 1\n"
    "s_sub_u32 s76, s76, 1\n"
    "s_cbranch_scc0 Lpoll96_%=\n"
    "s_branch Lback95_%=\n"
    "Lslow97_%=:\n"
    "s_nop 0\n"
    "s_nop 3\n"
    "s_mov_b32 s76, 0x30d40\n"
    "Lpoll99_%=:\n"
    "s_nop 0\n"
    "s_add_u32 s87, s22, 0x18000\n"
    "buffer_load_dwordx4 v[222:225], v20, s[52:55], s87 offen sc1\n"
    "buffer_load_dwordx4 v[226:229], v21, s[52:55], s87 offen sc1\n"
    "buffer_load_dwordx4 v[230:233], v22, s[52:55], s87 offen sc1\n"
    "buffer_load_dwordx4 v[234:237], v23, s[52:55], s87 offen sc1\n"
    "s_waitcnt vmcnt(0)\n"
    "v_cmp_eq_u32 vcc, s66, v225\n"
    "v_cmp_eq_u32 s[78:79], s66, v229\n"
    "v_cmp_eq_u32 s[80:81], s66, v233\n"
    "v_cmp_eq_u32 s[82:83], s66, v237\n"
    "s_or_b64 s[80:81], s[80:81], s[82:83]\n"
    "s_or_b64 s[78:79], s[78:79], s[80:81]\n"
    "s_or_b64 vcc, vcc, s[78:79]\n"
    "s_cbranch_vccz Lback98_%=\n"
    "s_sleep 1\n"
    "s_sub_u32 s76, s76, 1\n"
    "s_cbranch_scc0 Lpoll99_%=\n"
    "s_branch Lback98_%=\n"
    "Lslow100_%=:\n"
    "s_nop 0\n"
    "s_nop 3\n"
    "s_mov_b32 s76, 0x30d40\n"
    "Lpoll102_%=:\n"
    "s_nop 0\n"
    "s_add_u32 s87, s21, 0x1c000\n"
    "buffer_load_dwordx4 a[128:131], v21, s[52:55], s87 offen sc1\n"
    "buffer_load_dwordx4 a[132:135], v22, s[52:55], s87 offen sc1\n"
    "buffer_load_dwordx4 a[136:139], v23, s[52:55], s87 offen sc1\n"
    "s_waitcnt vmcnt(0)\n"
    "v_accvgpr_read_b32 v148, a131\n"
    "v_accvgpr_read_b32 v149, a135\n"
    "v_accvgpr_read_b32 v150, a139\n"
    "v_cmp_eq_u32 vcc, s66, v148\n"
    "v_cmp_eq_u32 s[78:79], s66, v149\n"
    "v_cmp_eq_u32 s[80:81], s66, v150\n"
    "s_or_b64 s[78:79], s[78:79], s[80:81]\n"
    "s_or_b64 vcc, vcc, s[78:79]\n"
    "s_cbranch_vccz Lback101_%=\n"
    "s_sleep 1\n"
    "s_sub_u32 s76, s76, 1\n"
    "s_cbranch_scc0 Lpoll102_%=\n"
    "s_branch Lback101_%=\n"
    "Lslow103_%=:\n"
    "s_nop 0\n"
    "s_nop 3\n"
    "s_mov_b32 s76, 0x30d40\n"
    "Lpoll105_%=:\n"
    "s_nop 0\n"
    "s_add_u32 s87, s22, 0x1c000\n"
    "buffer_load_dwordx4 a[140:143], v20, s[52:55], s87 offen sc1\n"
    "buffer_load_dwordx4 a[144:147], v21, s[52:55], s87 offen sc1\n"
    "buffer_load_dwordx4 a[148:151], v22, s[52:55], s87 offen sc1\n"
    "buffer_load_dwordx4 a[152:155], v23, s[52:55], s87 offen sc1\n"
    "s_waitcnt vmcnt(0)\n"
    "v_accvgpr_read_b32 v148, a143\n"
    "v_accvgpr_read_b32 v149, a147\n"
    "v_accvgpr_read_b32 v150, a151\n"
    "v_accvgpr_read_b32 v151, a155\n"
    "v_cmp_eq_u32 vcc, s66, v148\n"
    "v_cmp_eq_u32 s[78:79], s66, v149\n"
    "v_cmp_eq_u32 s[80:81], s66, v150\n"
    "v_cmp_eq_u32 s[82:83], s66, v151\n"
    "s_or_b64 s[80:81], s[80:81], s[82:83]\n"
    "s_or_b64 s[78:79], s[78:79], s[80:81]\n"
    "s_or_b64 vcc, vcc, s[78:79]\n"
    "s_cbranch_vccz Lback104_%=\n"
    "s_sleep 1\n"
    "s_sub_u32 s76, s76, 1\n"
    "s_cbranch_scc0 Lpoll105_%=\n"
    "s_branch Lback104_%=\n"
    "Lslow106_%=:\n"
    "s_nop 0\n"
    "s_nop 3\n"
    "s_mov_b32 s76, 0x30d40\n"
    "Lpoll108_%=:\n"
    "s_nop 0\n"
    "s_add_u32 s87, s21, 0x20000\n"
    "buffer_load_dwordx4 a[192:195], v21, s[52:55], s87 offen sc1\n"
    "buffer_load_dwordx4 a[196:199], v22, s[52:55], s87 offen sc1\n"
    "buffer_load_dwordx4 a[200:203], v23, s[52:55], s87 offen sc1\n"
    "s_waitcnt vmcnt(0)\n"
    "v_accvgpr_read_b32 v148, a195\n"
    "v_accvgpr_read_b32 v149, a199\n"
    "v_accvgpr_read_b32 v150, a203\n"
    "v_cmp_eq_u32 vcc, s66, v148\n"
    "v_cmp_eq_u32 s[78:79], s66, v149\n"
    "v_cmp_eq_u32 s[80:81], s66, v150\n"
    "s_or_b64 s[78:79], s[78:79], s[80:81]\n"
    "s_or_b64 vcc, vcc, s[78:79]\n"
    "s_cbranch_vccz Lback107_%=\n"
    "s_sleep 1\n"
    "s_sub_u32 s76, s76, 1\n"
    "s_cbranch_scc0 Lpoll108_%=\n"
    "s_branch Lback107_%=\n"
    "Lslow109_%=:\n"
    "s_nop 0\n"
    "s_nop 3\n"
    "s_mov_b32 s76, 0x30d40\n"
    "Lpoll111_%=:\n"
    "s_nop 0\n"
    "s_add_u32 s87, s22, 0x20000\n"
    "buffer_load_dwordx4 a[204:207], v20, s[52:55], s87 offen sc1\n"
    "buffer_load_dwordx4 a[208:211], v21, s[52:55], s87 offen sc1\n"
    "buffer_load_dwordx4 a[212:215], v22, s[52:55], s87 offen sc1\n"
    "buffer_load_dwordx4 a[216:219], v23, s[52:55], s87 offen sc1\n"
    "s_waitcnt vmcnt(0)\n"
    "v_accvgpr_read_b32 v148, a207\n"
    "v_accvgpr_read_b32 v149, a211\n"
    "v_accvgpr_read_b32 v150, a215\n"
    "v_accvgpr_read_b32 v151, a219\n"
    "v_cmp_eq_u32 vcc, s66, v148\n"
    "v_cmp_eq_u32 s[78:79], s66, v149\n"
    "v_cmp_eq_u32 s[80:81], s66, v150\n"
    "v_cmp_eq_u32 s[82:83], s66, v151\n"
    "s_or_b64 s[80:81], s[80:81], s[82:83]\n"
    "s_or_b64 s[78:79], s[78:79], s[80:81]\n"
    "s_or_b64 vcc, vcc, s[78:79]\n"
    "s_cbranch_vccz Lback110_%=\n"
    "s_sleep 1\n"
    "s_sub_u32 s76, s76, 1\n"
    "s_cbranch_scc0 Lpoll111_%=\n"
    "s_branch Lback110_%=\n"
    "Lslow112_%=:\n"
    "s_nop 0\n"
    "s_nop 3\n"
    "s_mov_b32 s76, 0x30d40\n"
    "Lpoll114_%=:\n"
    "s_nop 0\n"
    "s_add_u32 s87, s23, 0x20000\n"
    "buffer_load_dwordx4 a[220:223], v20, s[52:55], s87 offen sc1\n"
    "buffer_load_dwordx4 a[224:227], v21, s[52:55], s87 offen sc1\n"
    "buffer_load_dwordx4 a[228:231], v22, s[52:55], s87 offen sc1\n"
    "buffer_load_dwordx4 a[232:235], v23, s[52:55], s87 offen sc1\n"
    "s_waitcnt vmcnt(0)\n"
    "v_accvgpr_read_b32 v148, a223\n"
    "v_accvgpr_read_b32 v149, a227\n"
    "v_accvgpr_read_b32 v150, a231\n"
    "v_accvgpr_read_b32 v151, a235\n"
    "v_cmp_eq_u32 vcc, s66, v148\n"
    "v_cmp_eq_u32 s[78:79], s66, v149\n"
    "v_cmp_eq_u32 s[80:81], s66, v150\n"
    "v_cmp_eq_u32 s[82:83], s66, v151\n"
    "s_or_b64 s[80:81], s[80:81], s[82:83]\n"
    "s_or_b64 s[78:79], s[78:79], s[80:81]\n"
    "s_or_b64 vcc, vcc, s[78:79]\n"
    "s_cbranch_vccz Lback113_%=\n"
    "s_sleep 1\n"
    "s_sub_u32 s76, s76, 1\n"
    "s_cbranch_scc0 Lpoll114_%=\n"
    "s_branch Lback113_%=\n"
    "Lslow115_%=:\n"
    "s_nop 0\n"
    "s_nop 3\n"
    "s_mov_b32 s76, 0x30d40\n"
    "Lpoll117_%=:\n"
    "s_nop 0\n"
    "s_add_u32 s87, s26, 0x20000\n"
    "buffer_load_dwordx4 a[236:239], v20, s[52:55], s87 offen sc1\n"
    "buffer_load_dwordx4 a[240:243], v21, s[52:55], s87 offen sc1\n"
    "buffer_load_dwordx4 a[244:247], v22, s[52:55], s87 offen sc1\n"
    "buffer_load_dwordx4 a[248:251], v23, s[52:55], s87 offen sc1\n"
    "s_waitcnt vmcnt(0)\n"
    "v_accvgpr_read_b32 v148, a239\n"
    "v_accvgpr_read_b32 v149, a243\n"
    "v_accvgpr_read_b32 v150, a247\n"
    "v_accvgpr_read_b32 v151, a251\n"
    "v_cmp_eq_u32 vcc, s66, v148\n"
    "v_cmp_eq_u32 s[78:79], s66, v149\n"
    "v_cmp_eq_u32 s[80:81], s66, v150\n"
    "v_cmp_eq_u32 s[82:83], s66, v151\n"
    "s_or_b64 s[80:81], s[80:81], s[82:83]\n"
    "s_or_b64 s[78:79], s[78:79], s[80:81]\n"
    "s_or_b64 vcc, vcc, s[78:79]\n"
    "s_cbranch_vccz Lback116_%=\n"
    "s_sleep 1\n"
    "s_sub_u32 s76, s76, 1\n"
    "s_cbranch_scc0 Lpoll117_%=\n"
    "s_branch Lback116_%=\n"
    "Lslow118_%=:\n"
    "s_nop 0\n"
    "s_nop 3\n"
    "s_mov_b32 s76, 0x30d40\n"
    "Lpoll120_%=:\n"
    "s_nop 0\n"
    "buffer_load_dwordx4 v[104:107], v2, s[52:55], s17 offen sc1\n"
    "buffer_load_dwordx4 v[108:111], v2, s[52:55], s18 offen sc1\n"
    "buffer_load_dwordx4 v[112:115], v2, s[52:55], s27 offen sc1\n"
    "s_waitcnt vmcnt(0)\n"
    "v_xor_b32 v8, s66, v107\n"
    "v_bitop3_b32 v8, v111, s66, v8 bitop3:0xbe\n"
    "v_bitop3_b32 v8, v115, s66, v8 bitop3:0xbe\n"
    "v_cmp_ne_u32 vcc, 0, v8\n"
    "s_cbranch_vccz Lback119_%=\n"
    "s_sleep 1\n"
    "s_sub_u32 s76, s76, 1\n"
    "s_cbranch_scc0 Lpoll120_%=\n"
    "s_branch Lback119_%=\n"
    "Lfin_%=:\n"
    : "+v"(tid) : "s"(__builtin_amdgcn_kernarg_segment_ptr()), "s"(blockIdx.x)
    : "v1", "v2", "v3", "v4", "v5", "v6", "v7", "v8", "v9", "v10", "v11", "v12", "v13", "v14", "v15", "v16", "v17", "v18", "v19", "v20", "v21", "v22", "v23", "v24", "v25", "v26", "v27", "v28", "v29", "v30", "v31", "v32", "v33", "v34", "v35", "v36", "v37", "v38", "v39", "v40", "v41", "v42", "v43", "v44", "v45", "v46", "v47", "v48", "v49", "v50", "v51", "v52", "v53", "v54", "v55", "v56", "v57", "v58", "v59", "v60", "v61", "v62", "v63", "v64", "v65", "v66", "v67", "v68", "v69", "v70", "v71", "v72", "v73", "v74", "v75", "v76", "v77", "v78", "v79", "v80", "v81", "v82", "v83", "v84", "v85", "v86", "v87", "v88", "v89", "v90", "v91", "v92", "v93", "v94", "v95", "v96", "v97", "v98", "v99", "v100", "v101", "v102", "v103", "v104", "v105", "v106", "v107", "v108", "v109", "v110", "v111", "v112", "v113", "v114", "v115", "v116", "v117", "v118", "v119", "v120", "v121", "v122", "v123", "v124", "v125", "v126", "v127", "v128", "v129", "v130", "v131", "v132", "v133", "v134", "v135", "v136", "v137", "v138", "v139", "v140", "v141", "v142", "v143", "v144", "v145", "v146", "v147", "v148", "v149", "v150", "v151", "v152", "v153", "v154", "v155", "v156", "v157", "v158", "v159", "v160", "v161", "v162", "v163", "v164", "v165", "v166", "v167", "v168", "v169", "v170", "v171", "v172", "v173", "v174", "v175", "v176", "v177", "v178", "v179", "v180", "v181", "v182", "v183", "v184", "v185", "v186", "v187", "v188", "v189", "v190", "v191", "v192", "v193", "v194", "v195", "v196", "v197", "v198", "v199", "v200", "v201", "v202", "v203", "v204", "v205", "v206", "v207", "v208", "v209", "v210", "v211", "v212", "v213", "v214", "v215", "v216", "v217", "v218", "v219", "v220", "v221", "v222", "v223", "v224", "v225", "v226", "v227", "v228", "v229", "v230", "v231", "v232", "v233", "v234", "v235", "v236", "v237", "v238", "v239", "v240", "v241", "v242", "v243", "v244", "v245", "v246", "v247", "v248", "v249", "v250", "v251", "v252", "v253", "v254", "v255", "a0", "a1", "a2", "a3", "a4", "a5", "a6", "a7", "a8", "a9", "a10", "a11", "a12", "a13", "a14", "a15", "a16", "a17", "a18", "a19", "a20", "a21", "a22", "a23", "a24", "a25", "a26", "a27", "a28", "a29", "a30", "a31", "a32", "a33", "a34", "a35", "a36", "a37", "a38", "a39", "a40", "a41", "a42", "a43", "a44", "a45", "a46", "a47", "a48", "a49", "a50", "a51", "a52", "a53", "a54", "a55", "a56", "a57", "a58", "a59", "a60", "a61", "a62", "a63", "a64", "a65", "a66", "a67", "a68", "a69", "a70", "a71", "a72", "a73", "a74", "a75", "a76", "a77", "a78", "a79", "a80", "a81", "a82", "a83", "a84", "a85", "a86", "a87", "a88", "a89", "a90", "a91", "a92", "a93", "a94", "a95", "a96", "a97", "a98", "a99", "a100", "a101", "a102", "a103", "a104", "a105", "a106", "a107", "a108", "a109", "a110", "a111", "a112", "a113", "a114", "a115", "a116", "a117", "a118", "a119", "a120", "a121", "a122", "a123", "a124", "a125", "a126", "a127", "a128", "a129", "a130", "a131", "a132", "a133", "a134", "a135", "a136", "a137", "a138", "a139", "a140", "a141", "a142", "a143", "a144", "a145", "a146", "a147", "a148", "a149", "a150", "a151", "a152", "a153", "a154", "a155", "a156", "a157", "a158", "a159", "a160", "a161", "a162", "a163", "a164", "a165", "a166", "a167", "a168", "a169", "a170", "a171", "a172", "a173", "a174", "a175", "a176", "a177", "a178", "a179", "a180", "a181", "a182", "a183", "a184", "a185", "a186", "a187", "a188", "a189", "a190", "a191", "a192", "a193", "a194", "a195", "a196", "a197", "a198", "a199", "a200", "a201", "a202", "a203", "a204", "a205", "a206", "a207", "a208", "a209", "a210", "a211", "a212", "a213", "a214", "a215", "a216", "a217", "a218", "a219", "a220", "a221", "a222", "a223", "a224", "a225", "a226", "a227", "a228", "a229", "a230", "a231", "a232", "a233", "a234", "a235", "a236", "a237", "a238", "a239", "a240", "a241", "a242", "a243", "a244", "a245", "a246", "a247", "a248", "a249", "a250", "a251", "a252", "a253", "a254", "a255", "s8", "s9", "s10", "s11", "s12", "s13", "s14", "s15", "s16", "s17", "s18", "s19", "s20", "s21", "s22", "s23", "s24", "s25", "s26", "s27", "s28", "s29", "s30", "s31", "s33", "s34", "s35", "s36", "s37", "s38", "s39", "s40", "s41", "s42", "s43", "s44", "s45", "s46", "s47", "s48", "s49", "s50", "s51", "s52", "s53", "s54", "s55", "s56", "s57", "s58", "s59", "s60", "s61", "s62", "s63", "s64", "s65", "s66", "s67", "s68", "s69", "s70", "s71", "s72", "s73", "s74", "s75", "s76", "s77", "s78", "s79", "s80", "s81", "s82", "s83", "s84", "s85", "s86", "s87", "s88", "s89", "s90", "s91", "s92", "s93", "s94", "s95", "s96", "s97", "s98", "s99", "s100", "s101", "vcc", "scc", "memory");
}

extern "C" void kernel_launch(void* const* d_in, const int* in_sizes, int n_in, void* d_out, int out_size,
                              void* d_ws, size_t ws_size, hipStream_t stream) {
  (void)hipFuncSetAttribute((const void*)mega, hipFuncAttributeMaxDynamicSharedMemorySize, 163840);
  mega<<<256, 256, 163840, stream>>>(d_in[0], d_in[1], d_in[2], d_in[3], d_in[4], d_in[6], d_in[7], d_in[8], d_ws, d_out);
}
```
